```python
import torch

def _softmax(x, axis=-1):
    return torch.softmax(x, dim=axis)
def _gelu(x):
    return torch.nn.functional.gelu(x, approximate="tanh")
def _silu(x):
    return torch.nn.functional.silu(x)
def _sigmoid(x):
    return torch.sigmoid(x)
def _layernorm(x, axis=-1, eps=1e-5):
    m = x.mean(dim=axis, keepdim=True)
    v = x.var(dim=axis, keepdim=True, unbiased=False)
    return (x - m) / torch.sqrt(v + eps)
def _scatter_add(tgt, idx, src, axis=0):
    return tgt.clone().index_add_(axis, idx, src)
def _segment_sum(src, seg, n):
    out = torch.zeros((n,) + src.shape[1:], dtype=src.dtype)
    return out.index_add_(0, seg, src)
def _pad(x, pw):
    flat = tuple(v for pair in reversed(pw) for v in pair)
    return torch.nn.functional.pad(x, flat)

kernel_name = 'sequential_preLN__linear_attn_block__glu_gated_bilinear__prompt'

def setup_inputs():

    _w = (lambda a: (lambda t: t.to(torch.bfloat16) if t.is_floating_point() else t)(torch.as_tensor(a)))
    def _A_setup():
        B, S, D = 1, 2048, 512
        return {
            "x": _w((lambda *s: torch.randn(*s, dtype=torch.bfloat16) * 0.05)(B, S, D)),
            "wqkv": _w((lambda *s: torch.randn(*s, dtype=torch.bfloat16) * 0.05)(D, 3 * D)),
            "wo": _w((lambda *s: torch.randn(*s, dtype=torch.bfloat16) * 0.05)(D, D)),
        }
    _A_in = _A_setup()

    def _B_setup():
        B, S, D, F = 1, 2048, 512, 1376
        return {
            "x": _w((lambda *s: torch.randn(*s, dtype=torch.bfloat16) * 0.05)(B, S, D)),
            "w_gate": _w((lambda *s: torch.randn(*s, dtype=torch.bfloat16) * 0.2)(D, F) * (D ** -0.5)),
            "w_up": _w((lambda *s: torch.randn(*s, dtype=torch.bfloat16) * 0.2)(D, F) * (D ** -0.5)),
            "w_down": _w((lambda *s: torch.randn(*s, dtype=torch.bfloat16) * 0.2)(F, D) * (F ** -0.5)),
        }
    _B_in = _B_setup()

    return {
        "A_x": _A_in["x"],
        "A_wqkv": _A_in["wqkv"],
        "A_wo": _A_in["wo"],
        "B_x": _B_in["x"],
        "B_w_gate": _B_in["w_gate"],
        "B_w_up": _B_in["w_up"],
        "B_w_down": _B_in["w_down"],
    }

def reference(A_x, A_wqkv, A_wo, B_x, B_w_gate, B_w_up, B_w_down):
    def _A_ref(x, wqkv, wo):
        B, S, D = x.shape
        H = 32; Dh = D // H
        qkv = (lambda x, *d: x.permute(*d))((x @ wqkv).reshape(B, S, 3, H, Dh), 2, 0, 3, 1, 4)
        q, k, v = qkv[0], qkv[1], qkv[2]
        phi = lambda t: torch.where(t > 0, t + 1, torch.exp(t)) + 1e-6
        pq, pk = phi(q), phi(k)
        kv = torch.einsum("bhsd,bhse->bhde", pk, v)
        z = torch.einsum("bhsd->bhd", pk)
        num = torch.einsum("bhqd,bhde->bhqe", pq, kv)
        den = torch.einsum("bhqd,bhd->bhq", pq, z)[..., None] + 1e-6
        o = num / den
        return (lambda x, *d: x.permute(*d))(o, 0, 2, 1, 3).reshape(B, S, D) @ wo

    def _B_ref(x, w_gate, w_up, w_down):
        g = torch.einsum("bsd,df->bsf", x, w_gate)
        u = torch.einsum("bsd,df->bsf", x, w_up)
        h = g * u
        return torch.einsum("bsf,fd->bsd", h, w_down)

    x = A_x
    h = x + _A_ref(_layernorm(x, axis=-1), A_wqkv, A_wo)
    h2 = h + _B_ref(_layernorm(h, axis=-1), B_w_gate, B_w_up, B_w_down)
    return h2
```

```cpp
#include <hip/hip_runtime.h>
#include <hip/hip_bf16.h>
#include <stdint.h>

typedef unsigned short u16;
typedef __attribute__((ext_vector_type(8))) __bf16 bf16x8;
typedef __attribute__((ext_vector_type(2))) __bf16 bf16x2;
typedef __attribute__((ext_vector_type(4))) short s16x4;
typedef __attribute__((ext_vector_type(4))) float f32x4;
typedef __attribute__((ext_vector_type(2))) float f32x2;
typedef __attribute__((ext_vector_type(4))) unsigned u32x4;
typedef __attribute__((ext_vector_type(2))) unsigned u32x2;
typedef __attribute__((address_space(3))) s16x4* lds_s16x4_ptr;

#define DI __device__ __forceinline__

constexpr int S_ = 2048, D_ = 512, F_ = 1376, H_ = 32;
constexpr int KS4 = F_ / 32;

DI float bf_lo(unsigned u) { return __uint_as_float(u << 16); }
DI float bf_hi(unsigned u) { return __uint_as_float(u & 0xffff0000u); }
DI unsigned pack_bf(float lo, float hi) {
  f32x2 f = {lo, hi};
  bf16x2 r = __builtin_convertvector(f, bf16x2);
  return __builtin_bit_cast(unsigned, r);
}
DI float bfr(float f) { return bf_lo(pack_bf(f, 0.f) & 0xffffu); }
DI f32x4 mfma16(u32x4 a, u32x4 b, f32x4 c) {
  return __builtin_amdgcn_mfma_f32_16x16x32_bf16(__builtin_bit_cast(bf16x8, a),
                                                 __builtin_bit_cast(bf16x8, b), c, 0, 0, 0);
}
DI float dot2(unsigned a, unsigned b, float c) {
  return __builtin_amdgcn_fdot2_f32_bf16(__builtin_bit_cast(bf16x2, a), __builtin_bit_cast(bf16x2, b), c, false);
}
DI u32x4 tr_frag(const char* lds0, const char* lds1) {
  s16x4 lo = __builtin_amdgcn_ds_read_tr16_b64_v4i16((lds_s16x4_ptr)lds0);
  s16x4 hi = __builtin_amdgcn_ds_read_tr16_b64_v4i16((lds_s16x4_ptr)lds1);
  u32x2 l2 = __builtin_bit_cast(u32x2, lo), h2 = __builtin_bit_cast(u32x2, hi);
  u32x4 r = {l2[0], l2[1], h2[0], h2[1]};
  return r;
}
DI void glds16(const void* g, char* l) {
  __builtin_amdgcn_global_load_lds((const unsigned*)g, (unsigned*)l, 16, 0, 0);
}
#define WAIT_VM(n) asm volatile("s_waitcnt vmcnt(" #n ")" ::: "memory")
#define RAW_BAR() do { asm volatile("s_waitcnt lgkmcnt(0)" ::: "memory"); __builtin_amdgcn_s_barrier(); } while (0)

DI float phi_f(float t) {
  float a = bfr(t > 0.f ? t + 1.f : __expf(t));
  return bfr(a + 1e-6f);
}

template <int MB>
DI void ln_frags(u32x4 (&a)[MB][16]) {
#pragma unroll
  for (int mb = 0; mb < MB; ++mb) {
    float s = 0.f, q = 0.f;
#pragma unroll
    for (int ks = 0; ks < 16; ++ks) {
#pragma unroll
      for (int i = 0; i < 4; ++i) {
        s = dot2(a[mb][ks][i], 0x3f803f80u, s);
        q = dot2(a[mb][ks][i], a[mb][ks][i], q);
      }
    }
    s += __shfl_xor(s, 16); q += __shfl_xor(q, 16);
    s += __shfl_xor(s, 32); q += __shfl_xor(q, 32);
    const float mean = s * (1.f / 512.f);
    float var = q * (1.f / 512.f) - mean * mean;
    var = fmaxf(var, 0.f);
    const float mb16 = bfr(mean);
    const float sd = bfr(sqrtf(bfr(bfr(var) + 1e-5f)));
    const float inv = 1.f / sd;
    const float off = -mb16 * inv;
#pragma unroll
    for (int ks = 0; ks < 16; ++ks) {
#pragma unroll
      for (int i = 0; i < 4; ++i) {
        const unsigned u = a[mb][ks][i];
        a[mb][ks][i] = pack_bf(fmaf(bf_lo(u), inv, off), fmaf(bf_hi(u), inv, off));
      }
    }
  }
}

__global__ __launch_bounds__(512) void k1_qkv(const u16* __restrict__ x, const u16* __restrict__ wqkv,
                                              u16* __restrict__ pq, float* __restrict__ part) {
  __shared__ __attribute__((aligned(16))) char smem[98304 + 8 * 272 * 4];
  const int tid = threadIdx.x, lane = tid & 63;
  const int wave = __builtin_amdgcn_readfirstlane(tid >> 6);
  const int xcd = blockIdx.x & 7, idx = blockIdx.x >> 3;
  const int rt = (xcd >> 1) * 4 + (idx >> 3);
  const int cg = (xcd & 1) * 8 + (idx & 7);
  const int rw = wave & 3, hw = wave >> 2;
  const int r = lane & 15, g = lane >> 4;
  const int row0 = rt * 128 + rw * 32;

  u32x4 a[2][16];
#pragma unroll
  for (int mb = 0; mb < 2; ++mb)
#pragma unroll
    for (int ks = 0; ks < 16; ++ks)
      a[mb][ks] = *(const u32x4*)(x + (size_t)(row0 + mb * 16 + r) * D_ + ks * 32 + g * 8);

  {
    const int kkp = (lane & 15) >> 1, half = lane & 1;
#pragma unroll
    for (int i = 0; i < 12; ++i) {
      const int blk = 4 * i + g;
      const int kbl = blk / 6, nb = blk % 6;
      const int kk = kkp ^ ((kbl & 1) << 2);
      const int k = wave * 64 + kbl * 8 + kk;
      const int col = (nb >> 1) * 512 + cg * 32 + (nb & 1) * 16 + half * 8;
      glds16(wqkv + (size_t)k * 1536 + col, smem + (wave * 48 + 4 * i) * 256 + lane * 16);
    }
  }
  ln_frags<2>(a);
  WAIT_VM(0);
  __syncthreads();

  const int q4 = (lane & 15) >> 2, p = lane & 3;
  const char* lb0 = smem + g * 1536 + ((q4) ^ (4 * (g & 1))) * 32 + p * 8 + hw * 256;
  const char* lb1 = smem + g * 1536 + ((4 + q4) ^ (4 * (g & 1))) * 32 + p * 8 + hw * 256;
  f32x4 accq[2] = {}, acck[2] = {}, accv[2] = {};
#pragma unroll
  for (int ks = 0; ks < 16; ++ks) {
    const int base = ks * 6144;
    const u32x4 bq = tr_frag(lb0 + base, lb1 + base);
    const u32x4 bk = tr_frag(lb0 + base + 512, lb1 + base + 512);
    const u32x4 bv = tr_frag(lb0 + base + 1024, lb1 + base + 1024);
#pragma unroll
    for (int mb = 0; mb < 2; ++mb) {
      accq[mb] = mfma16(bq, a[mb][ks], accq[mb]);
      acck[mb] = mfma16(a[mb][ks], bk, acck[mb]);
      accv[mb] = mfma16(a[mb][ks], bv, accv[mb]);
    }
  }
  const int head = cg * 2 + hw;
#pragma unroll
  for (int mb = 0; mb < 2; ++mb) {
    u32x2 o;
    o[0] = pack_bf(phi_f(bfr(accq[mb][0])), phi_f(bfr(accq[mb][1])));
    o[1] = pack_bf(phi_f(bfr(accq[mb][2])), phi_f(bfr(accq[mb][3])));
    *(u32x2*)(pq + (size_t)(row0 + mb * 16 + r) * D_ + head * 16 + g * 4) = o;
  }
  u32x4 pk, vv;
  float zs = 0.f;
#pragma unroll
  for (int mb = 0; mb < 2; ++mb) {
    const float p0 = phi_f(bfr(acck[mb][0])), p1 = phi_f(bfr(acck[mb][1]));
    const float p2 = phi_f(bfr(acck[mb][2])), p3 = phi_f(bfr(acck[mb][3]));
    zs += (p0 + p1) + (p2 + p3);
    pk[2 * mb] = pack_bf(p0, p1);
    pk[2 * mb + 1] = pack_bf(p2, p3);
    vv[2 * mb] = pack_bf(accv[mb][0], accv[mb][1]);
    vv[2 * mb + 1] = pack_bf(accv[mb][2], accv[mb][3]);
  }
  f32x4 kv = {};
  kv = mfma16(pk, vv, kv);
  zs += __shfl_xor(zs, 16);
  zs += __shfl_xor(zs, 32);
  float* red = (float*)(smem + 98304) + wave * 272;
#pragma unroll
  for (int j = 0; j < 4; ++j) red[(4 * g + j) * 16 + r] = kv[j];
  if (lane < 16) red[256 + lane] = zs;
  __syncthreads();
  for (int i = tid; i < 544; i += 512) {
    const int hh = i >= 272 ? 1 : 0, e = i - hh * 272;
    const float* rr = (const float*)(smem + 98304) + hh * 4 * 272 + e;
    const float s = (rr[0] + rr[272]) + (rr[544] + rr[816]);
    part[(size_t)(rt * 32 + cg * 2 + hh) * 272 + e] = s;
  }
}

__global__ __launch_bounds__(256) void k15_fold(const float* __restrict__ part, const u16* __restrict__ wo,
                                                u16* __restrict__ wp, float* __restrict__ zf) {
  __shared__ float kv[272];
  const int h = blockIdx.x >> 3, ch = blockIdx.x & 7;
  const int tid = threadIdx.x;
  const int c = tid & 63, dh = (tid >> 6) & 1;
  float w[16];
  if (tid < 128) {
#pragma unroll
    for (int e = 0; e < 16; ++e) w[e] = bf_lo(wo[(size_t)(h * 16 + e) * D_ + ch * 64 + c]);
  }
  for (int i = tid; i < 272; i += 256) {
    float s = 0.f;
#pragma unroll
    for (int p = 0; p < 16; ++p) s += part[(size_t)(p * 32 + h) * 272 + i];
    kv[i] = bfr(s);
  }
  __syncthreads();
  if (ch == 0 && tid < 16) zf[h * 16 + tid] = kv[256 + tid];
  if (tid < 128) {
    float o[8];
#pragma unroll
    for (int j = 0; j < 8; ++j) {
      float s = 0.f;
#pragma unroll
      for (int e = 0; e < 16; ++e) s = fmaf(kv[(dh * 8 + j) * 16 + e], w[e], s);
      o[j] = s;
    }
    const int n = ch * 64 + c, nb = n >> 4, cc = n & 15;
    const int ks = h >> 1, g = (h & 1) * 2 + dh;
    u32x4 v = {pack_bf(o[0], o[1]), pack_bf(o[2], o[3]), pack_bf(o[4], o[5]), pack_bf(o[6], o[7])};
    *(u32x4*)(wp + (size_t)((nb * 16 + ks) * 64 + g * 16 + cc) * 8) = v;
  }
}

__global__ __launch_bounds__(256) void k2_attn_out(const u16* __restrict__ x, const u16* __restrict__ pq,
                                                   const u16* __restrict__ wp, const float* __restrict__ zf,
                                                   u16* __restrict__ hbuf) {
  __shared__ __attribute__((aligned(16))) char smem[65536 + 2048];
  const int tid = threadIdx.x, lane = tid & 63;
  const int wave = __builtin_amdgcn_readfirstlane(tid >> 6);
  const int xcd = blockIdx.x & 7, idx = blockIdx.x >> 3;
  const int rt = xcd * 4 + (idx >> 3);
  const int ct = idx & 7;
  const int wr = wave >> 1, wc = wave & 1;
  const int r = lane & 15, g = lane >> 4;
  const int row0 = rt * 64 + wr * 32;

  u32x4 a[2][16];
#pragma unroll
  for (int mb = 0; mb < 2; ++mb)
#pragma unroll
    for (int ks = 0; ks < 16; ++ks)
      a[mb][ks] = *(const u32x4*)(pq + (size_t)(row0 + mb * 16 + r) * D_ + ks * 32 + g * 8);
#pragma unroll
  for (int i = 0; i < 16; ++i) {
    const int ins = wave * 16 + i;
    glds16(wp + (size_t)ct * 32768 + ins * 512 + lane * 8, smem + ins * 1024 + lane * 16);
  }
  {
    f32x2 zz = *(const f32x2*)(zf + tid * 2);
    *(f32x2*)(smem + 65536 + tid * 8) = zz;
  }
  u32x2 xr[2][2];
#pragma unroll
  for (int mb = 0; mb < 2; ++mb)
#pragma unroll
    for (int nb = 0; nb < 2; ++nb)
      xr[mb][nb] = *(const u32x2*)(x + (size_t)(row0 + mb * 16 + r) * D_ + ct * 64 + wc * 32 + nb * 16 + g * 4);
  WAIT_VM(0);
  __syncthreads();

  const float* zl = (const float*)(smem + 65536) + g * 8;
  f32x4 acc[2][2] = {};
#pragma unroll
  for (int ks = 0; ks < 16; ++ks) {
    const f32x4 z0 = *(const f32x4*)(zl + ks * 32);
    const f32x4 z1 = *(const f32x4*)(zl + ks * 32 + 4);
    u32x4 as[2];
#pragma unroll
    for (int mb = 0; mb < 2; ++mb) {
      const u32x4 u = a[mb][ks];
      float f[8];
#pragma unroll
      for (int i = 0; i < 4; ++i) { f[2 * i] = bf_lo(u[i]); f[2 * i + 1] = bf_hi(u[i]); }
      float s = f[0] * z0[0];
      s = fmaf(f[1], z0[1], s); s = fmaf(f[2], z0[2], s); s = fmaf(f[3], z0[3], s);
      s = fmaf(f[4], z1[0], s); s = fmaf(f[5], z1[1], s); s = fmaf(f[6], z1[2], s); s = fmaf(f[7], z1[3], s);
      s += __shfl_xor(s, 16);
      const float den = bfr(bfr(s) + 1e-6f);
      const float inv = 1.f / den;
#pragma unroll
      for (int i = 0; i < 4; ++i) as[mb][i] = pack_bf(f[2 * i] * inv, f[2 * i + 1] * inv);
    }
#pragma unroll
    for (int nb = 0; nb < 2; ++nb) {
      const u32x4 b = *(const u32x4*)(smem + ((wc * 2 + nb) * 16 + ks) * 1024 + lane * 16);
#pragma unroll
      for (int mb = 0; mb < 2; ++mb) acc[mb][nb] = mfma16(b, as[mb], acc[mb][nb]);
    }
  }
#pragma unroll
  for (int mb = 0; mb < 2; ++mb)
#pragma unroll
    for (int nb = 0; nb < 2; ++nb) {
      u32x2 o;
      o[0] = pack_bf(bf_lo(xr[mb][nb][0]) + bfr(acc[mb][nb][0]), bf_hi(xr[mb][nb][0]) + bfr(acc[mb][nb][1]));
      o[1] = pack_bf(bf_lo(xr[mb][nb][1]) + bfr(acc[mb][nb][2]), bf_hi(xr[mb][nb][1]) + bfr(acc[mb][nb][3]));
      *(u32x2*)(hbuf + (size_t)(row0 + mb * 16 + r) * D_ + ct * 64 + wc * 32 + nb * 16 + g * 4) = o;
    }
}

__global__ __launch_bounds__(512) void k3_gateup(const u16* __restrict__ hbuf, const u16* __restrict__ wg,
                                                 const u16* __restrict__ wu, u16* __restrict__ hhf) {
  __shared__ __attribute__((aligned(16))) char smem[3 * 49152];
  const int tid = threadIdx.x, lane = tid & 63;
  const int wave = __builtin_amdgcn_readfirstlane(tid >> 6);
  const int xcd = blockIdx.x & 7, idx = blockIdx.x >> 3;
  const int rt = (xcd >> 1) * 4 + (idx >> 3);
  const int cg = (xcd & 1) * 8 + (idx & 7);
  const int rw = wave & 3, hw = wave >> 2;
  const int r = lane & 15, g = lane >> 4;
  const int row0 = rt * 128 + rw * 32;
  const int f0 = cg < 11 ? cg * 96 : 1056 + (cg - 11) * 64;
  const int nvalid = cg < 11 ? 6 : 4;

  u32x4 a[2][16];
#pragma unroll
  for (int mb = 0; mb < 2; ++mb)
#pragma unroll
    for (int ks = 0; ks < 16; ++ks)
      a[mb][ks] = *(const u32x4*)(hbuf + (size_t)(row0 + mb * 16 + r) * D_ + ks * 32 + g * 8);

  const u16* src[6];
  {
    const int kkp = (lane & 15) >> 1, half = lane & 1;
#pragma unroll
    for (int i = 0; i < 6; ++i) {
      const int blk = 4 * i + g;
      const int kbl = blk / 12, nb = blk % 12;
      const int kk = kkp ^ ((kbl & 1) << 2);
      const int k = wave * 16 + kbl * 8 + kk;
      int fb = nb < 6 ? nb : nb - 6;
      fb = fb < nvalid ? fb : 0;
      const u16* wsel = nb < 6 ? wg : wu;
      src[i] = wsel + (size_t)k * F_ + f0 + fb * 16 + half * 8;
    }
  }
  auto stage = [&](int c, int buf) {
#pragma unroll
    for (int i = 0; i < 6; ++i)
      glds16(src[i] + (size_t)c * 128 * F_, smem + buf * 49152 + (wave * 24 + 4 * i) * 256 + lane * 16);
  };
  stage(0, 0);
  ln_frags<2>(a);
  WAIT_VM(0);
  stage(1, 1);
  stage(2, 2);
  RAW_BAR();

  const int q4 = (lane & 15) >> 2, p = lane & 3;
  const char* lb0 = smem + g * 3072 + ((q4) ^ (4 * (g & 1))) * 32 + p * 8 + hw * 768;
  const char* lb1 = smem + g * 3072 + ((4 + q4) ^ (4 * (g & 1))) * 32 + p * 8 + hw * 768;
  f32x4 accg[2][3] = {}, accu[2][3] = {};
  auto compute = [&](int c, int buf) {
#pragma unroll
    for (int ksl = 0; ksl < 4; ++ksl) {
      const int base = buf * 49152 + ksl * 12288;
#pragma unroll
      for (int nb = 0; nb < 3; ++nb) {
        const u32x4 bg = tr_frag(lb0 + base + nb * 256, lb1 + base + nb * 256);
        const u32x4 bu = tr_frag(lb0 + base + 1536 + nb * 256, lb1 + base + 1536 + nb * 256);
#pragma unroll
        for (int mb = 0; mb < 2; ++mb) {
          accg[mb][nb] = mfma16(bg, a[mb][c * 4 + ksl], accg[mb][nb]);
          accu[mb][nb] = mfma16(bu, a[mb][c * 4 + ksl], accu[mb][nb]);
        }
      }
    }
  };
  compute(0, 0);
  WAIT_VM(6);
  RAW_BAR();
  stage(3, 0);
  compute(1, 1);
  WAIT_VM(6);
  RAW_BAR();
  compute(2, 2);
  WAIT_VM(0);
  RAW_BAR();
  compute(3, 0);

#pragma unroll
  for (int nb = 0; nb < 3; ++nb) {
    const int fbl = hw * 3 + nb;
    if (fbl < nvalid) {
      const int fb = (f0 >> 4) + fbl;
      const int ks4 = fb >> 1, half = fb & 1;
#pragma unroll
      for (int mb = 0; mb < 2; ++mb) {
        u32x2 o;
        o[0] = pack_bf(bfr(accg[mb][nb][0]) * bfr(accu[mb][nb][0]), bfr(accg[mb][nb][1]) * bfr(accu[mb][nb][1]));
        o[1] = pack_bf(bfr(accg[mb][nb][2]) * bfr(accu[mb][nb][2]), bfr(accg[mb][nb][3]) * bfr(accu[mb][nb][3]));
        const int mblk = (row0 >> 4) + mb;
        *(u32x2*)(hhf + ((size_t)(mblk * KS4 + ks4) * 64 + lane) * 8 + half * 4) = o;
      }
    }
  }
}

__global__ __launch_bounds__(512) void k4_down(const u16* __restrict__ hbuf, const u16* __restrict__ hhf,
                                               const u16* __restrict__ wd, u16* __restrict__ out) {
  __shared__ __attribute__((aligned(16))) char smem[4 * 32768];
  const int tid = threadIdx.x, lane = tid & 63;
  const int wave = __builtin_amdgcn_readfirstlane(tid >> 6);
  const int xcd = blockIdx.x & 7, idx = blockIdx.x >> 3;
  const int rt = xcd * 4 + (idx >> 3);
  const int ct = idx & 7;
  const int kw = wave >> 2, wr = (wave >> 1) & 1, wc = wave & 1;
  const int r = lane & 15, g = lane >> 4;

  auto stage = [&](int s) {
    char* slot = smem + (s & 3) * 32768;
#pragma unroll
    for (int ii = 0; ii < 2; ++ii) {
      const int i = wave * 2 + ii;
      int ks = 4 * s + (i & 3);
      ks = ks < KS4 ? ks : KS4 - 1;
      glds16(hhf + ((size_t)((rt * 4 + (i >> 2)) * KS4 + ks) * 64 + lane) * 8, slot + i * 1024 + lane * 16);
    }
#pragma unroll
    for (int ii = 0; ii < 2; ++ii) {
      const int i = wave * 2 + ii;
      int k = 128 * s + 8 * i + ((lane & 15) >> 1);
      k = k < F_ ? k : F_ - 1;
      glds16(wd + (size_t)k * D_ + ct * 64 + g * 16 + (lane & 1) * 8, slot + 16384 + i * 1024 + lane * 16);
    }
  };
  u32x2 hr[2][2];
  if (kw == 0) {
#pragma unroll
    for (int mb = 0; mb < 2; ++mb)
#pragma unroll
      for (int nb = 0; nb < 2; ++nb)
        hr[mb][nb] = *(const u32x2*)(hbuf + (size_t)(rt * 64 + wr * 32 + mb * 16 + r) * D_ + ct * 64 + wc * 32 + nb * 16 + g * 4);
    WAIT_VM(0);
  }
  stage(0); stage(1); stage(2);

  const int q4 = (lane & 15) >> 2, p = lane & 3;
  const int boff = 16384 + (g >> 1) * 1024 + (4 * (g & 1) + q4) * 32 + p * 8 + wc * 512;
  const int aoff = wr * 8192 + lane * 16;
  f32x4 acc[2][2] = {};
#pragma unroll
  for (int s = 0; s < 11; ++s) {
    if (s <= 8) { WAIT_VM(8); } else if (s == 9) { WAIT_VM(4); } else { WAIT_VM(0); }
    RAW_BAR();
    if (s + 3 < 11) stage(s + 3);
    const char* slot = smem + (s & 3) * 32768;
#pragma unroll
    for (int kk = 0; kk < 2; ++kk) {
      const int ksl = kw * 2 + kk;
      if (4 * s + ksl < KS4) {
        u32x4 af[2], bf[2];
#pragma unroll
        for (int mb = 0; mb < 2; ++mb) af[mb] = *(const u32x4*)(slot + aoff + mb * 4096 + ksl * 1024);
#pragma unroll
        for (int nb = 0; nb < 2; ++nb)
          bf[nb] = tr_frag(slot + boff + ksl * 4096 + nb * 256, slot + boff + ksl * 4096 + 2048 + nb * 256);
#pragma unroll
        for (int mb = 0; mb < 2; ++mb)
#pragma unroll
          for (int nb = 0; nb < 2; ++nb) acc[mb][nb] = mfma16(bf[nb], af[mb], acc[mb][nb]);
      }
    }
  }
  __syncthreads();
  float* red = (float*)smem + (wave & 3) * 1024;
  if (kw == 1) {
#pragma unroll
    for (int mb = 0; mb < 2; ++mb)
#pragma unroll
      for (int nb = 0; nb < 2; ++nb) *(f32x4*)(red + ((mb * 2 + nb) * 64 + lane) * 4) = acc[mb][nb];
  }
  __syncthreads();
  if (kw == 0) {
#pragma unroll
    for (int mb = 0; mb < 2; ++mb)
#pragma unroll
      for (int nb = 0; nb < 2; ++nb) {
        const f32x4 o4 = acc[mb][nb] + *(const f32x4*)(red + ((mb * 2 + nb) * 64 + lane) * 4);
        u32x2 o;
        o[0] = pack_bf(bf_lo(hr[mb][nb][0]) + bfr(o4[0]), bf_hi(hr[mb][nb][0]) + bfr(o4[1]));
        o[1] = pack_bf(bf_lo(hr[mb][nb][1]) + bfr(o4[2]), bf_hi(hr[mb][nb][1]) + bfr(o4[3]));
        *(u32x2*)(out + (size_t)(rt * 64 + wr * 32 + mb * 16 + r) * D_ + ct * 64 + wc * 32 + nb * 16 + g * 4) = o;
      }
  }
}

extern "C" void kernel_launch(void* const* d_in, const int* in_sizes, int n_in, void* d_out, int out_size,
                              void* d_ws, size_t ws_size, hipStream_t stream) {
  const u16* x = (const u16*)d_in[0];
  const u16* wqkv = (const u16*)d_in[1];
  const u16* wo = (const u16*)d_in[2];
  const u16* wg = (const u16*)d_in[4];
  const u16* wu = (const u16*)d_in[5];
  const u16* wd = (const u16*)d_in[6];
  u16* out = (u16*)d_out;
  char* ws = (char*)d_ws;
  u16* pq = (u16*)(ws);
  u16* hbuf = (u16*)(ws + (2u << 20));
  u16* wp = (u16*)(ws + (4u << 20));
  float* part = (float*)(ws + (5u << 20));
  float* zf = (float*)(ws + (6u << 20));
  u16* hhf = (u16*)(ws + (8u << 20));
  k1_qkv<<<256, 512, 0, stream>>>(x, wqkv, pq, part);
  k15_fold<<<256, 256, 0, stream>>>(part, wo, wp, zf);
  k2_attn_out<<<256, 256, 0, stream>>>(x, pq, wp, zf, hbuf);
  k3_gateup<<<256, 512, 0, stream>>>(hbuf, wg, wu, hhf);
  k4_down<<<256, 512, 0, stream>>>(hbuf, hhf, wd, out);
}
```

```cpp
#include <hip/hip_runtime.h>
#include <hip/hip_bf16.h>
#include <stdint.h>

typedef unsigned short u16;
typedef __attribute__((ext_vector_type(8))) __bf16 bf16x8;
typedef __attribute__((ext_vector_type(2))) __bf16 bf16x2;
typedef __attribute__((ext_vector_type(4))) short s16x4;
typedef __attribute__((ext_vector_type(4))) float f32x4;
typedef __attribute__((ext_vector_type(2))) float f32x2;
typedef __attribute__((ext_vector_type(4))) unsigned u32x4;
typedef __attribute__((ext_vector_type(2))) unsigned u32x2;
typedef __attribute__((address_space(3))) s16x4* lds_s16x4_ptr;

#define DI __device__ __forceinline__
#define STAMP(kid,i)

constexpr int S_ = 2048, D_ = 512, F_ = 1376, H_ = 32;
constexpr int KS4 = F_ / 32;

DI float bf_lo(unsigned u) { return __uint_as_float(u << 16); }
DI float bf_hi(unsigned u) { return __uint_as_float(u & 0xffff0000u); }
DI unsigned pack_bf(float lo, float hi) {
  f32x2 f = {lo, hi};
  bf16x2 r = __builtin_convertvector(f, bf16x2);
  return __builtin_bit_cast(unsigned, r);
}
DI float bfr(float f) { return bf_lo(pack_bf(f, 0.f) & 0xffffu); }
DI f32x4 mfma16(u32x4 a, u32x4 b, f32x4 c) {
  return __builtin_amdgcn_mfma_f32_16x16x32_bf16(__builtin_bit_cast(bf16x8, a),
                                                 __builtin_bit_cast(bf16x8, b), c, 0, 0, 0);
}
DI float dot2(unsigned a, unsigned b, float c) {
  return __builtin_amdgcn_fdot2_f32_bf16(__builtin_bit_cast(bf16x2, a), __builtin_bit_cast(bf16x2, b), c, false);
}
DI u32x4 tr_frag(const char* lds0, const char* lds1) {
  s16x4 lo = __builtin_amdgcn_ds_read_tr16_b64_v4i16((lds_s16x4_ptr)lds0);
  s16x4 hi = __builtin_amdgcn_ds_read_tr16_b64_v4i16((lds_s16x4_ptr)lds1);
  u32x2 l2 = __builtin_bit_cast(u32x2, lo), h2 = __builtin_bit_cast(u32x2, hi);
  u32x4 r = {l2[0], l2[1], h2[0], h2[1]};
  return r;
}
DI unsigned lds_addr(char* l) { return (unsigned)(size_t)(__attribute__((address_space(3))) char*)l; }
DI void glds16(const void* sbase, unsigned voff, unsigned dst) {
  asm volatile("s_mov_b32 m0, %2\n\ts_nop 0\n\tglobal_load_lds_dwordx4 %0, %1"
               :: "v"(voff), "s"(sbase), "s"(dst) : "memory");
}
DI float red16(float s) {
  const unsigned u = __float_as_uint(s);
  auto r = __builtin_amdgcn_permlane16_swap(u, u, false, false);
  return __uint_as_float(r[0]) + __uint_as_float(r[1]);
}
DI float red32(float s) {
  const unsigned u = __float_as_uint(s);
  auto r = __builtin_amdgcn_permlane32_swap(u, u, false, false);
  return __uint_as_float(r[0]) + __uint_as_float(r[1]);
}
#define WAIT_VM(n) asm volatile("s_waitcnt vmcnt(" #n ")" ::: "memory")
#define RAW_BAR() do { asm volatile("s_waitcnt lgkmcnt(0)" ::: "memory"); __builtin_amdgcn_s_barrier(); } while (0)
#define BARE_BAR() do { asm volatile("" ::: "memory"); __builtin_amdgcn_s_barrier(); } while (0)
template <int N> DI void wait_vm() { asm volatile("s_waitcnt vmcnt(%0)" ::"i"(N) : "memory"); }

DI float phi_f(float t) { return (t > 0.f ? t + 1.f : __expf(t)) + 1e-6f; }
DI void sq_frag(const u32x4& a, float& q) {
#pragma unroll
  for (int i = 0; i < 4; ++i) q = dot2(a[i], a[i], q);
}
#define ONES_FRAG ((u32x4){0x3f803f80u, 0x3f803f80u, 0x3f803f80u, 0x3f803f80u})

constexpr int K1_SLOT = 16384 + 12288, K1_NSLOT = 4;
__global__ __launch_bounds__(512) void k1_qkv(const u16* __restrict__ x, const u16* __restrict__ wqkv,
                                              u16* __restrict__ pq, float* __restrict__ part,
                                              const u16* __restrict__ wg, const u16* __restrict__ wu,
                                              float* __restrict__ csp) {
  __shared__ __attribute__((aligned(16))) char smem[K1_SLOT * K1_NSLOT];
  const int tid = threadIdx.x, lane = tid & 63;
  const int wave = __builtin_amdgcn_readfirstlane(tid >> 6);
  const int xcd = blockIdx.x & 7, idx = blockIdx.x >> 3;
  const int rt = (xcd >> 1) * 4 + (idx >> 3);
  const int cg = (xcd & 1) * 8 + (idx & 7);
  const int r = lane & 15, g = lane >> 4;
  STAMP(0,0);
  if (wave >= 4) {
    const int lw = wave - 4;
    const unsigned lbase = lds_addr(smem);
    const unsigned aoffs = (unsigned)(lw * 32 + (lane >> 3)) * 1024u + (((lane & 7) ^ ((lane >> 4) & 3)) << 4);
    unsigned boffs[3];
#pragma unroll
    for (int i = 0; i < 3; ++i) {
      const int line = (lw * 3 + i) * 4 + g;
      const int kq = line / 3, cgrp = line % 3;
      const int k = kq * 4 + ((lane >> 2) & 3);
      const int ch = (lane & 3) ^ (((k >> 3) & 1) << 1);
      boffs[i] = (unsigned)(k * 1536 + cgrp * 512 + ch * 8) * 2u;
    }
    const char* abase = (const char*)(x + (size_t)rt * 128 * D_);
    const char* bbase = (const char*)(wqkv + cg * 32);
    auto stage = [&](int s) {
      const unsigned slot = lbase + (s % K1_NSLOT) * K1_SLOT;
#pragma unroll
      for (int i = 0; i < 4; ++i)
        glds16(abase + s * 128 + i * 8192, (i & 1) ? (aoffs ^ 64u) : aoffs, slot + lw * 4096 + i * 1024);
#pragma unroll
      for (int i = 0; i < 3; ++i)
        glds16(bbase + (size_t)s * 64 * 3072, boffs[i], slot + 16384 + (lw * 3 + i) * 1024);
    };
    stage(0); stage(1);
    wait_vm<7>();
    BARE_BAR();
    stage(2); stage(3);
#pragma unroll
    for (int s = 0; s < 7; ++s) {
      if (s <= 4) wait_vm<14>(); else if (s == 5) wait_vm<7>(); else wait_vm<0>();
      BARE_BAR();
      if (s + 4 < 8) stage(s + 4);
    }
    const int W = blockIdx.x * 4 + lw;
    const int grp = W % 86, kp = (W / 86) & 7;
    u32x4 wv[4];
    {
      const u16* wsrc = (grp < 43 ? wg : wu) + (grp % 43) * 32 + (lane & 3) * 8 + (size_t)(kp * 64 + (lane >> 2)) * F_;
#pragma unroll
      for (int i = 0; i < 4; ++i) wv[i] = *(const u32x4*)(wsrc + (size_t)i * 16 * F_);
    }
    BARE_BAR();
    if (W < 86 * 8) {
      float c8[8];
#pragma unroll
      for (int j = 0; j < 4; ++j) {
        c8[2 * j] = (bf_lo(wv[0][j]) + bf_lo(wv[1][j])) + (bf_lo(wv[2][j]) + bf_lo(wv[3][j]));
        c8[2 * j + 1] = (bf_hi(wv[0][j]) + bf_hi(wv[1][j])) + (bf_hi(wv[2][j]) + bf_hi(wv[3][j]));
      }
#pragma unroll
      for (int j = 0; j < 8; ++j) {
        float v = c8[j];
        v += __shfl_xor(v, 4); v += __shfl_xor(v, 8);
        c8[j] = red32(red16(v));
      }
      if (lane < 4) {
        float* dst = csp + kp * 2752 + grp * 32 + lane * 8;
        f32x4 v0 = {c8[0], c8[1], c8[2], c8[3]}, v1 = {c8[4], c8[5], c8[6], c8[7]};
        *(f32x4*)dst = v0; *(f32x4*)(dst + 4) = v1;
      }
    }
    BARE_BAR();
    return;
  }
  const int wr = wave >> 1, wc = wave & 1;
  const int q4 = (lane & 15) >> 2, p = lane & 3;
  const int aoff = (wr * 64 + r) * 128 + ((g ^ ((r >> 1) & 7)) << 4);
  const int aoff0 = aoff + wc * 4096, aoff1 = aoff + (wc ^ 1) * 4096;
  const int boff = 16384 + (2 * g) * 768 + q4 * 64 + ((wc ^ (g & 1)) << 5) + p * 8;
  const u32x4 ones = ONES_FRAG;
  f32x4 accq[4] = {}, acck[4] = {}, accv[4] = {};
  f32x4 rs[2] = {}, sqm[2] = {}, cq = {}, ck = {}, cv = {};
  u32x4 fa[2][4], fb[2][3];
  auto rdfrags = [&](int t) {
    const char* slot = smem + ((t >> 1) % K1_NSLOT) * K1_SLOT;
    const int kk = t & 1;
#pragma unroll
    for (int i = 0; i < 4; ++i)
      fa[kk][i] = *(const u32x4*)(slot + (((i < 2 ? aoff0 : aoff1) + (i & 1) * 2048) ^ (kk << 6)));
#pragma unroll
    for (int j = 0; j < 3; ++j)
      fb[kk][j] = tr_frag(slot + boff + kk * 6144 + j * 256, slot + boff + kk * 6144 + 768 + j * 256);
  };
  auto compute = [&](int t) {
    const int kk = t & 1;
    const u32x4* a = fa[kk];
    const u32x4 bq = fb[kk][0], bk = fb[kk][1], bv = fb[kk][2];
#pragma unroll
    for (int i = 0; i < 4; ++i) {
      accq[i] = mfma16(bq, a[i], accq[i]);
      acck[i] = mfma16(a[i], bk, acck[i]);
      accv[i] = mfma16(a[i], bv, accv[i]);
    }
    rs[0] = mfma16(ones, a[0], rs[0]);
    rs[1] = mfma16(ones, a[1], rs[1]);
    sqm[0] = mfma16(a[0], a[0], sqm[0]);
    sqm[1] = mfma16(a[1], a[1], sqm[1]);
    if (kk == wr) {
      cq = mfma16(bq, ones, cq);
      ck = mfma16(ones, bk, ck);
      cv = mfma16(ones, bv, cv);
    }
  };
  BARE_BAR();
  STAMP(0,2);
  rdfrags(0);
#pragma unroll
  for (int s = 0; s < 8; ++s) {
    rdfrags(2 * s + 1);
    compute(2 * s);
    __builtin_amdgcn_sched_barrier(0);
    if (s + 1 < 8) {
      RAW_BAR();
      STAMP(0,3+s);
      rdfrags(2 * s + 2);
    }
    compute(2 * s + 1);
    __builtin_amdgcn_sched_barrier(0);
  }
  STAMP(0,11);
  float* st = (float*)(smem);
  float* csl = (float*)(smem + 2048);
  float* st2 = (float*)(smem + 4096);
  float* red = (float*)(smem + 8192);
  {
#pragma unroll
    for (int i = 0; i < 2; ++i) {
      const float q01 = (r & 1) ? sqm[i][1] : sqm[i][0], q23 = (r & 1) ? sqm[i][3] : sqm[i][2];
      const float q = (r & 2) ? q23 : q01;
      if (g == (r >> 2)) {
        f32x2 v = {rs[i][0], q};
        *(f32x2*)(st + (wr * 64 + wc * 32 + i * 16 + r) * 2) = v;
      }
    }
    float* c = csl + (wr * 2 + wc) * 48;
    if (r == 0) *(f32x4*)(c + 4 * g) = cq;
    if (g == 0) { c[16 + r] = ck[0]; c[32 + r] = cv[0]; }
  }
  __syncthreads();
  STAMP(0,12);
  {
    const f32x2 a0 = *(const f32x2*)(st + (wr * 64 + lane) * 2);
    const float mean = a0[0] * (1.f / 512.f);
    const float var = fmaxf(a0[1] * (1.f / 512.f) - mean * mean, 0.f);
    f32x2 v = {mean, __builtin_amdgcn_rsqf(var + 1e-5f)};
    *(f32x2*)(st2 + (wave * 64 + lane) * 2) = v;
  }
  const f32x4 csq4 = *(const f32x4*)(csl + wc * 48 + 4 * g) + *(const f32x4*)(csl + (2 + wc) * 48 + 4 * g);
  const float csk = csl[wc * 48 + 16 + r] + csl[(2 + wc) * 48 + 16 + r];
  const float csv = csl[wc * 48 + 32 + r] + csl[(2 + wc) * 48 + 32 + r];
  f32x4 kv = {}, zz = {};
#pragma unroll
  for (int ih = 0; ih < 2; ++ih) {
    u32x4 pk, vv;
#pragma unroll
    for (int i2 = 0; i2 < 2; ++i2) {
      const int i = ih * 2 + i2;
      const int mbl = ((ih ^ wc) << 1) + i2;
      {
        const f32x2 mi = *(const f32x2*)(st2 + (wave * 64 + mbl * 16 + r) * 2);
        float qv[4];
#pragma unroll
        for (int j = 0; j < 4; ++j) qv[j] = phi_f((accq[i][j] - mi[0] * csq4[j]) * mi[1]);
        u32x2 o = {pack_bf(qv[0], qv[1]), pack_bf(qv[2], qv[3])};
        const int mblk = rt * 8 + wr * 4 + mbl;
        *(u32x2*)(pq + ((size_t)(mblk * 16 + cg) * 64 + r + 16 * (2 * wc + (g >> 1))) * 8 + (g & 1) * 4) = o;
      }
      {
        const f32x4 m0 = *(const f32x4*)(st2 + (wave * 64 + mbl * 16 + 4 * g) * 2);
        const f32x4 m1 = *(const f32x4*)(st2 + (wave * 64 + mbl * 16 + 4 * g) * 2 + 4);
        const float mean[4] = {m0[0], m0[2], m1[0], m1[2]}, inv[4] = {m0[1], m0[3], m1[1], m1[3]};
        float kk[4], vj[4];
#pragma unroll
        for (int j = 0; j < 4; ++j) {
          kk[j] = phi_f((acck[i][j] - mean[j] * csk) * inv[j]);
          vj[j] = (accv[i][j] - mean[j] * csv) * inv[j];
        }
        pk[2 * i2] = pack_bf(kk[0], kk[1]);
        pk[2 * i2 + 1] = pack_bf(kk[2], kk[3]);
        vv[2 * i2] = pack_bf(vj[0], vj[1]);
        vv[2 * i2 + 1] = pack_bf(vj[2], vj[3]);
      }
    }
    kv = mfma16(pk, vv, kv);
    zz = mfma16(pk, ones, zz);
  }
  {
    float* rd = red + (wc * 2 + wr) * 272;
#pragma unroll
    for (int j = 0; j < 4; ++j) rd[(4 * g + j) * 16 + r] = kv[j];
    if (r == 0) *(f32x4*)(rd + 256 + 4 * g) = zz;
  }
  __syncthreads();
  for (int i = tid; i < 544; i += 256) {
    const int hh = i >= 272 ? 1 : 0, e = i - hh * 272;
    const float* rr = red + hh * 2 * 272 + e;
    part[(size_t)(rt * 32 + cg * 2 + hh) * 272 + e] = rr[0] + rr[272];
  }
  STAMP(0,13);
}

__global__ __launch_bounds__(320) void k15_fold(const float* __restrict__ part, const u16* __restrict__ wo,
                                                u16* __restrict__ wp, u16* __restrict__ zb,
                                                const float* __restrict__ csp, float* __restrict__ csgu) {
  __shared__ float kv[272];
  const int h = blockIdx.x >> 3, ch = blockIdx.x & 7;
  const int tid = threadIdx.x;
  const int c = tid & 63, dh = (tid >> 6) & 1;
  STAMP(1,0);
  float pv[16];
  if (tid < 272) {
#pragma unroll
    for (int p = 0; p < 16; ++p) pv[p] = part[(size_t)(p * 32 + h) * 272 + tid];
  }
  float w[16];
  if (tid < 128) {
#pragma unroll
    for (int e = 0; e < 16; ++e) w[e] = bf_lo(wo[(size_t)(h * 16 + e) * D_ + ch * 64 + c]);
  }
  {
    const int col = blockIdx.x * 11 + (tid - 272);
    if (tid >= 272 && tid < 283 && col < 2752) {
      float c = 0.f;
#pragma unroll
      for (int kp = 0; kp < 8; ++kp) c += csp[kp * 2752 + col];
      csgu[col] = c;
    }
  }
  if (tid < 272) {
    float s = 0.f;
#pragma unroll
    for (int p = 0; p < 16; ++p) s += pv[p];
    kv[tid] = s;
    if (ch == 0 && tid >= 256) zb[h * 16 + tid - 256] = (u16)(pack_bf(s, 0.f) & 0xffffu);
  }
  STAMP(1,1);
  __syncthreads();
  STAMP(1,2);
  if (tid < 128) {
    float o[8];
#pragma unroll
    for (int j = 0; j < 8; ++j) {
      float s = 0.f;
#pragma unroll
      for (int e = 0; e < 16; ++e) s = fmaf(kv[(dh * 8 + j) * 16 + e], w[e], s);
      o[j] = s;
    }
    const int n = ch * 64 + c, nb = n >> 4, cc = n & 15;
    const int ks = h >> 1, g = (h & 1) * 2 + dh;
    u32x4 v = {pack_bf(o[0], o[1]), pack_bf(o[2], o[3]), pack_bf(o[4], o[5]), pack_bf(o[6], o[7])};
    *(u32x4*)(wp + (size_t)((nb * 16 + ks) * 64 + g * 16 + cc) * 8) = v;
  }
  STAMP(1,3);
}

__global__ __launch_bounds__(512) void k2_attn_out(const u16* __restrict__ x, const u16* __restrict__ pq,
                                                   const u16* __restrict__ wp, const u16* __restrict__ zb,
                                                   u16* __restrict__ hf, float* __restrict__ hst) {
  __shared__ __attribute__((aligned(16))) char smem[65536 + 16384];
  const int tid = threadIdx.x, lane = tid & 63;
  const int wave = __builtin_amdgcn_readfirstlane(tid >> 6);
  const int xcd = blockIdx.x & 7, idx = blockIdx.x >> 3;
  const int rt = xcd * 4 + (idx >> 3);
  const int ct = idx & 7;
  const int kg = wave >> 2, rb = wave & 3;
  const int r = lane & 15, g = lane >> 4;
  const int mblk = rt * 4 + rb;
  STAMP(2,0);

  u32x4 a[8];
#pragma unroll
  for (int i = 0; i < 8; ++i) a[i] = *(const u32x4*)(pq + ((size_t)(mblk * 16 + kg * 8 + i) * 64 + lane) * 8);
  u32x4 zf[8];
#pragma unroll
  for (int i = 0; i < 8; ++i) zf[i] = *(const u32x4*)(zb + (kg * 8 + i) * 32 + g * 8);
  {
    const char* wbase = (const char*)(wp + (size_t)ct * 32768) + wave * 8192;
#pragma unroll
    for (int i = 0; i < 8; ++i) glds16(wbase + i * 1024, lane * 16, lds_addr(smem) + wave * 8192 + i * 1024);
  }
  u32x2 xr[2];
#pragma unroll
  for (int j = 0; j < 2; ++j)
    xr[j] = *(const u32x2*)(x + (size_t)(mblk * 16 + r) * D_ + ct * 64 + (kg * 2 + j) * 16 + g * 4);
  STAMP(2,1);
#pragma unroll
  for (int i = 0; i < 8; ++i) {
    float s = dot2(a[i][0], zf[i][0], 0.f);
    s = dot2(a[i][1], zf[i][1], s);
    s = dot2(a[i][2], zf[i][2], s);
    s = dot2(a[i][3], zf[i][3], s);
    const float inv = __builtin_amdgcn_rcpf(red16(s) + 1e-6f);
#pragma unroll
    for (int j = 0; j < 4; ++j) a[i][j] = pack_bf(bf_lo(a[i][j]) * inv, bf_hi(a[i][j]) * inv);
  }
  STAMP(2,2);
  WAIT_VM(0);
  __syncthreads();
  STAMP(2,3);
  f32x4 acc[4] = {};
#pragma unroll
  for (int i = 0; i < 8; ++i) {
#pragma unroll
    for (int nb = 0; nb < 4; ++nb) {
      const u32x4 b = *(const u32x4*)(smem + (nb * 16 + kg * 8 + i) * 1024 + lane * 16);
      acc[nb] = mfma16(b, a[i], acc[nb]);
    }
  }
  STAMP(2,4);
  f32x4* xch = (f32x4*)(smem + 65536);
#pragma unroll
  for (int j = 0; j < 2; ++j) xch[(wave * 2 + j) * 64 + lane] = kg ? acc[j] : acc[2 + j];
  __syncthreads();
  float hs = 0.f, hq = 0.f;
#pragma unroll
  for (int j = 0; j < 2; ++j) {
    const f32x4 o4 = (kg ? acc[2 + j] : acc[j]) + xch[((wave ^ 4) * 2 + j) * 64 + lane];
    u32x2 o;
    o[0] = pack_bf(bf_lo(xr[j][0]) + o4[0], bf_hi(xr[j][0]) + o4[1]);
    o[1] = pack_bf(bf_lo(xr[j][1]) + o4[2], bf_hi(xr[j][1]) + o4[3]);
    *(u32x2*)(hf + ((size_t)(mblk * 16 + ct * 2 + kg) * 64 + r + 16 * (2 * j + (g >> 1))) * 8 + (g & 1) * 4) = o;
    hs = dot2(o[0], 0x3f803f80u, hs); hs = dot2(o[1], 0x3f803f80u, hs);
    hq = dot2(o[0], o[0], hq); hq = dot2(o[1], o[1], hq);
  }
  hs = red32(red16(hs)); hq = red32(red16(hq));
  if (g == 0) {
    f32x2 v = {hs, hq};
    *(f32x2*)(hst + ((size_t)(mblk * 16 + r) * 16 + ct * 2 + kg) * 2) = v;
  }
  STAMP(2,5);
}

constexpr int K3_SLOT = 16384 + 24576, K3_NSLOT = 4;
__global__ __launch_bounds__(512) void k3_gateup(const u16* __restrict__ hf, const u16* __restrict__ wg,
                                                 const u16* __restrict__ wu, u16* __restrict__ hhf,
                                                 const float* __restrict__ hst, const float* __restrict__ csgu) {
  __shared__ __attribute__((aligned(16))) char smem[K3_SLOT * K3_NSLOT];
  const int tid = threadIdx.x, lane = tid & 63;
  const int wave = __builtin_amdgcn_readfirstlane(tid >> 6);
  const int xcd = blockIdx.x & 7, idx = blockIdx.x >> 3;
  const int rt = (xcd >> 1) * 4 + (idx >> 3);
  const int cg = (xcd & 1) * 8 + (idx & 7);
  const int r = lane & 15, g = lane >> 4;
  const int f0 = cg < 11 ? cg * 96 : 1056 + (cg - 11) * 64;
  const int nvalid = cg < 11 ? 6 : 4;
  STAMP(3,0);
  if (wave >= 4) {
    const int lw = wave - 4;
    const char* abase = (const char*)(hf + (size_t)((rt * 8 + lw * 2) * 16) * 512);
    const long wdelta = (const char*)wu - (const char*)wg;
    const char* bsrc[6];
#pragma unroll
    for (int i = 0; i < 6; ++i) {
      const int line = (lw * 6 + i) * 4 + g;
      const int kq = line / 6, cgrp = line % 6;
      const int k = kq * 4 + ((lane >> 2) & 3);
      const int ch = (lane & 3) ^ (((k >> 3) & 1) << 1);
      int c3 = cgrp < 3 ? cgrp : cgrp - 3;
      c3 = (c3 * 2 < nvalid) ? c3 : 0;
      bsrc[i] = (const char*)(wg + f0) + (unsigned)(k * F_ + c3 * 32 + ch * 8) * 2u + (cgrp >= 3 ? wdelta : 0);
    }
    auto glds16v = [&](const char* gsrc, unsigned dst) {
      asm volatile("s_mov_b32 m0, %1\n\ts_nop 0\n\tglobal_load_lds_dwordx4 %0, off" :: "v"(gsrc), "s"(dst) : "memory");
    };
    auto stage = [&](int s) {
      const unsigned slot = lds_addr(smem) + (s % K3_NSLOT) * K3_SLOT;
#pragma unroll
      for (int i = 0; i < 4; ++i)
        glds16(abase + (i >> 1) * 16384 + (s * 2 + (i & 1)) * 1024, lane * 16, slot + (lw * 4 + i) * 1024);
#pragma unroll
      for (int i = 0; i < 6; ++i) glds16v(bsrc[i] + (size_t)s * 64 * F_ * 2, slot + 16384 + (lw * 6 + i) * 1024);
    };
    stage(0); stage(1);
    wait_vm<10>();
    BARE_BAR();
    stage(2); stage(3);
#pragma unroll
    for (int s = 0; s < 7; ++s) {
      if (s <= 4) wait_vm<20>(); else if (s == 5) wait_vm<10>(); else wait_vm<0>();
      BARE_BAR();
      if (s + 4 < 8) stage(s + 4);
    }
    return;
  }
  const int wr = wave >> 1, wc = wave & 1;
  const int q4 = (lane & 15) >> 2, p = lane & 3;
  const int aoff = wr * 8192 + lane * 16;
  const int bbase2 = 16384 + (2 * g) * 1536 + q4 * 64 + p * 8;
  int boff[3];
#pragma unroll
  for (int j = 0; j < 3; ++j) {
    const int fbl = 3 * wc + j;
    boff[j] = bbase2 + (fbl >> 1) * 256 + (((fbl & 1) ^ (g & 1)) << 5);
  }
  f32x4 hp[8];
  f32x4 cg4[3], cu4[3];
  f32x4 accg[4][3] = {}, accu[4][3] = {};
  u32x4 fa[2][4], fb[2][6];
  auto rdfrags = [&](int t) {
    const char* slot = smem + ((t >> 1) % K3_NSLOT) * K3_SLOT;
    const int kk = t & 1;
#pragma unroll
    for (int i = 0; i < 4; ++i) fa[kk][i] = *(const u32x4*)(slot + aoff + i * 2048 + kk * 1024);
#pragma unroll
    for (int j = 0; j < 3; ++j) {
      fb[kk][j] = tr_frag(slot + boff[j] + kk * 12288, slot + boff[j] + kk * 12288 + 1536);
      fb[kk][3 + j] = tr_frag(slot + boff[j] + 768 + kk * 12288, slot + boff[j] + 768 + kk * 12288 + 1536);
    }
  };
  auto compute = [&](int t) {
    const int kk = t & 1;
    const u32x4* a = fa[kk];
#pragma unroll
    for (int j = 0; j < 3; ++j) {
      const u32x4 bg = fb[kk][j], bu = fb[kk][3 + j];
#pragma unroll
      for (int i = 0; i < 4; ++i) {
        accg[i][j] = mfma16(bg, a[i], accg[i][j]);
        accu[i][j] = mfma16(bu, a[i], accu[i][j]);
      }
    }
  };
  BARE_BAR();
  STAMP(3,2);
  rdfrags(0);
#pragma unroll
  for (int s = 0; s < 8; ++s) {
    rdfrags(2 * s + 1);
    compute(2 * s);
    __builtin_amdgcn_sched_barrier(0);
    if (s + 1 < 8) {
      RAW_BAR();
      STAMP(3,3+s);
      rdfrags(2 * s + 2);
    }
    if (s == 6) {
#pragma unroll
      for (int i = 0; i < 8; ++i) hp[i] = *(const f32x4*)(hst + (size_t)(rt * 128 + wr * 64 + lane) * 32 + i * 4);
    }
    if (s == 7) {
#pragma unroll
      for (int j = 0; j < 3; ++j) {
        const int fbl = (3 * wc + j) < nvalid ? (3 * wc + j) : 0;
        cg4[j] = *(const f32x4*)(csgu + f0 + fbl * 16 + 4 * g);
        cu4[j] = *(const f32x4*)(csgu + F_ + f0 + fbl * 16 + 4 * g);
      }
    }
    compute(2 * s + 1);
    __builtin_amdgcn_sched_barrier(0);
  }
  STAMP(3,11);
  float mean_l, inv2_l;
  {
    f32x4 t4 = (hp[0] + hp[1]) + (hp[2] + hp[3]) + ((hp[4] + hp[5]) + (hp[6] + hp[7]));
    mean_l = (t4[0] + t4[2]) * (1.f / 512.f);
    const float var = fmaxf((t4[1] + t4[3]) * (1.f / 512.f) - mean_l * mean_l, 0.f);
    inv2_l = __builtin_amdgcn_rcpf(var + 1e-5f);
  }
#pragma unroll
  for (int i = 0; i < 4; ++i) {
    const float mean = __shfl(mean_l, i * 16 + r), inv2 = __shfl(inv2_l, i * 16 + r);
    const int mblk = rt * 8 + wr * 4 + i;
#pragma unroll
    for (int j = 0; j < 3; ++j) {
      const int fbl = 3 * wc + j;
      float hv[4];
#pragma unroll
      for (int e = 0; e < 4; ++e)
        hv[e] = ((accg[i][j][e] - mean * cg4[j][e]) * inv2) * (accu[i][j][e] - mean * cu4[j][e]);
      if (fbl < nvalid) {
        const int fb_ = (f0 >> 4) + fbl;
        u32x2 o = {pack_bf(hv[0], hv[1]), pack_bf(hv[2], hv[3])};
        *(u32x2*)(hhf + ((size_t)(mblk * KS4 + (fb_ >> 1)) * 64 + r + 16 * (2 * (fb_ & 1) + (g >> 1))) * 8 + (g & 1) * 4) = o;
      }
    }
  }
  STAMP(3,13);
}

__global__ __launch_bounds__(512) void k4_down(const u16* __restrict__ hf, const u16* __restrict__ hhf,
                                               const u16* __restrict__ wd, u16* __restrict__ out) {
  __shared__ __attribute__((aligned(16))) char smem[4 * 32768];
  const int tid = threadIdx.x, lane = tid & 63;
  const int wave = __builtin_amdgcn_readfirstlane(tid >> 6);
  const int xcd = blockIdx.x & 7, idx = blockIdx.x >> 3;
  const int rt = xcd * 4 + (idx >> 3);
  const int ct = idx & 7;
  const int r = lane & 15, g = lane >> 4;
  STAMP(4,0);
  if (wave >= 4) {
    const int lw = wave - 4;
    const char* abase = (const char*)(hhf + (size_t)((rt * 4 + lw) * KS4) * 512);
    const char* bbase = (const char*)(wd + (size_t)lw * 32 * D_ + ct * 64);
    const unsigned boffs = (unsigned)(((g >> 1) * 4 + ((lane >> 2) & 3)) * D_ + (g & 1) * 32 + (lane & 3) * 8) * 2u;
    auto stage = [&](int s) {
      const unsigned slot = lds_addr(smem) + (s & 3) * 32768;
#pragma unroll
      for (int i = 0; i < 4; ++i)
        glds16(abase + (s * 4 + ((s == 10 && i == 3) ? 2 : i)) * 1024, lane * 16, slot + lw * 4096 + i * 1024);
      const int so = (s == 10 && lw == 3) ? s - 1 : s;
#pragma unroll
      for (int i = 0; i < 4; ++i)
        glds16(bbase + (size_t)(so * 128 + i * 8) * D_ * 2, (i & 1) ? (boffs ^ 32u) : boffs, slot + 16384 + lw * 4096 + i * 1024);
    };
    stage(0); stage(1);
    wait_vm<8>();
    BARE_BAR();
    stage(2); stage(3);
#pragma unroll
    for (int s = 0; s < 10; ++s) {
      if (s <= 7) wait_vm<16>(); else if (s == 8) wait_vm<8>(); else wait_vm<0>();
      BARE_BAR();
      if (s + 4 < 11) stage(s + 4);
    }
    return;
  }
  const int wr = wave >> 1, wc = wave & 1;
  const int q4 = (lane & 15) >> 2, p = lane & 3;
  const int boff = 16384 + (2 * g) * 512 + wc * 256 + q4 * 64 + p * 8;
  const int aoff = wr * 8192 + lane * 16;
  u32x2 hr[2][2];
#pragma unroll
  for (int mb = 0; mb < 2; ++mb)
#pragma unroll
    for (int nb = 0; nb < 2; ++nb)
      hr[mb][nb] = *(const u32x2*)(hf + ((size_t)((rt * 4 + wr * 2 + mb) * 16 + ct * 2 + wc) * 64 + r + 16 * (2 * nb + (g >> 1))) * 8 + (g & 1) * 4);
  f32x4 acc[2][2] = {};
  u32x4 fa[2][2][2], fb[2][2][2];
  auto rdfrags = [&](int t) {
    const char* slot = smem + ((t >> 1) & 3) * 32768;
#pragma unroll
    for (int kk = 0; kk < 2; ++kk) {
      const int ksl = (t & 1) * 2 + kk;
#pragma unroll
      for (int mb = 0; mb < 2; ++mb) fa[t & 1][kk][mb] = *(const u32x4*)(slot + aoff + mb * 4096 + ksl * 1024);
#pragma unroll
      for (int nb = 0; nb < 2; ++nb) {
        const int ho = (nb ^ (g & 1)) << 5;
        fb[t & 1][kk][nb] = tr_frag(slot + boff + ksl * 4096 + ho, slot + boff + ksl * 4096 + 512 + ho);
      }
    }
  };
  auto compute = [&](int t) {
#pragma unroll
    for (int kk = 0; kk < 2; ++kk) {
      if (2 * t + kk < KS4) {
#pragma unroll
        for (int mb = 0; mb < 2; ++mb)
#pragma unroll
          for (int nb = 0; nb < 2; ++nb) acc[mb][nb] = mfma16(fb[t & 1][kk][nb], fa[t & 1][kk][mb], acc[mb][nb]);
      }
    }
  };
  BARE_BAR();
  STAMP(4,2);
  rdfrags(0);
#pragma unroll
  for (int s = 0; s < 11; ++s) {
    rdfrags(2 * s + 1);
    compute(2 * s);
    if (s + 1 < 11) {
      RAW_BAR();
      STAMP(4,3+s);
      rdfrags(2 * s + 2);
    }
    compute(2 * s + 1);
  }
#pragma unroll
  for (int mb = 0; mb < 2; ++mb)
#pragma unroll
    for (int nb = 0; nb < 2; ++nb) {
      const f32x4 o4 = acc[mb][nb];
      u32x2 o;
      o[0] = pack_bf(bf_lo(hr[mb][nb][0]) + o4[0], bf_hi(hr[mb][nb][0]) + o4[1]);
      o[1] = pack_bf(bf_lo(hr[mb][nb][1]) + o4[2], bf_hi(hr[mb][nb][1]) + o4[3]);
      *(u32x2*)(out + (size_t)(rt * 64 + wr * 32 + mb * 16 + r) * D_ + ct * 64 + wc * 32 + nb * 16 + g * 4) = o;
    }
  STAMP(4,14);
}

extern "C" void kernel_launch(void* const* d_in, const int* in_sizes, int n_in, void* d_out, int out_size,
                              void* d_ws, size_t ws_size, hipStream_t stream) {
  const u16* x = (const u16*)d_in[0];
  const u16* wqkv = (const u16*)d_in[1];
  const u16* wo = (const u16*)d_in[2];
  const u16* wg = (const u16*)d_in[4];
  const u16* wu = (const u16*)d_in[5];
  const u16* wd = (const u16*)d_in[6];
  u16* out = (u16*)d_out;
  char* ws = (char*)d_ws;
  u16* pq = (u16*)(ws);
  u16* hf = (u16*)(ws + (2u << 20));
  u16* wp = (u16*)(ws + (4u << 20));
  float* part = (float*)(ws + (5u << 20));
  u16* zb = (u16*)(ws + (6u << 20));
  float* csgu = (float*)(ws + (6u << 20) + 4096);
  float* csp = (float*)(ws + (6u << 20) + 65536);
  float* hst = (float*)(ws + (7u << 20));
  u16* hhf = (u16*)(ws + (8u << 20));
  k1_qkv<<<256, 512, 0, stream>>>(x, wqkv, pq, part, wg, wu, csp);
  k15_fold<<<256, 320, 0, stream>>>(part, wo, wp, zb, csp, csgu);
  k2_attn_out<<<256, 512, 0, stream>>>(x, pq, wp, zb, hf, hst);
  k3_gateup<<<256, 512, 0, stream>>>(hf, wg, wu, hhf, hst, csgu);
  k4_down<<<256, 512, 0, stream>>>(hf, hhf, wd, out);
}
```

```cpp
#include <hip/hip_runtime.h>
#include <hip/hip_bf16.h>
#include <stdint.h>

typedef unsigned short u16;
typedef __attribute__((ext_vector_type(8))) __bf16 bf16x8;
typedef __attribute__((ext_vector_type(2))) __bf16 bf16x2;
typedef __attribute__((ext_vector_type(4))) short s16x4;
typedef __attribute__((ext_vector_type(4))) float f32x4;
typedef __attribute__((ext_vector_type(2))) float f32x2;
typedef __attribute__((ext_vector_type(4))) unsigned u32x4;
typedef __attribute__((ext_vector_type(2))) unsigned u32x2;
typedef __attribute__((address_space(3))) s16x4* lds_s16x4_ptr;

#define DI __device__ __forceinline__
#define STAMP(kid,i)

constexpr int S_ = 2048, D_ = 512, F_ = 1376, H_ = 32;
constexpr int KS4 = F_ / 32;

DI float bf_lo(unsigned u) { return __uint_as_float(u << 16); }
DI float bf_hi(unsigned u) { return __uint_as_float(u & 0xffff0000u); }
DI unsigned pack_bf(float lo, float hi) {
  f32x2 f = {lo, hi};
  bf16x2 r = __builtin_convertvector(f, bf16x2);
  return __builtin_bit_cast(unsigned, r);
}
DI float bfr(float f) { return bf_lo(pack_bf(f, 0.f) & 0xffffu); }
DI f32x4 mfma16(u32x4 a, u32x4 b, f32x4 c) {
  return __builtin_amdgcn_mfma_f32_16x16x32_bf16(__builtin_bit_cast(bf16x8, a),
                                                 __builtin_bit_cast(bf16x8, b), c, 0, 0, 0);
}
DI float dot2(unsigned a, unsigned b, float c) {
  return __builtin_amdgcn_fdot2_f32_bf16(__builtin_bit_cast(bf16x2, a), __builtin_bit_cast(bf16x2, b), c, false);
}
DI u32x4 tr_frag(const char* lds0, const char* lds1) {
  s16x4 lo = __builtin_amdgcn_ds_read_tr16_b64_v4i16((lds_s16x4_ptr)lds0);
  s16x4 hi = __builtin_amdgcn_ds_read_tr16_b64_v4i16((lds_s16x4_ptr)lds1);
  u32x2 l2 = __builtin_bit_cast(u32x2, lo), h2 = __builtin_bit_cast(u32x2, hi);
  u32x4 r = {l2[0], l2[1], h2[0], h2[1]};
  return r;
}
DI unsigned lds_addr(char* l) { return (unsigned)(size_t)(__attribute__((address_space(3))) char*)l; }
DI void glds16(const void* sbase, unsigned voff, unsigned dst) {
  asm volatile("s_mov_b32 m0, %2\n\ts_nop 0\n\tglobal_load_lds_dwordx4 %0, %1"
               :: "v"(voff), "s"(sbase), "s"(dst) : "memory");
}
DI float red16(float s) {
  const unsigned u = __float_as_uint(s);
  auto r = __builtin_amdgcn_permlane16_swap(u, u, false, false);
  return __uint_as_float(r[0]) + __uint_as_float(r[1]);
}
DI float red32(float s) {
  const unsigned u = __float_as_uint(s);
  auto r = __builtin_amdgcn_permlane32_swap(u, u, false, false);
  return __uint_as_float(r[0]) + __uint_as_float(r[1]);
}
#define WAIT_VM(n) asm volatile("s_waitcnt vmcnt(" #n ")" ::: "memory")
#define RAW_BAR() do { asm volatile("s_waitcnt lgkmcnt(0)" ::: "memory"); __builtin_amdgcn_s_barrier(); } while (0)
#define BARE_BAR() do { asm volatile("" ::: "memory"); __builtin_amdgcn_s_barrier(); } while (0)
template <int N> DI void wait_vm() { asm volatile("s_waitcnt vmcnt(%0)" ::"i"(N) : "memory"); }

DI float phi_f(float t) { return (t > 0.f ? t + 1.f : __expf(t)) + 1e-6f; }
DI void sq_frag(const u32x4& a, float& q) {
#pragma unroll
  for (int i = 0; i < 4; ++i) q = dot2(a[i], a[i], q);
}
#define ONES_FRAG ((u32x4){0x3f803f80u, 0x3f803f80u, 0x3f803f80u, 0x3f803f80u})

constexpr int K1_SLOT = 16384 + 12288, K1_NSLOT = 4;
__global__ __launch_bounds__(512) void k1_qkv(const u16* __restrict__ x, const u16* __restrict__ wqkv,
                                              u16* __restrict__ pq, float* __restrict__ part,
                                              const u16* __restrict__ wg, const u16* __restrict__ wu,
                                              float* __restrict__ csp, unsigned* __restrict__ cnt) {
  __shared__ __attribute__((aligned(16))) char smem[K1_SLOT * K1_NSLOT];
  const int tid = threadIdx.x, lane = tid & 63;
  const int wave = __builtin_amdgcn_readfirstlane(tid >> 6);
  const int xcd = blockIdx.x & 7, idx = blockIdx.x >> 3;
  const int rt = (xcd >> 1) * 4 + (idx >> 3);
  const int cg = (xcd & 1) * 8 + (idx & 7);
  const int r = lane & 15, g = lane >> 4;
  STAMP(0,0);
  if (blockIdx.x == 0 && tid < 8) cnt[tid * 64] = 0u;
  if (wave >= 4) {
    const int lw = wave - 4;
    const unsigned lbase = lds_addr(smem);
    const unsigned aoffs = (unsigned)(lw * 32 + (lane >> 3)) * 1024u + (((lane & 7) ^ ((lane >> 4) & 3)) << 4);
    unsigned boffs[3];
#pragma unroll
    for (int i = 0; i < 3; ++i) {
      const int line = (lw * 3 + i) * 4 + g;
      const int kq = line / 3, cgrp = line % 3;
      const int k = kq * 4 + ((lane >> 2) & 3);
      const int ch = (lane & 3) ^ (((k >> 3) & 1) << 1);
      boffs[i] = (unsigned)(k * 1536 + cgrp * 512 + ch * 8) * 2u;
    }
    const char* abase = (const char*)(x + (size_t)rt * 128 * D_);
    const char* bbase = (const char*)(wqkv + cg * 32);
    auto stage = [&](int s) {
      const unsigned slot = lbase + (s % K1_NSLOT) * K1_SLOT;
#pragma unroll
      for (int i = 0; i < 4; ++i)
        glds16(abase + s * 128 + i * 8192, (i & 1) ? (aoffs ^ 64u) : aoffs, slot + lw * 4096 + i * 1024);
#pragma unroll
      for (int i = 0; i < 3; ++i)
        glds16(bbase + (size_t)s * 64 * 3072, boffs[i], slot + 16384 + (lw * 3 + i) * 1024);
    };
    stage(0); stage(1);
    wait_vm<7>();
    BARE_BAR();
    stage(2);
#pragma unroll
    for (int s = 0; s < 7; ++s) {
      if (s == 0 || s == 5) wait_vm<7>(); else if (s <= 4) wait_vm<14>(); else wait_vm<0>();
      BARE_BAR();
      if (s == 0) stage(3);
      if (s + 4 < 8) stage(s + 4);
    }
    const int W = blockIdx.x * 4 + lw;
    const int grp = W % 86, kp = (W / 86) & 7;
    u32x4 wv[4];
    {
      const u16* wsrc = (grp < 43 ? wg : wu) + (grp % 43) * 32 + (lane & 3) * 8 + (size_t)(kp * 64 + (lane >> 2)) * F_;
#pragma unroll
      for (int i = 0; i < 4; ++i) wv[i] = *(const u32x4*)(wsrc + (size_t)i * 16 * F_);
    }
    BARE_BAR();
    if (W < 86 * 8) {
      float c8[8];
#pragma unroll
      for (int j = 0; j < 4; ++j) {
        c8[2 * j] = (bf_lo(wv[0][j]) + bf_lo(wv[1][j])) + (bf_lo(wv[2][j]) + bf_lo(wv[3][j]));
        c8[2 * j + 1] = (bf_hi(wv[0][j]) + bf_hi(wv[1][j])) + (bf_hi(wv[2][j]) + bf_hi(wv[3][j]));
      }
#pragma unroll
      for (int j = 0; j < 8; ++j) {
        float v = c8[j];
        v += __shfl_xor(v, 4); v += __shfl_xor(v, 8);
        c8[j] = red32(red16(v));
      }
      if (lane < 4) {
        float* dst = csp + kp * 2752 + grp * 32 + lane * 8;
        f32x4 v0 = {c8[0], c8[1], c8[2], c8[3]}, v1 = {c8[4], c8[5], c8[6], c8[7]};
        *(f32x4*)dst = v0; *(f32x4*)(dst + 4) = v1;
      }
    }
    BARE_BAR();
    return;
  }
  const int wr = wave >> 1, wc = wave & 1;
  const int q4 = (lane & 15) >> 2, p = lane & 3;
  const int aoff = (wr * 64 + r) * 128 + ((g ^ ((r >> 1) & 7)) << 4);
  const int aoff0 = aoff + wc * 4096, aoff1 = aoff + (wc ^ 1) * 4096;
  const int boff = 16384 + (2 * g) * 768 + q4 * 64 + ((wc ^ (g & 1)) << 5) + p * 8;
  const u32x4 ones = ONES_FRAG;
  f32x4 accq[4] = {}, acck[4] = {}, accv[4] = {};
  f32x4 rs[2] = {}, sqm[2] = {}, cq = {}, ck = {}, cv = {};
  u32x4 fa[2][4], fb[2][3];
  auto rdfrags = [&](int t) {
    const char* slot = smem + ((t >> 1) % K1_NSLOT) * K1_SLOT;
    const int kk = t & 1;
#pragma unroll
    for (int i = 0; i < 4; ++i)
      fa[kk][i] = *(const u32x4*)(slot + (((i < 2 ? aoff0 : aoff1) + (i & 1) * 2048) ^ (kk << 6)));
#pragma unroll
    for (int j = 0; j < 3; ++j)
      fb[kk][j] = tr_frag(slot + boff + kk * 6144 + j * 256, slot + boff + kk * 6144 + 768 + j * 256);
  };
  auto compute = [&](int t) {
    const int kk = t & 1;
    const u32x4* a = fa[kk];
    const u32x4 bq = fb[kk][0], bk = fb[kk][1], bv = fb[kk][2];
#pragma unroll
    for (int i = 0; i < 4; ++i) {
      accq[i] = mfma16(bq, a[i], accq[i]);
      acck[i] = mfma16(a[i], bk, acck[i]);
      accv[i] = mfma16(a[i], bv, accv[i]);
    }
    rs[0] = mfma16(ones, a[0], rs[0]);
    rs[1] = mfma16(ones, a[1], rs[1]);
    sqm[0] = mfma16(a[0], a[0], sqm[0]);
    sqm[1] = mfma16(a[1], a[1], sqm[1]);
    if (kk == wr) {
      cq = mfma16(bq, ones, cq);
      ck = mfma16(ones, bk, ck);
      cv = mfma16(ones, bv, cv);
    }
  };
  BARE_BAR();
  STAMP(0,2);
  rdfrags(0);
#pragma unroll
  for (int s = 0; s < 8; ++s) {
    rdfrags(2 * s + 1);
    compute(2 * s);
    __builtin_amdgcn_sched_barrier(0);
    if (s + 1 < 8) {
      RAW_BAR();
      STAMP(0,3+s);
      rdfrags(2 * s + 2);
    }
    compute(2 * s + 1);
    __builtin_amdgcn_sched_barrier(0);
  }
  STAMP(0,11);
  float* st = (float*)(smem);
  float* csl = (float*)(smem + 2048);
  float* st2 = (float*)(smem + 4096);
  float* red = (float*)(smem + 8192);
  {
#pragma unroll
    for (int i = 0; i < 2; ++i) {
      const float q01 = (r & 1) ? sqm[i][1] : sqm[i][0], q23 = (r & 1) ? sqm[i][3] : sqm[i][2];
      const float q = (r & 2) ? q23 : q01;
      if (g == (r >> 2)) {
        f32x2 v = {rs[i][0], q};
        *(f32x2*)(st + (wr * 64 + wc * 32 + i * 16 + r) * 2) = v;
      }
    }
    float* c = csl + (wr * 2 + wc) * 48;
    if (r == 0) *(f32x4*)(c + 4 * g) = cq;
    if (g == 0) { c[16 + r] = ck[0]; c[32 + r] = cv[0]; }
  }
  __syncthreads();
  STAMP(0,12);
  {
    const f32x2 a0 = *(const f32x2*)(st + (wr * 64 + lane) * 2);
    const float mean = a0[0] * (1.f / 512.f);
    const float var = fmaxf(a0[1] * (1.f / 512.f) - mean * mean, 0.f);
    f32x2 v = {mean, __builtin_amdgcn_rsqf(var + 1e-5f)};
    *(f32x2*)(st2 + (wave * 64 + lane) * 2) = v;
  }
  const f32x4 csq4 = *(const f32x4*)(csl + wc * 48 + 4 * g) + *(const f32x4*)(csl + (2 + wc) * 48 + 4 * g);
  const float csk = csl[wc * 48 + 16 + r] + csl[(2 + wc) * 48 + 16 + r];
  const float csv = csl[wc * 48 + 32 + r] + csl[(2 + wc) * 48 + 32 + r];
  f32x4 kv = {}, zz = {};
#pragma unroll
  for (int ih = 0; ih < 2; ++ih) {
    u32x4 pk, vv;
#pragma unroll
    for (int i2 = 0; i2 < 2; ++i2) {
      const int i = ih * 2 + i2;
      const int mbl = ((ih ^ wc) << 1) + i2;
      {
        const f32x2 mi = *(const f32x2*)(st2 + (wave * 64 + mbl * 16 + r) * 2);
        float qv[4];
#pragma unroll
        for (int j = 0; j < 4; ++j) qv[j] = phi_f((accq[i][j] - mi[0] * csq4[j]) * mi[1]);
        u32x2 o = {pack_bf(qv[0], qv[1]), pack_bf(qv[2], qv[3])};
        const int mblk = rt * 8 + wr * 4 + mbl;
        *(u32x2*)(pq + ((size_t)(mblk * 16 + cg) * 64 + r + 16 * (2 * wc + (g >> 1))) * 8 + (g & 1) * 4) = o;
      }
      {
        const f32x4 m0 = *(const f32x4*)(st2 + (wave * 64 + mbl * 16 + 4 * g) * 2);
        const f32x4 m1 = *(const f32x4*)(st2 + (wave * 64 + mbl * 16 + 4 * g) * 2 + 4);
        const float mean[4] = {m0[0], m0[2], m1[0], m1[2]}, inv[4] = {m0[1], m0[3], m1[1], m1[3]};
        float kk[4], vj[4];
#pragma unroll
        for (int j = 0; j < 4; ++j) {
          kk[j] = phi_f((acck[i][j] - mean[j] * csk) * inv[j]);
          vj[j] = (accv[i][j] - mean[j] * csv) * inv[j];
        }
        pk[2 * i2] = pack_bf(kk[0], kk[1]);
        pk[2 * i2 + 1] = pack_bf(kk[2], kk[3]);
        vv[2 * i2] = pack_bf(vj[0], vj[1]);
        vv[2 * i2 + 1] = pack_bf(vj[2], vj[3]);
      }
    }
    kv = mfma16(pk, vv, kv);
    zz = mfma16(pk, ones, zz);
  }
  {
    float* rd = red + (wc * 2 + wr) * 272;
#pragma unroll
    for (int j = 0; j < 4; ++j) rd[(4 * g + j) * 16 + r] = kv[j];
    if (r == 0) *(f32x4*)(rd + 256 + 4 * g) = zz;
  }
  __syncthreads();
  for (int i = tid; i < 544; i += 256) {
    const int hh = i >= 272 ? 1 : 0, e = i - hh * 272;
    const float* rr = red + hh * 2 * 272 + e;
    part[(size_t)(rt * 32 + cg * 2 + hh) * 272 + e] = rr[0] + rr[272];
  }
  STAMP(0,13);
}

typedef __attribute__((address_space(1))) unsigned gu32;
__global__ __launch_bounds__(512) void k2_attn_out(const u16* __restrict__ x, const u16* __restrict__ pq,
                                                   const float* __restrict__ part, const u16* __restrict__ wo,
                                                   u16* wp, unsigned* cnt,
                                                   u16* __restrict__ hf, float* __restrict__ hst,
                                                   const float* __restrict__ csp, float* __restrict__ csgu) {
  __shared__ __attribute__((aligned(16))) char smem[65536 + 16384 + 1088];
  const int tid = threadIdx.x, lane = tid & 63;
  const int wave = __builtin_amdgcn_readfirstlane(tid >> 6);
  const int xcd = blockIdx.x & 7, idx = blockIdx.x >> 3;
  const int rt = xcd * 4 + (idx >> 3);
  const int ct = idx & 7;
  const int kg = wave >> 2, rb = wave & 3;
  const int r = lane & 15, g = lane >> 4;
  const int mblk = rt * 4 + rb;
  const __amdgpu_buffer_rsrc_t wrs = __builtin_amdgcn_make_buffer_rsrc(wp, 0, 524288 + 8192, 0x00020000);
  STAMP(2,0);
  const int ph = blockIdx.x >> 3, pc = blockIdx.x & 7;
  float* kv = (float*)(smem + 65536 + 16384);
  float pv[16];
  if (tid < 272) {
#pragma unroll
    for (int p = 0; p < 16; ++p) pv[p] = part[(size_t)(p * 32 + ph) * 272 + tid];
  }
  float w[16];
  if (tid < 128) {
#pragma unroll
    for (int e = 0; e < 16; ++e) w[e] = bf_lo(wo[(size_t)(ph * 16 + e) * D_ + pc * 64 + (tid & 63)]);
  }
  float cpart[8];
  const int ccol = blockIdx.x * 11 + (tid - 272);
  const bool do_cs = tid >= 272 && tid < 283 && ccol < 2752;
  if (do_cs) {
#pragma unroll
    for (int kp = 0; kp < 8; ++kp) cpart[kp] = csp[kp * 2752 + ccol];
  }
  u32x4 a[8];
#pragma unroll
  for (int i = 0; i < 8; ++i) a[i] = *(const u32x4*)(pq + ((size_t)(mblk * 16 + kg * 8 + i) * 64 + lane) * 8);
  u32x2 xr[2];
#pragma unroll
  for (int j = 0; j < 2; ++j)
    xr[j] = *(const u32x2*)(x + (size_t)(mblk * 16 + r) * D_ + ct * 64 + (kg * 2 + j) * 16 + g * 4);
  if (tid < 272) {
    float s = 0.f;
#pragma unroll
    for (int p = 0; p < 16; ++p) s += pv[p];
    kv[tid] = s;
  }
  if (do_cs) csgu[ccol] = ((cpart[0] + cpart[1]) + (cpart[2] + cpart[3])) + ((cpart[4] + cpart[5]) + (cpart[6] + cpart[7]));
  __syncthreads();
  STAMP(2,1);
  if (tid < 128) {
    const int c = tid & 63, dh = tid >> 6;
    float o[8];
#pragma unroll
    for (int j = 0; j < 8; ++j) {
      float s = 0.f;
#pragma unroll
      for (int e4 = 0; e4 < 4; ++e4) {
        const f32x4 k4 = *(const f32x4*)(kv + (dh * 8 + j) * 16 + e4 * 4);
        s = fmaf(k4[0], w[e4 * 4], s); s = fmaf(k4[1], w[e4 * 4 + 1], s);
        s = fmaf(k4[2], w[e4 * 4 + 2], s); s = fmaf(k4[3], w[e4 * 4 + 3], s);
      }
      o[j] = s;
    }
    const int n = pc * 64 + c, nb = n >> 4, cc = n & 15;
    const int ks = ph >> 1, gg = (ph & 1) * 2 + dh;
    u32x4 v = {pack_bf(o[0], o[1]), pack_bf(o[2], o[3]), pack_bf(o[4], o[5]), pack_bf(o[6], o[7])};
    __builtin_amdgcn_raw_buffer_store_b128(v, wrs, (unsigned)((nb * 16 + ks) * 64 + gg * 16 + cc) * 16u, 0, 16);
  } else if (tid < 130) {
    const float* zs = kv + 256 + (tid - 128) * 8;
    u32x4 v = {pack_bf(zs[0], zs[1]), pack_bf(zs[2], zs[3]), pack_bf(zs[4], zs[5]), pack_bf(zs[6], zs[7])};
    __builtin_amdgcn_raw_buffer_store_b128(v, wrs, 524288u + (unsigned)(pc * 512 + ph * 16 + (tid - 128) * 8) * 2u, 0, 16);
  }
  WAIT_VM(0);
  __syncthreads();
  if (tid == 0) __hip_atomic_fetch_add((gu32*)(cnt + pc * 64), 1u, __ATOMIC_RELAXED, __HIP_MEMORY_SCOPE_AGENT);
  STAMP(2,2);
  if (wave == 0) {
    for (unsigned spins = 0; spins < (1u << 22); ++spins) {
      const unsigned v = __builtin_amdgcn_readfirstlane(
          __hip_atomic_load((gu32*)(cnt + ct * 64), __ATOMIC_RELAXED, __HIP_MEMORY_SCOPE_AGENT));
      if (v >= 32u) break;
      __builtin_amdgcn_s_sleep(1);
    }
  }
  __syncthreads();
  STAMP(2,3);
  u32x4 zf[8], wv[8];
#pragma unroll
  for (int i = 0; i < 8; ++i)
    zf[i] = __builtin_amdgcn_raw_buffer_load_b128(wrs, 524288u + (unsigned)(ct * 512 + (kg * 8 + i) * 32 + g * 8) * 2u, 0, 16);
#pragma unroll
  for (int i = 0; i < 8; ++i)
    wv[i] = __builtin_amdgcn_raw_buffer_load_b128(wrs, (unsigned)(ct * 65536 + (wave * 8 + i) * 1024 + lane * 16), 0, 16);
#pragma unroll
  for (int i = 0; i < 8; ++i) {
    float s = dot2(a[i][0], zf[i][0], 0.f);
    s = dot2(a[i][1], zf[i][1], s);
    s = dot2(a[i][2], zf[i][2], s);
    s = dot2(a[i][3], zf[i][3], s);
    const float inv = __builtin_amdgcn_rcpf(red16(s) + 1e-6f);
#pragma unroll
    for (int j = 0; j < 4; ++j) a[i][j] = pack_bf(bf_lo(a[i][j]) * inv, bf_hi(a[i][j]) * inv);
  }
#pragma unroll
  for (int i = 0; i < 8; ++i) *(u32x4*)(smem + (wave * 8 + i) * 1024 + lane * 16) = wv[i];
  __syncthreads();
  STAMP(2,4);
  f32x4 acc[4] = {};
#pragma unroll
  for (int i = 0; i < 8; ++i) {
#pragma unroll
    for (int nb = 0; nb < 4; ++nb) {
      const u32x4 b = *(const u32x4*)(smem + (nb * 16 + kg * 8 + i) * 1024 + lane * 16);
      acc[nb] = mfma16(b, a[i], acc[nb]);
    }
  }
  STAMP(2,5);
  f32x4* xch = (f32x4*)(smem + 65536);
#pragma unroll
  for (int j = 0; j < 2; ++j) xch[(wave * 2 + j) * 64 + lane] = kg ? acc[j] : acc[2 + j];
  __syncthreads();
  float hs = 0.f, hq = 0.f;
#pragma unroll
  for (int j = 0; j < 2; ++j) {
    const f32x4 o4 = (kg ? acc[2 + j] : acc[j]) + xch[((wave ^ 4) * 2 + j) * 64 + lane];
    u32x2 o;
    o[0] = pack_bf(bf_lo(xr[j][0]) + o4[0], bf_hi(xr[j][0]) + o4[1]);
    o[1] = pack_bf(bf_lo(xr[j][1]) + o4[2], bf_hi(xr[j][1]) + o4[3]);
    *(u32x2*)(hf + ((size_t)(mblk * 16 + ct * 2 + kg) * 64 + r + 16 * (2 * j + (g >> 1))) * 8 + (g & 1) * 4) = o;
    hs = dot2(o[0], 0x3f803f80u, hs); hs = dot2(o[1], 0x3f803f80u, hs);
    hq = dot2(o[0], o[0], hq); hq = dot2(o[1], o[1], hq);
  }
  hs = red32(red16(hs)); hq = red32(red16(hq));
  if (g == 0) {
    f32x2 v = {hs, hq};
    *(f32x2*)(hst + ((size_t)(mblk * 16 + r) * 16 + ct * 2 + kg) * 2) = v;
  }
  STAMP(2,6);
}

constexpr int K3_SLOT = 16384 + 24576, K3_NSLOT = 4;
__global__ __launch_bounds__(512) void k3_gateup(const u16* __restrict__ hf, const u16* __restrict__ wg,
                                                 const u16* __restrict__ wu, u16* __restrict__ hhf,
                                                 const float* __restrict__ hst, const float* __restrict__ csgu) {
  __shared__ __attribute__((aligned(16))) char smem[K3_SLOT * K3_NSLOT];
  const int tid = threadIdx.x, lane = tid & 63;
  const int wave = __builtin_amdgcn_readfirstlane(tid >> 6);
  const int xcd = blockIdx.x & 7, idx = blockIdx.x >> 3;
  const int rt = (xcd >> 1) * 4 + (idx >> 3);
  const int cg = (xcd & 1) * 8 + (idx & 7);
  const int r = lane & 15, g = lane >> 4;
  const int f0 = cg < 11 ? cg * 96 : 1056 + (cg - 11) * 64;
  const int nvalid = cg < 11 ? 6 : 4;
  STAMP(3,0);
  if (wave >= 4) {
    const int lw = wave - 4;
    const char* abase = (const char*)(hf + (size_t)((rt * 8 + lw * 2) * 16) * 512);
    const long wdelta = (const char*)wu - (const char*)wg;
    const char* bsrc[6];
#pragma unroll
    for (int i = 0; i < 6; ++i) {
      const int line = (lw * 6 + i) * 4 + g;
      const int kq = line / 6, cgrp = line % 6;
      const int k = kq * 4 + ((lane >> 2) & 3);
      const int ch = (lane & 3) ^ (((k >> 3) & 1) << 1);
      int c3 = cgrp < 3 ? cgrp : cgrp - 3;
      c3 = (c3 * 2 < nvalid) ? c3 : 0;
      bsrc[i] = (const char*)(wg + f0) + (unsigned)(k * F_ + c3 * 32 + ch * 8) * 2u + (cgrp >= 3 ? wdelta : 0);
    }
    auto glds16v = [&](const char* gsrc, unsigned dst) {
      asm volatile("s_mov_b32 m0, %1\n\ts_nop 0\n\tglobal_load_lds_dwordx4 %0, off" :: "v"(gsrc), "s"(dst) : "memory");
    };
    auto stage = [&](int s) {
      const unsigned slot = lds_addr(smem) + (s % K3_NSLOT) * K3_SLOT;
#pragma unroll
      for (int i = 0; i < 4; ++i)
        glds16(abase + (i >> 1) * 16384 + (s * 2 + (i & 1)) * 1024, lane * 16, slot + (lw * 4 + i) * 1024);
#pragma unroll
      for (int i = 0; i < 6; ++i) glds16v(bsrc[i] + (size_t)s * 64 * F_ * 2, slot + 16384 + (lw * 6 + i) * 1024);
    };
    stage(0); stage(1);
    wait_vm<10>();
    BARE_BAR();
    stage(2);
#pragma unroll
    for (int s = 0; s < 7; ++s) {
      if (s == 0 || s == 5) wait_vm<10>(); else if (s <= 4) wait_vm<20>(); else wait_vm<0>();
      BARE_BAR();
      if (s == 0) stage(3);
      if (s + 4 < 8) stage(s + 4);
    }
    return;
  }
  const int wr = wave >> 1, wc = wave & 1;
  const int q4 = (lane & 15) >> 2, p = lane & 3;
  const int aoff = wr * 8192 + lane * 16;
  const int bbase2 = 16384 + (2 * g) * 1536 + q4 * 64 + p * 8;
  int boff[3];
#pragma unroll
  for (int j = 0; j < 3; ++j) {
    const int fbl = 3 * wc + j;
    boff[j] = bbase2 + (fbl >> 1) * 256 + (((fbl & 1) ^ (g & 1)) << 5);
  }
  f32x4 hp[8];
  f32x4 cg4[3], cu4[3];
  float mean_l = 0.f, inv2_l = 0.f;
  f32x4 accg[4][3] = {}, accu[4][3] = {};
  u32x4 fa[2][4], fb[2][6];
  auto rdfrags = [&](int t) {
    const char* slot = smem + ((t >> 1) % K3_NSLOT) * K3_SLOT;
    const int kk = t & 1;
#pragma unroll
    for (int i = 0; i < 4; ++i) fa[kk][i] = *(const u32x4*)(slot + aoff + i * 2048 + kk * 1024);
#pragma unroll
    for (int j = 0; j < 3; ++j) {
      fb[kk][j] = tr_frag(slot + boff[j] + kk * 12288, slot + boff[j] + kk * 12288 + 1536);
      fb[kk][3 + j] = tr_frag(slot + boff[j] + 768 + kk * 12288, slot + boff[j] + 768 + kk * 12288 + 1536);
    }
  };
  auto compute = [&](int t) {
    const int kk = t & 1;
    const u32x4* a = fa[kk];
#pragma unroll
    for (int j = 0; j < 3; ++j) {
      const u32x4 bg = fb[kk][j], bu = fb[kk][3 + j];
#pragma unroll
      for (int i = 0; i < 4; ++i) {
        accg[i][j] = mfma16(bg, a[i], accg[i][j]);
        accu[i][j] = mfma16(bu, a[i], accu[i][j]);
      }
    }
  };
  BARE_BAR();
  STAMP(3,2);
  rdfrags(0);
#pragma unroll
  for (int s = 0; s < 8; ++s) {
    rdfrags(2 * s + 1);
    compute(2 * s);
    __builtin_amdgcn_sched_barrier(0);
    if (s + 1 < 8) {
      RAW_BAR();
      STAMP(3,3+s);
      rdfrags(2 * s + 2);
    }
    if (s == 4) {
#pragma unroll
      for (int i = 0; i < 8; ++i) hp[i] = *(const f32x4*)(hst + (size_t)(rt * 128 + wr * 64 + lane) * 32 + i * 4);
    }
    if (s == 6) {
      f32x4 t4 = (hp[0] + hp[1]) + (hp[2] + hp[3]) + ((hp[4] + hp[5]) + (hp[6] + hp[7]));
      mean_l = (t4[0] + t4[2]) * (1.f / 512.f);
      const float var = fmaxf((t4[1] + t4[3]) * (1.f / 512.f) - mean_l * mean_l, 0.f);
      inv2_l = __builtin_amdgcn_rcpf(var + 1e-5f);
      asm volatile("" : "+v"(mean_l), "+v"(inv2_l));
#pragma unroll
      for (int j = 0; j < 3; ++j) {
        const int fbl = (3 * wc + j) < nvalid ? (3 * wc + j) : 0;
        cg4[j] = *(const f32x4*)(csgu + f0 + fbl * 16 + 4 * g);
        cu4[j] = *(const f32x4*)(csgu + F_ + f0 + fbl * 16 + 4 * g);
      }
    }
    compute(2 * s + 1);
    __builtin_amdgcn_sched_barrier(0);
  }
  STAMP(3,11);
#pragma unroll
  for (int i = 0; i < 4; ++i) {
    const float mean = __shfl(mean_l, i * 16 + r), inv2 = __shfl(inv2_l, i * 16 + r);
    const int mblk = rt * 8 + wr * 4 + i;
#pragma unroll
    for (int j = 0; j < 3; ++j) {
      const int fbl = 3 * wc + j;
      float hv[4];
#pragma unroll
      for (int e = 0; e < 4; ++e)
        hv[e] = ((accg[i][j][e] - mean * cg4[j][e]) * inv2) * (accu[i][j][e] - mean * cu4[j][e]);
      if (fbl < nvalid) {
        const int fb_ = (f0 >> 4) + fbl;
        u32x2 o = {pack_bf(hv[0], hv[1]), pack_bf(hv[2], hv[3])};
        *(u32x2*)(hhf + ((size_t)(mblk * KS4 + (fb_ >> 1)) * 64 + r + 16 * (2 * (fb_ & 1) + (g >> 1))) * 8 + (g & 1) * 4) = o;
      }
    }
  }
  STAMP(3,13);
}

__global__ __launch_bounds__(512) void k4_down(const u16* __restrict__ hf, const u16* __restrict__ hhf,
                                               const u16* __restrict__ wd, u16* __restrict__ out) {
  __shared__ __attribute__((aligned(16))) char smem[4 * 32768];
  const int tid = threadIdx.x, lane = tid & 63;
  const int wave = __builtin_amdgcn_readfirstlane(tid >> 6);
  const int xcd = blockIdx.x & 7, idx = blockIdx.x >> 3;
  const int rt = xcd * 4 + (idx >> 3);
  const int ct = idx & 7;
  const int r = lane & 15, g = lane >> 4;
  STAMP(4,0);
  if (wave >= 4) {
    const int lw = wave - 4;
    const char* abase = (const char*)(hhf + (size_t)((rt * 4 + lw) * KS4) * 512);
    const char* bbase = (const char*)(wd + (size_t)lw * 32 * D_ + ct * 64);
    const unsigned boffs = (unsigned)(((g >> 1) * 4 + ((lane >> 2) & 3)) * D_ + (g & 1) * 32 + (lane & 3) * 8) * 2u;
    auto stage = [&](int s) {
      const unsigned slot = lds_addr(smem) + (s & 3) * 32768;
#pragma unroll
      for (int i = 0; i < 4; ++i)
        glds16(abase + (s * 4 + ((s == 10 && i == 3) ? 2 : i)) * 1024, lane * 16, slot + lw * 4096 + i * 1024);
      const int so = (s == 10 && lw == 3) ? s - 1 : s;
#pragma unroll
      for (int i = 0; i < 4; ++i)
        glds16(bbase + (size_t)(so * 128 + i * 8) * D_ * 2, (i & 1) ? (boffs ^ 32u) : boffs, slot + 16384 + lw * 4096 + i * 1024);
    };
    stage(0); stage(1);
    wait_vm<8>();
    BARE_BAR();
    stage(2);
#pragma unroll
    for (int s = 0; s < 10; ++s) {
      if (s == 0 || s == 8) wait_vm<8>(); else if (s <= 7) wait_vm<16>(); else wait_vm<0>();
      BARE_BAR();
      if (s == 0) stage(3);
      if (s + 4 < 11) stage(s + 4);
    }
    return;
  }
  const int wr = wave >> 1, wc = wave & 1;
  const int q4 = (lane & 15) >> 2, p = lane & 3;
  const int boff = 16384 + (2 * g) * 512 + wc * 256 + q4 * 64 + p * 8;
  const int aoff = wr * 8192 + lane * 16;
  u32x2 hr[2][2];
#pragma unroll
  for (int mb = 0; mb < 2; ++mb)
#pragma unroll
    for (int nb = 0; nb < 2; ++nb)
      hr[mb][nb] = *(const u32x2*)(hf + ((size_t)((rt * 4 + wr * 2 + mb) * 16 + ct * 2 + wc) * 64 + r + 16 * (2 * nb + (g >> 1))) * 8 + (g & 1) * 4);
  f32x4 acc[2][2] = {};
  u32x4 fa[2][2][2], fb[2][2][2];
  auto rdfrags = [&](int t) {
    const char* slot = smem + ((t >> 1) & 3) * 32768;
#pragma unroll
    for (int kk = 0; kk < 2; ++kk) {
      const int ksl = (t & 1) * 2 + kk;
#pragma unroll
      for (int mb = 0; mb < 2; ++mb) fa[t & 1][kk][mb] = *(const u32x4*)(slot + aoff + mb * 4096 + ksl * 1024);
#pragma unroll
      for (int nb = 0; nb < 2; ++nb) {
        const int ho = (nb ^ (g & 1)) << 5;
        fb[t & 1][kk][nb] = tr_frag(slot + boff + ksl * 4096 + ho, slot + boff + ksl * 4096 + 512 + ho);
      }
    }
  };
  auto compute = [&](int t) {
#pragma unroll
    for (int kk = 0; kk < 2; ++kk) {
      if (2 * t + kk < KS4) {
#pragma unroll
        for (int mb = 0; mb < 2; ++mb)
#pragma unroll
          for (int nb = 0; nb < 2; ++nb) acc[mb][nb] = mfma16(fb[t & 1][kk][nb], fa[t & 1][kk][mb], acc[mb][nb]);
      }
    }
  };
  BARE_BAR();
  STAMP(4,2);
  rdfrags(0);
#pragma unroll
  for (int s = 0; s < 11; ++s) {
    rdfrags(2 * s + 1);
    compute(2 * s);
    if (s + 1 < 11) {
      RAW_BAR();
      STAMP(4,3+s);
      rdfrags(2 * s + 2);
    }
    compute(2 * s + 1);
  }
#pragma unroll
  for (int mb = 0; mb < 2; ++mb)
#pragma unroll
    for (int nb = 0; nb < 2; ++nb) {
      const f32x4 o4 = acc[mb][nb];
      u32x2 o;
      o[0] = pack_bf(bf_lo(hr[mb][nb][0]) + o4[0], bf_hi(hr[mb][nb][0]) + o4[1]);
      o[1] = pack_bf(bf_lo(hr[mb][nb][1]) + o4[2], bf_hi(hr[mb][nb][1]) + o4[3]);
      *(u32x2*)(out + (size_t)(rt * 64 + wr * 32 + mb * 16 + r) * D_ + ct * 64 + wc * 32 + nb * 16 + g * 4) = o;
    }
  STAMP(4,14);
}

extern "C" void kernel_launch(void* const* d_in, const int* in_sizes, int n_in, void* d_out, int out_size,
                              void* d_ws, size_t ws_size, hipStream_t stream) {
  const u16* x = (const u16*)d_in[0];
  const u16* wqkv = (const u16*)d_in[1];
  const u16* wo = (const u16*)d_in[2];
  const u16* wg = (const u16*)d_in[4];
  const u16* wu = (const u16*)d_in[5];
  const u16* wd = (const u16*)d_in[6];
  u16* out = (u16*)d_out;
  char* ws = (char*)d_ws;
  u16* pq = (u16*)(ws);
  u16* hf = (u16*)(ws + (2u << 20));
  u16* wp = (u16*)(ws + (4u << 20));
  float* part = (float*)(ws + (5u << 20));
  unsigned* cnt = (unsigned*)(ws + (6u << 20));
  float* csgu = (float*)(ws + (6u << 20) + 4096);
  float* csp = (float*)(ws + (6u << 20) + 65536);
  float* hst = (float*)(ws + (7u << 20));
  u16* hhf = (u16*)(ws + (8u << 20));
  k1_qkv<<<256, 512, 0, stream>>>(x, wqkv, pq, part, wg, wu, csp, cnt);
  k2_attn_out<<<256, 512, 0, stream>>>(x, pq, part, wo, wp, cnt, hf, hst, csp, csgu);
  k3_gateup<<<256, 512, 0, stream>>>(hf, wg, wu, hhf, hst, csgu);
  k4_down<<<256, 512, 0, stream>>>(hf, hhf, wd, out);
}
```

```cpp
#include <hip/hip_runtime.h>
#include <hip/hip_bf16.h>
#include <stdint.h>

typedef unsigned short u16;
typedef __attribute__((ext_vector_type(8))) __bf16 bf16x8;
typedef __attribute__((ext_vector_type(2))) __bf16 bf16x2;
typedef __attribute__((ext_vector_type(4))) short s16x4;
typedef __attribute__((ext_vector_type(4))) float f32x4;
typedef __attribute__((ext_vector_type(2))) float f32x2;
typedef __attribute__((ext_vector_type(4))) unsigned u32x4;
typedef __attribute__((ext_vector_type(2))) unsigned u32x2;
typedef __attribute__((address_space(3))) s16x4* lds_s16x4_ptr;

#define DI __device__ __forceinline__
#define STAMP(kid,i)

constexpr int S_ = 2048, D_ = 512, F_ = 1376, H_ = 32;
constexpr int KS4 = F_ / 32;

DI float bf_lo(unsigned u) { return __uint_as_float(u << 16); }
DI float bf_hi(unsigned u) { return __uint_as_float(u & 0xffff0000u); }
DI unsigned pack_bf(float lo, float hi) {
  f32x2 f = {lo, hi};
  bf16x2 r = __builtin_convertvector(f, bf16x2);
  return __builtin_bit_cast(unsigned, r);
}
DI float bfr(float f) { return bf_lo(pack_bf(f, 0.f) & 0xffffu); }
DI f32x4 mfma16(u32x4 a, u32x4 b, f32x4 c) {
  return __builtin_amdgcn_mfma_f32_16x16x32_bf16(__builtin_bit_cast(bf16x8, a),
                                                 __builtin_bit_cast(bf16x8, b), c, 0, 0, 0);
}
DI float dot2(unsigned a, unsigned b, float c) {
  return __builtin_amdgcn_fdot2_f32_bf16(__builtin_bit_cast(bf16x2, a), __builtin_bit_cast(bf16x2, b), c, false);
}
DI u32x4 tr_frag(const char* lds0, const char* lds1) {
  s16x4 lo = __builtin_amdgcn_ds_read_tr16_b64_v4i16((lds_s16x4_ptr)lds0);
  s16x4 hi = __builtin_amdgcn_ds_read_tr16_b64_v4i16((lds_s16x4_ptr)lds1);
  u32x2 l2 = __builtin_bit_cast(u32x2, lo), h2 = __builtin_bit_cast(u32x2, hi);
  u32x4 r = {l2[0], l2[1], h2[0], h2[1]};
  return r;
}
DI unsigned lds_addr(char* l) { return (unsigned)(size_t)(__attribute__((address_space(3))) char*)l; }
DI void glds16(const void* sbase, unsigned voff, unsigned dst) {
  asm volatile("s_mov_b32 m0, %2\n\ts_nop 0\n\tglobal_load_lds_dwordx4 %0, %1"
               :: "v"(voff), "s"(sbase), "s"(dst) : "memory");
}
DI float red16(float s) {
  const unsigned u = __float_as_uint(s);
  auto r = __builtin_amdgcn_permlane16_swap(u, u, false, false);
  return __uint_as_float(r[0]) + __uint_as_float(r[1]);
}
DI float red32(float s) {
  const unsigned u = __float_as_uint(s);
  auto r = __builtin_amdgcn_permlane32_swap(u, u, false, false);
  return __uint_as_float(r[0]) + __uint_as_float(r[1]);
}
#define WAIT_VM(n) asm volatile("s_waitcnt vmcnt(" #n ")" ::: "memory")
#define RAW_BAR() do { asm volatile("s_waitcnt lgkmcnt(0)" ::: "memory"); __builtin_amdgcn_s_barrier(); } while (0)
#define BARE_BAR() do { asm volatile("" ::: "memory"); __builtin_amdgcn_s_barrier(); } while (0)
template <int N> DI void wait_vm() { asm volatile("s_waitcnt vmcnt(%0)" ::"i"(N) : "memory"); }

DI float phi_f(float t) { return (t > 0.f ? t + 1.f : __expf(t)) + 1e-6f; }
DI void sq_frag(const u32x4& a, float& q) {
#pragma unroll
  for (int i = 0; i < 4; ++i) q = dot2(a[i], a[i], q);
}
#define ONES_FRAG ((u32x4){0x3f803f80u, 0x3f803f80u, 0x3f803f80u, 0x3f803f80u})

constexpr int K1_SLOT = 16384 + 12288, K1_NSLOT = 4;
__global__ __launch_bounds__(512) void k1_qkv(const u16* __restrict__ x, const u16* __restrict__ wqkv,
                                              u16* __restrict__ pq, float* __restrict__ part,
                                              const u16* __restrict__ wg, const u16* __restrict__ wu,
                                              float* __restrict__ csp, unsigned* __restrict__ cnt) {
  __shared__ __attribute__((aligned(16))) char smem[K1_SLOT * K1_NSLOT];
  const int tid = threadIdx.x, lane = tid & 63;
  const int wave = __builtin_amdgcn_readfirstlane(tid >> 6);
  const int xcd = blockIdx.x & 7, idx = blockIdx.x >> 3;
  const int rt = (xcd >> 1) * 4 + (idx >> 3);
  const int cg = (xcd & 1) * 8 + (idx & 7);
  const int r = lane & 15, g = lane >> 4;
  STAMP(0,0);
  if (blockIdx.x == 0 && tid < 8) cnt[tid * 64] = 0u;
  if (wave >= 4) {
    const int lw = wave - 4;
    const unsigned lbase = lds_addr(smem);
    const unsigned aoffs = (unsigned)(lw * 32 + (lane >> 3)) * 1024u + (((lane & 7) ^ ((lane >> 4) & 3)) << 4);
    unsigned boffs[3];
#pragma unroll
    for (int i = 0; i < 3; ++i) {
      const int line = (lw * 3 + i) * 4 + g;
      const int kq = line / 3, cgrp = line % 3;
      const int k = kq * 4 + ((lane >> 2) & 3);
      const int ch = (lane & 3) ^ (((k >> 3) & 1) << 1);
      boffs[i] = (unsigned)(k * 1536 + cgrp * 512 + ch * 8) * 2u;
    }
    const char* abase = (const char*)(x + (size_t)rt * 128 * D_);
    const char* bbase = (const char*)(wqkv + cg * 32);
    auto stage = [&](int s) {
      const unsigned slot = lbase + (s % K1_NSLOT) * K1_SLOT;
#pragma unroll
      for (int i = 0; i < 4; ++i)
        glds16(abase + s * 128 + i * 8192, (i & 1) ? (aoffs ^ 64u) : aoffs, slot + lw * 4096 + i * 1024);
#pragma unroll
      for (int i = 0; i < 3; ++i)
        glds16(bbase + (size_t)s * 64 * 3072, boffs[i], slot + 16384 + (lw * 3 + i) * 1024);
    };
    stage(0); stage(1);
    wait_vm<7>();
    BARE_BAR();
    stage(2);
#pragma unroll
    for (int s = 0; s < 7; ++s) {
      if (s == 0 || s == 5) wait_vm<7>(); else if (s <= 4) wait_vm<14>(); else wait_vm<0>();
      BARE_BAR();
      if (s == 0) stage(3);
      if (s + 4 < 8) stage(s + 4);
    }
    const int W = blockIdx.x * 4 + lw;
    const int grp = W % 86, kp = (W / 86) & 7;
    u32x4 wv[4];
    {
      const u16* wsrc = (grp < 43 ? wg : wu) + (grp % 43) * 32 + (lane & 3) * 8 + (size_t)(kp * 64 + (lane >> 2)) * F_;
#pragma unroll
      for (int i = 0; i < 4; ++i) wv[i] = *(const u32x4*)(wsrc + (size_t)i * 16 * F_);
    }
    BARE_BAR();
    if (W < 86 * 8) {
      float c8[8];
#pragma unroll
      for (int j = 0; j < 4; ++j) {
        c8[2 * j] = (bf_lo(wv[0][j]) + bf_lo(wv[1][j])) + (bf_lo(wv[2][j]) + bf_lo(wv[3][j]));
        c8[2 * j + 1] = (bf_hi(wv[0][j]) + bf_hi(wv[1][j])) + (bf_hi(wv[2][j]) + bf_hi(wv[3][j]));
      }
#pragma unroll
      for (int j = 0; j < 8; ++j) {
        float v = c8[j];
        v += __shfl_xor(v, 4); v += __shfl_xor(v, 8);
        c8[j] = red32(red16(v));
      }
      if (lane < 4) {
        float* dst = csp + kp * 2752 + grp * 32 + lane * 8;
        f32x4 v0 = {c8[0], c8[1], c8[2], c8[3]}, v1 = {c8[4], c8[5], c8[6], c8[7]};
        *(f32x4*)dst = v0; *(f32x4*)(dst + 4) = v1;
      }
    }
    BARE_BAR();
    return;
  }
  const int wr = wave >> 1, wc = wave & 1;
  const int q4 = (lane & 15) >> 2, p = lane & 3;
  const int aoff = (wr * 64 + r) * 128 + ((g ^ ((r >> 1) & 7)) << 4);
  const int aoff0 = aoff + wc * 4096, aoff1 = aoff + (wc ^ 1) * 4096;
  const int boff = 16384 + (2 * g) * 768 + q4 * 64 + ((wc ^ (g & 1)) << 5) + p * 8;
  const u32x4 ones = ONES_FRAG;
  f32x4 accq[4] = {}, acck[4] = {}, accv[4] = {};
  f32x4 rs[2] = {}, sqm[2] = {}, cq = {}, ck = {}, cv = {};
  u32x4 fa[2][4], fb[2][3];
  auto rdfrags = [&](int t) {
    const char* slot = smem + ((t >> 1) % K1_NSLOT) * K1_SLOT;
    const int kk = t & 1;
#pragma unroll
    for (int i = 0; i < 4; ++i)
      fa[kk][i] = *(const u32x4*)(slot + (((i < 2 ? aoff0 : aoff1) + (i & 1) * 2048) ^ (kk << 6)));
#pragma unroll
    for (int j = 0; j < 3; ++j)
      fb[kk][j] = tr_frag(slot + boff + kk * 6144 + j * 256, slot + boff + kk * 6144 + 768 + j * 256);
  };
  auto compute = [&](int t) {
    const int kk = t & 1;
    const u32x4* a = fa[kk];
    const u32x4 bq = fb[kk][0], bk = fb[kk][1], bv = fb[kk][2];
#pragma unroll
    for (int i = 0; i < 4; ++i) {
      accq[i] = mfma16(bq, a[i], accq[i]);
      acck[i] = mfma16(a[i], bk, acck[i]);
      accv[i] = mfma16(a[i], bv, accv[i]);
    }
    rs[0] = mfma16(ones, a[0], rs[0]);
    rs[1] = mfma16(ones, a[1], rs[1]);
    sqm[0] = mfma16(a[0], a[0], sqm[0]);
    sqm[1] = mfma16(a[1], a[1], sqm[1]);
    if (kk == wr) {
      cq = mfma16(bq, ones, cq);
      ck = mfma16(ones, bk, ck);
      cv = mfma16(ones, bv, cv);
    }
  };
  BARE_BAR();
  STAMP(0,2);
  rdfrags(0);
#pragma unroll
  for (int s = 0; s < 8; ++s) {
    rdfrags(2 * s + 1);
    compute(2 * s);
    __builtin_amdgcn_sched_barrier(0);
    if (s + 1 < 8) {
      RAW_BAR();
      STAMP(0,3+s);
      rdfrags(2 * s + 2);
    }
    compute(2 * s + 1);
    __builtin_amdgcn_sched_barrier(0);
  }
  STAMP(0,11);
  float* st = (float*)(smem);
  float* csl = (float*)(smem + 2048);
  float* st2 = (float*)(smem + 4096);
  float* red = (float*)(smem + 8192);
  {
#pragma unroll
    for (int i = 0; i < 2; ++i) {
      const float q01 = (r & 1) ? sqm[i][1] : sqm[i][0], q23 = (r & 1) ? sqm[i][3] : sqm[i][2];
      const float q = (r & 2) ? q23 : q01;
      if (g == (r >> 2)) {
        f32x2 v = {rs[i][0], q};
        *(f32x2*)(st + (wr * 64 + wc * 32 + i * 16 + r) * 2) = v;
      }
    }
    float* c = csl + (wr * 2 + wc) * 48;
    if (r == 0) *(f32x4*)(c + 4 * g) = cq;
    if (g == 0) { c[16 + r] = ck[0]; c[32 + r] = cv[0]; }
  }
  __syncthreads();
  STAMP(0,12);
  {
    const f32x2 a0 = *(const f32x2*)(st + (wr * 64 + lane) * 2);
    const float mean = a0[0] * (1.f / 512.f);
    const float var = fmaxf(a0[1] * (1.f / 512.f) - mean * mean, 0.f);
    f32x2 v = {mean, __builtin_amdgcn_rsqf(var + 1e-5f)};
    *(f32x2*)(st2 + (wave * 64 + lane) * 2) = v;
  }
  const f32x4 csq4 = *(const f32x4*)(csl + wc * 48 + 4 * g) + *(const f32x4*)(csl + (2 + wc) * 48 + 4 * g);
  const float csk = csl[wc * 48 + 16 + r] + csl[(2 + wc) * 48 + 16 + r];
  const float csv = csl[wc * 48 + 32 + r] + csl[(2 + wc) * 48 + 32 + r];
  f32x4 kv = {}, zz = {};
#pragma unroll
  for (int ih = 0; ih < 2; ++ih) {
    u32x4 pk, vv;
#pragma unroll
    for (int i2 = 0; i2 < 2; ++i2) {
      const int i = ih * 2 + i2;
      const int mbl = ((ih ^ wc) << 1) + i2;
      {
        const f32x2 mi = *(const f32x2*)(st2 + (wave * 64 + mbl * 16 + r) * 2);
        float qv[4];
#pragma unroll
        for (int j = 0; j < 4; ++j) qv[j] = phi_f((accq[i][j] - mi[0] * csq4[j]) * mi[1]);
        u32x2 o = {pack_bf(qv[0], qv[1]), pack_bf(qv[2], qv[3])};
        const int mblk = rt * 8 + wr * 4 + mbl;
        *(u32x2*)(pq + ((size_t)(mblk * 16 + cg) * 64 + r + 16 * (2 * wc + (g >> 1))) * 8 + (g & 1) * 4) = o;
      }
      {
        const f32x4 m0 = *(const f32x4*)(st2 + (wave * 64 + mbl * 16 + 4 * g) * 2);
        const f32x4 m1 = *(const f32x4*)(st2 + (wave * 64 + mbl * 16 + 4 * g) * 2 + 4);
        const float mean[4] = {m0[0], m0[2], m1[0], m1[2]}, inv[4] = {m0[1], m0[3], m1[1], m1[3]};
        float kk[4], vj[4];
#pragma unroll
        for (int j = 0; j < 4; ++j) {
          kk[j] = phi_f((acck[i][j] - mean[j] * csk) * inv[j]);
          vj[j] = (accv[i][j] - mean[j] * csv) * inv[j];
        }
        pk[2 * i2] = pack_bf(kk[0], kk[1]);
        pk[2 * i2 + 1] = pack_bf(kk[2], kk[3]);
        vv[2 * i2] = pack_bf(vj[0], vj[1]);
        vv[2 * i2 + 1] = pack_bf(vj[2], vj[3]);
      }
    }
    kv = mfma16(pk, vv, kv);
    zz = mfma16(pk, ones, zz);
  }
  {
    float* rd = red + (wc * 2 + wr) * 272;
#pragma unroll
    for (int j = 0; j < 4; ++j) rd[(4 * g + j) * 16 + r] = kv[j];
    if (r == 0) *(f32x4*)(rd + 256 + 4 * g) = zz;
  }
  __syncthreads();
  for (int i = tid; i < 544; i += 256) {
    const int hh = i >= 272 ? 1 : 0, e = i - hh * 272;
    const float* rr = red + hh * 2 * 272 + e;
    part[(size_t)(rt * 32 + cg * 2 + hh) * 272 + e] = rr[0] + rr[272];
  }
  STAMP(0,13);
}

typedef __attribute__((address_space(1))) unsigned gu32;
__global__ __launch_bounds__(512) void k2_attn_out(const u16* __restrict__ x, const u16* __restrict__ pq,
                                                   const float* __restrict__ part, const u16* __restrict__ wo,
                                                   u16* wp, unsigned* cnt,
                                                   u16* __restrict__ hf, float* __restrict__ hst,
                                                   const float* __restrict__ csp, float* __restrict__ csgu,
                                                   const u16* __restrict__ wg, const u16* __restrict__ wu,
                                                   const u16* __restrict__ wd) {
  __shared__ __attribute__((aligned(16))) char smem[65536 + 16384 + 1088];
  const int tid = threadIdx.x, lane = tid & 63;
  const int wave = __builtin_amdgcn_readfirstlane(tid >> 6);
  const int xcd = blockIdx.x & 7, idx = blockIdx.x >> 3;
  const int rt = xcd * 4 + (idx >> 3);
  const int ct = idx & 7;
  const int kg = wave >> 2, rb = wave & 3;
  const int r = lane & 15, g = lane >> 4;
  const int mblk = rt * 4 + rb;
  const __amdgpu_buffer_rsrc_t wrs = __builtin_amdgcn_make_buffer_rsrc(wp, 0, 524288 + 8192, 0x00020000);
  STAMP(2,0);
  const int ph = blockIdx.x >> 3, pc = blockIdx.x & 7;
  float* kv = (float*)(smem + 65536 + 16384);
  float pv[16];
  if (tid < 272) {
#pragma unroll
    for (int p = 0; p < 16; ++p) pv[p] = part[(size_t)(p * 32 + ph) * 272 + tid];
  }
  float w[16];
  if (tid < 128) {
#pragma unroll
    for (int e = 0; e < 16; ++e) w[e] = bf_lo(wo[(size_t)(ph * 16 + e) * D_ + pc * 64 + (tid & 63)]);
  }
  float cpart[8];
  const int ccol = blockIdx.x * 11 + (tid - 272);
  const bool do_cs = tid >= 272 && tid < 283 && ccol < 2752;
  if (do_cs) {
#pragma unroll
    for (int kp = 0; kp < 8; ++kp) cpart[kp] = csp[kp * 2752 + ccol];
  }
  u32x4 a[8];
#pragma unroll
  for (int i = 0; i < 8; ++i) a[i] = *(const u32x4*)(pq + ((size_t)(mblk * 16 + kg * 8 + i) * 64 + lane) * 8);
  u32x2 xr[2];
#pragma unroll
  for (int j = 0; j < 2; ++j)
    xr[j] = *(const u32x2*)(x + (size_t)(mblk * 16 + r) * D_ + ct * 64 + (kg * 2 + j) * 16 + g * 4);
  if (tid < 272) {
    float s = 0.f;
#pragma unroll
    for (int p = 0; p < 16; ++p) s += pv[p];
    kv[tid] = s;
  }
  if (do_cs) csgu[ccol] = ((cpart[0] + cpart[1]) + (cpart[2] + cpart[3])) + ((cpart[4] + cpart[5]) + (cpart[6] + cpart[7]));
  __syncthreads();
  STAMP(2,1);
  if (tid < 128) {
    const int c = tid & 63, dh = tid >> 6;
    float o[8];
#pragma unroll
    for (int j = 0; j < 8; ++j) {
      float s = 0.f;
#pragma unroll
      for (int e4 = 0; e4 < 4; ++e4) {
        const f32x4 k4 = *(const f32x4*)(kv + (dh * 8 + j) * 16 + e4 * 4);
        s = fmaf(k4[0], w[e4 * 4], s); s = fmaf(k4[1], w[e4 * 4 + 1], s);
        s = fmaf(k4[2], w[e4 * 4 + 2], s); s = fmaf(k4[3], w[e4 * 4 + 3], s);
      }
      o[j] = s;
    }
    const int n = pc * 64 + c, nb = n >> 4, cc = n & 15;
    const int ks = ph >> 1, gg = (ph & 1) * 2 + dh;
    u32x4 v = {pack_bf(o[0], o[1]), pack_bf(o[2], o[3]), pack_bf(o[4], o[5]), pack_bf(o[6], o[7])};
    __builtin_amdgcn_raw_buffer_store_b128(v, wrs, (unsigned)((nb * 16 + ks) * 64 + gg * 16 + cc) * 16u, 0, 16);
  } else if (tid < 130) {
    const float* zs = kv + 256 + (tid - 128) * 8;
    u32x4 v = {pack_bf(zs[0], zs[1]), pack_bf(zs[2], zs[3]), pack_bf(zs[4], zs[5]), pack_bf(zs[6], zs[7])};
    __builtin_amdgcn_raw_buffer_store_b128(v, wrs, 524288u + (unsigned)(pc * 512 + ph * 16 + (tid - 128) * 8) * 2u, 0, 16);
  }
  WAIT_VM(0);
  __syncthreads();
  if (tid == 0) __hip_atomic_fetch_add((gu32*)(cnt + pc * 64), 1u, __ATOMIC_RELAXED, __HIP_MEMORY_SCOPE_AGENT);
  STAMP(2,2);
  unsigned touch = 0;
  if (wave >= 1) {
    const int l0 = idx * 344 + (wave - 1) * 64 + lane;
    if (wave <= 6 && (wave - 1) * 64 + lane < 344) touch += *(const unsigned*)((const char*)wd + (size_t)l0 * 128);
    const int t = (wave - 1) * 64 + lane;
    if (t < 384) {
      const int row = idx * 16 + (t >> 5 << 1 >> 1) % 16, m = (t / 12) & 1;
      const int rr = idx * 16 + (t / 24), ln = t % 12;
      const size_t off = (size_t)rr * (F_ * 2) + (xcd & 1) * 1536 + (size_t)ln * 128;
      const size_t lim = (size_t)rr * (F_ * 2) + F_ * 2 - 4;
      touch += *(const unsigned*)((const char*)(m ? wu : wg) + (off < lim ? off : lim));
      (void)row;
    }
  }
  if (wave == 0) {
    for (unsigned spins = 0; spins < (1u << 22); ++spins) {
      const unsigned v = __builtin_amdgcn_readfirstlane(
          __hip_atomic_load((gu32*)(cnt + ct * 64), __ATOMIC_RELAXED, __HIP_MEMORY_SCOPE_AGENT));
      if (v >= 32u) break;
      __builtin_amdgcn_s_sleep(1);
    }
  }
  __syncthreads();
  STAMP(2,3);
  u32x4 zf[8], wv[8];
#pragma unroll
  for (int i = 0; i < 8; ++i)
    zf[i] = __builtin_amdgcn_raw_buffer_load_b128(wrs, 524288u + (unsigned)(ct * 512 + (kg * 8 + i) * 32 + g * 8) * 2u, 0, 16);
#pragma unroll
  for (int i = 0; i < 8; ++i)
    wv[i] = __builtin_amdgcn_raw_buffer_load_b128(wrs, (unsigned)(ct * 65536 + (wave * 8 + i) * 1024 + lane * 16), 0, 16);
#pragma unroll
  for (int i = 0; i < 8; ++i) {
    float s = dot2(a[i][0], zf[i][0], 0.f);
    s = dot2(a[i][1], zf[i][1], s);
    s = dot2(a[i][2], zf[i][2], s);
    s = dot2(a[i][3], zf[i][3], s);
    const float inv = __builtin_amdgcn_rcpf(red16(s) + 1e-6f);
#pragma unroll
    for (int j = 0; j < 4; ++j) a[i][j] = pack_bf(bf_lo(a[i][j]) * inv, bf_hi(a[i][j]) * inv);
  }
#pragma unroll
  for (int i = 0; i < 8; ++i) *(u32x4*)(smem + (wave * 8 + i) * 1024 + lane * 16) = wv[i];
  __syncthreads();
  STAMP(2,4);
  f32x4 acc[4] = {};
#pragma unroll
  for (int i = 0; i < 8; ++i) {
#pragma unroll
    for (int nb = 0; nb < 4; ++nb) {
      const u32x4 b = *(const u32x4*)(smem + (nb * 16 + kg * 8 + i) * 1024 + lane * 16);
      acc[nb] = mfma16(b, a[i], acc[nb]);
    }
  }
  STAMP(2,5);
  f32x4* xch = (f32x4*)(smem + 65536);
#pragma unroll
  for (int j = 0; j < 2; ++j) xch[(wave * 2 + j) * 64 + lane] = kg ? acc[j] : acc[2 + j];
  __syncthreads();
  float hs = 0.f, hq = 0.f;
#pragma unroll
  for (int j = 0; j < 2; ++j) {
    const f32x4 o4 = (kg ? acc[2 + j] : acc[j]) + xch[((wave ^ 4) * 2 + j) * 64 + lane];
    u32x2 o;
    o[0] = pack_bf(bf_lo(xr[j][0]) + o4[0], bf_hi(xr[j][0]) + o4[1]);
    o[1] = pack_bf(bf_lo(xr[j][1]) + o4[2], bf_hi(xr[j][1]) + o4[3]);
    *(u32x2*)(hf + ((size_t)(mblk * 16 + ct * 2 + kg) * 64 + r + 16 * (2 * j + (g >> 1))) * 8 + (g & 1) * 4) = o;
    hs = dot2(o[0], 0x3f803f80u, hs); hs = dot2(o[1], 0x3f803f80u, hs);
    hq = dot2(o[0], o[0], hq); hq = dot2(o[1], o[1], hq);
  }
  hs = red32(red16(hs)); hq = red32(red16(hq));
  if (g == 0) {
    f32x2 v = {hs, hq};
    *(f32x2*)(hst + ((size_t)(mblk * 16 + r) * 16 + ct * 2 + kg) * 2) = v;
  }
  asm volatile("" :: "v"(touch));
  STAMP(2,6);
}

constexpr int K3_SLOT = 16384 + 24576, K3_NSLOT = 4;
__global__ __launch_bounds__(512) void k3_gateup(const u16* __restrict__ hf, const u16* __restrict__ wg,
                                                 const u16* __restrict__ wu, u16* __restrict__ hhf,
                                                 const float* __restrict__ hst, const float* __restrict__ csgu) {
  __shared__ __attribute__((aligned(16))) char smem[K3_SLOT * K3_NSLOT];
  const int tid = threadIdx.x, lane = tid & 63;
  const int wave = __builtin_amdgcn_readfirstlane(tid >> 6);
  const int xcd = blockIdx.x & 7, idx = blockIdx.x >> 3;
  const int rt = (xcd >> 1) * 4 + (idx >> 3);
  const int cg = (xcd & 1) * 8 + (idx & 7);
  const int r = lane & 15, g = lane >> 4;
  const int f0 = cg < 11 ? cg * 96 : 1056 + (cg - 11) * 64;
  const int nvalid = cg < 11 ? 6 : 4;
  STAMP(3,0);
  if (wave >= 4) {
    const int lw = wave - 4;
    const char* abase = (const char*)(hf + (size_t)((rt * 8 + lw * 2) * 16) * 512);
    const long wdelta = (const char*)wu - (const char*)wg;
    const char* bsrc[6];
#pragma unroll
    for (int i = 0; i < 6; ++i) {
      const int line = (lw * 6 + i) * 4 + g;
      const int kq = line / 6, cgrp = line % 6;
      const int k = kq * 4 + ((lane >> 2) & 3);
      const int ch = (lane & 3) ^ (((k >> 3) & 1) << 1);
      int c3 = cgrp < 3 ? cgrp : cgrp - 3;
      c3 = (c3 * 2 < nvalid) ? c3 : 0;
      bsrc[i] = (const char*)(wg + f0) + (unsigned)(k * F_ + c3 * 32 + ch * 8) * 2u + (cgrp >= 3 ? wdelta : 0);
    }
    auto glds16v = [&](const char* gsrc, unsigned dst) {
      asm volatile("s_mov_b32 m0, %1\n\ts_nop 0\n\tglobal_load_lds_dwordx4 %0, off" :: "v"(gsrc), "s"(dst) : "memory");
    };
    auto stage = [&](int s) {
      const unsigned slot = lds_addr(smem) + (s % K3_NSLOT) * K3_SLOT;
#pragma unroll
      for (int i = 0; i < 4; ++i)
        glds16(abase + (i >> 1) * 16384 + (s * 2 + (i & 1)) * 1024, lane * 16, slot + (lw * 4 + i) * 1024);
#pragma unroll
      for (int i = 0; i < 6; ++i) glds16v(bsrc[i] + (size_t)s * 64 * F_ * 2, slot + 16384 + (lw * 6 + i) * 1024);
    };
    stage(0); stage(1);
    wait_vm<10>();
    BARE_BAR();
    stage(2);
#pragma unroll
    for (int s = 0; s < 7; ++s) {
      if (s == 0 || s == 5) wait_vm<10>(); else if (s <= 4) wait_vm<20>(); else wait_vm<0>();
      BARE_BAR();
      if (s == 0) stage(3);
      if (s + 4 < 8) stage(s + 4);
    }
    return;
  }
  const int wr = wave >> 1, wc = wave & 1;
  const int q4 = (lane & 15) >> 2, p = lane & 3;
  const int aoff = wr * 8192 + lane * 16;
  const int bbase2 = 16384 + (2 * g) * 1536 + q4 * 64 + p * 8;
  int boff[3];
#pragma unroll
  for (int j = 0; j < 3; ++j) {
    const int fbl = 3 * wc + j;
    boff[j] = bbase2 + (fbl >> 1) * 256 + (((fbl & 1) ^ (g & 1)) << 5);
  }
  f32x4 hp[8];
  f32x4 cg4[3], cu4[3];
  float mean_l = 0.f, inv2_l = 0.f;
  f32x4 accg[4][3] = {}, accu[4][3] = {};
  u32x4 fa[2][4], fb[2][6];
  auto rdfrags = [&](int t) {
    const char* slot = smem + ((t >> 1) % K3_NSLOT) * K3_SLOT;
    const int kk = t & 1;
#pragma unroll
    for (int i = 0; i < 4; ++i) fa[kk][i] = *(const u32x4*)(slot + aoff + i * 2048 + kk * 1024);
#pragma unroll
    for (int j = 0; j < 3; ++j) {
      fb[kk][j] = tr_frag(slot + boff[j] + kk * 12288, slot + boff[j] + kk * 12288 + 1536);
      fb[kk][3 + j] = tr_frag(slot + boff[j] + 768 + kk * 12288, slot + boff[j] + 768 + kk * 12288 + 1536);
    }
  };
  auto compute = [&](int t) {
    const int kk = t & 1;
    const u32x4* a = fa[kk];
#pragma unroll
    for (int j = 0; j < 3; ++j) {
      const u32x4 bg = fb[kk][j], bu = fb[kk][3 + j];
#pragma unroll
      for (int i = 0; i < 4; ++i) {
        accg[i][j] = mfma16(bg, a[i], accg[i][j]);
        accu[i][j] = mfma16(bu, a[i], accu[i][j]);
      }
    }
  };
  BARE_BAR();
  STAMP(3,2);
  rdfrags(0);
#pragma unroll
  for (int s = 0; s < 8; ++s) {
    rdfrags(2 * s + 1);
    compute(2 * s);
    __builtin_amdgcn_sched_barrier(0);
    if (s + 1 < 8) {
      RAW_BAR();
      STAMP(3,3+s);
      rdfrags(2 * s + 2);
    }
    if (s == 4) {
#pragma unroll
      for (int i = 0; i < 8; ++i) hp[i] = *(const f32x4*)(hst + (size_t)(rt * 128 + wr * 64 + lane) * 32 + i * 4);
    }
    if (s == 6) {
      f32x4 t4 = (hp[0] + hp[1]) + (hp[2] + hp[3]) + ((hp[4] + hp[5]) + (hp[6] + hp[7]));
      mean_l = (t4[0] + t4[2]) * (1.f / 512.f);
      const float var = fmaxf((t4[1] + t4[3]) * (1.f / 512.f) - mean_l * mean_l, 0.f);
      inv2_l = __builtin_amdgcn_rcpf(var + 1e-5f);
      asm volatile("" : "+v"(mean_l), "+v"(inv2_l));
#pragma unroll
      for (int j = 0; j < 3; ++j) {
        const int fbl = (3 * wc + j) < nvalid ? (3 * wc + j) : 0;
        cg4[j] = *(const f32x4*)(csgu + f0 + fbl * 16 + 4 * g);
        cu4[j] = *(const f32x4*)(csgu + F_ + f0 + fbl * 16 + 4 * g);
      }
    }
    compute(2 * s + 1);
    __builtin_amdgcn_sched_barrier(0);
  }
  STAMP(3,11);
#pragma unroll
  for (int i = 0; i < 4; ++i) {
    const float mean = __shfl(mean_l, i * 16 + r), inv2 = __shfl(inv2_l, i * 16 + r);
    const int mblk = rt * 8 + wr * 4 + i;
#pragma unroll
    for (int j = 0; j < 3; ++j) {
      const int fbl = 3 * wc + j;
      float hv[4];
#pragma unroll
      for (int e = 0; e < 4; ++e)
        hv[e] = ((accg[i][j][e] - mean * cg4[j][e]) * inv2) * (accu[i][j][e] - mean * cu4[j][e]);
      if (fbl < nvalid) {
        const int fb_ = (f0 >> 4) + fbl;
        u32x2 o = {pack_bf(hv[0], hv[1]), pack_bf(hv[2], hv[3])};
        *(u32x2*)(hhf + ((size_t)(mblk * KS4 + (fb_ >> 1)) * 64 + r + 16 * (2 * (fb_ & 1) + (g >> 1))) * 8 + (g & 1) * 4) = o;
      }
    }
  }
  STAMP(3,13);
}

__global__ __launch_bounds__(512) void k4_down(const u16* __restrict__ hf, const u16* __restrict__ hhf,
                                               const u16* __restrict__ wd, u16* __restrict__ out) {
  __shared__ __attribute__((aligned(16))) char smem[4 * 32768];
  const int tid = threadIdx.x, lane = tid & 63;
  const int wave = __builtin_amdgcn_readfirstlane(tid >> 6);
  const int xcd = blockIdx.x & 7, idx = blockIdx.x >> 3;
  const int rt = xcd * 4 + (idx >> 3);
  const int ct = idx & 7;
  const int r = lane & 15, g = lane >> 4;
  STAMP(4,0);
  if (wave >= 4) {
    const int lw = wave - 4;
    const char* abase = (const char*)(hhf + (size_t)((rt * 4 + lw) * KS4) * 512);
    const char* bbase = (const char*)(wd + (size_t)lw * 32 * D_ + ct * 64);
    const unsigned boffs = (unsigned)(((g >> 1) * 4 + ((lane >> 2) & 3)) * D_ + (g & 1) * 32 + (lane & 3) * 8) * 2u;
    auto stage = [&](int s) {
      const unsigned slot = lds_addr(smem) + (s & 3) * 32768;
#pragma unroll
      for (int i = 0; i < 4; ++i)
        glds16(abase + (s * 4 + ((s == 10 && i == 3) ? 2 : i)) * 1024, lane * 16, slot + lw * 4096 + i * 1024);
      const int so = (s == 10 && lw == 3) ? s - 1 : s;
#pragma unroll
      for (int i = 0; i < 4; ++i)
        glds16(bbase + (size_t)(so * 128 + i * 8) * D_ * 2, (i & 1) ? (boffs ^ 32u) : boffs, slot + 16384 + lw * 4096 + i * 1024);
    };
    stage(0); stage(1);
    wait_vm<8>();
    BARE_BAR();
    stage(2);
#pragma unroll
    for (int s = 0; s < 10; ++s) {
      if (s == 0 || s == 8) wait_vm<8>(); else if (s <= 7) wait_vm<16>(); else wait_vm<0>();
      BARE_BAR();
      if (s == 0) stage(3);
      if (s + 4 < 11) stage(s + 4);
    }
    return;
  }
  const int wr = wave >> 1, wc = wave & 1;
  const int q4 = (lane & 15) >> 2, p = lane & 3;
  const int boff = 16384 + (2 * g) * 512 + wc * 256 + q4 * 64 + p * 8;
  const int aoff = wr * 8192 + lane * 16;
  u32x2 hr[2][2];
#pragma unroll
  for (int mb = 0; mb < 2; ++mb)
#pragma unroll
    for (int nb = 0; nb < 2; ++nb)
      hr[mb][nb] = *(const u32x2*)(hf + ((size_t)((rt * 4 + wr * 2 + mb) * 16 + ct * 2 + wc) * 64 + r + 16 * (2 * nb + (g >> 1))) * 8 + (g & 1) * 4);
  f32x4 acc[2][2] = {};
  u32x4 fa[2][2][2], fb[2][2][2];
  auto rdfrags = [&](int t) {
    const char* slot = smem + ((t >> 1) & 3) * 32768;
#pragma unroll
    for (int kk = 0; kk < 2; ++kk) {
      const int ksl = (t & 1) * 2 + kk;
#pragma unroll
      for (int mb = 0; mb < 2; ++mb) fa[t & 1][kk][mb] = *(const u32x4*)(slot + aoff + mb * 4096 + ksl * 1024);
#pragma unroll
      for (int nb = 0; nb < 2; ++nb) {
        const int ho = (nb ^ (g & 1)) << 5;
        fb[t & 1][kk][nb] = tr_frag(slot + boff + ksl * 4096 + ho, slot + boff + ksl * 4096 + 512 + ho);
      }
    }
  };
  auto compute = [&](int t) {
#pragma unroll
    for (int kk = 0; kk < 2; ++kk) {
      if (2 * t + kk < KS4) {
#pragma unroll
        for (int mb = 0; mb < 2; ++mb)
#pragma unroll
          for (int nb = 0; nb < 2; ++nb) acc[mb][nb] = mfma16(fb[t & 1][kk][nb], fa[t & 1][kk][mb], acc[mb][nb]);
      }
    }
  };
  BARE_BAR();
  STAMP(4,2);
  rdfrags(0);
#pragma unroll
  for (int s = 0; s < 11; ++s) {
    rdfrags(2 * s + 1);
    compute(2 * s);
    if (s + 1 < 11) {
      RAW_BAR();
      STAMP(4,3+s);
      rdfrags(2 * s + 2);
    }
    compute(2 * s + 1);
  }
#pragma unroll
  for (int mb = 0; mb < 2; ++mb)
#pragma unroll
    for (int nb = 0; nb < 2; ++nb) {
      const f32x4 o4 = acc[mb][nb];
      u32x2 o;
      o[0] = pack_bf(bf_lo(hr[mb][nb][0]) + o4[0], bf_hi(hr[mb][nb][0]) + o4[1]);
      o[1] = pack_bf(bf_lo(hr[mb][nb][1]) + o4[2], bf_hi(hr[mb][nb][1]) + o4[3]);
      *(u32x2*)(out + (size_t)(rt * 64 + wr * 32 + mb * 16 + r) * D_ + ct * 64 + wc * 32 + nb * 16 + g * 4) = o;
    }
  STAMP(4,14);
}

extern "C" void kernel_launch(void* const* d_in, const int* in_sizes, int n_in, void* d_out, int out_size,
                              void* d_ws, size_t ws_size, hipStream_t stream) {
  const u16* x = (const u16*)d_in[0];
  const u16* wqkv = (const u16*)d_in[1];
  const u16* wo = (const u16*)d_in[2];
  const u16* wg = (const u16*)d_in[4];
  const u16* wu = (const u16*)d_in[5];
  const u16* wd = (const u16*)d_in[6];
  u16* out = (u16*)d_out;
  char* ws = (char*)d_ws;
  u16* pq = (u16*)(ws);
  u16* hf = (u16*)(ws + (2u << 20));
  u16* wp = (u16*)(ws + (4u << 20));
  float* part = (float*)(ws + (5u << 20));
  unsigned* cnt = (unsigned*)(ws + (6u << 20));
  float* csgu = (float*)(ws + (6u << 20) + 4096);
  float* csp = (float*)(ws + (6u << 20) + 65536);
  float* hst = (float*)(ws + (7u << 20));
  u16* hhf = (u16*)(ws + (8u << 20));
  k1_qkv<<<256, 512, 0, stream>>>(x, wqkv, pq, part, wg, wu, csp, cnt);
  k2_attn_out<<<256, 512, 0, stream>>>(x, pq, part, wo, wp, cnt, hf, hst, csp, csgu, wg, wu, wd);
  k3_gateup<<<256, 512, 0, stream>>>(hf, wg, wu, hhf, hst, csgu);
  k4_down<<<256, 512, 0, stream>>>(hf, hhf, wd, out);
}
```

```cpp
#include <hip/hip_runtime.h>
#include <hip/hip_bf16.h>
#include <stdint.h>

typedef unsigned short u16;
typedef __attribute__((ext_vector_type(8))) __bf16 bf16x8;
typedef __attribute__((ext_vector_type(2))) __bf16 bf16x2;
typedef __attribute__((ext_vector_type(4))) short s16x4;
typedef __attribute__((ext_vector_type(4))) float f32x4;
typedef __attribute__((ext_vector_type(2))) float f32x2;
typedef __attribute__((ext_vector_type(4))) unsigned u32x4;
typedef __attribute__((ext_vector_type(2))) unsigned u32x2;
typedef __attribute__((address_space(3))) s16x4* lds_s16x4_ptr;

#define DI __device__ __forceinline__
#define STAMP(kid,i)

constexpr int S_ = 2048, D_ = 512, F_ = 1376, H_ = 32;
constexpr int KS4 = F_ / 32;

DI float bf_lo(unsigned u) { return __uint_as_float(u << 16); }
DI float bf_hi(unsigned u) { return __uint_as_float(u & 0xffff0000u); }
DI unsigned pack_bf(float lo, float hi) {
  f32x2 f = {lo, hi};
  bf16x2 r = __builtin_convertvector(f, bf16x2);
  return __builtin_bit_cast(unsigned, r);
}
DI float bfr(float f) { return bf_lo(pack_bf(f, 0.f) & 0xffffu); }
DI f32x4 mfma16(u32x4 a, u32x4 b, f32x4 c) {
  return __builtin_amdgcn_mfma_f32_16x16x32_bf16(__builtin_bit_cast(bf16x8, a),
                                                 __builtin_bit_cast(bf16x8, b), c, 0, 0, 0);
}
DI float dot2(unsigned a, unsigned b, float c) {
  return __builtin_amdgcn_fdot2_f32_bf16(__builtin_bit_cast(bf16x2, a), __builtin_bit_cast(bf16x2, b), c, false);
}
DI u32x4 tr_frag(const char* lds0, const char* lds1) {
  s16x4 lo = __builtin_amdgcn_ds_read_tr16_b64_v4i16((lds_s16x4_ptr)lds0);
  s16x4 hi = __builtin_amdgcn_ds_read_tr16_b64_v4i16((lds_s16x4_ptr)lds1);
  u32x2 l2 = __builtin_bit_cast(u32x2, lo), h2 = __builtin_bit_cast(u32x2, hi);
  u32x4 r = {l2[0], l2[1], h2[0], h2[1]};
  return r;
}
DI unsigned lds_addr(char* l) { return (unsigned)(size_t)(__attribute__((address_space(3))) char*)l; }
DI void glds16(const void* sbase, unsigned voff, unsigned dst) {
  asm volatile("s_mov_b32 m0, %2\n\ts_nop 0\n\tglobal_load_lds_dwordx4 %0, %1"
               :: "v"(voff), "s"(sbase), "s"(dst) : "memory");
}
DI float red16(float s) {
  const unsigned u = __float_as_uint(s);
  auto r = __builtin_amdgcn_permlane16_swap(u, u, false, false);
  return __uint_as_float(r[0]) + __uint_as_float(r[1]);
}
DI float red32(float s) {
  const unsigned u = __float_as_uint(s);
  auto r = __builtin_amdgcn_permlane32_swap(u, u, false, false);
  return __uint_as_float(r[0]) + __uint_as_float(r[1]);
}
#define WAIT_VM(n) asm volatile("s_waitcnt vmcnt(" #n ")" ::: "memory")
#define RAW_BAR() do { asm volatile("s_waitcnt lgkmcnt(0)" ::: "memory"); __builtin_amdgcn_s_barrier(); } while (0)
#define BARE_BAR() do { asm volatile("" ::: "memory"); __builtin_amdgcn_s_barrier(); } while (0)
template <int N> DI void wait_vm() { asm volatile("s_waitcnt vmcnt(%0)" ::"i"(N) : "memory"); }

DI float phi_f(float t) { return (t > 0.f ? t + 1.f : __expf(t)) + 1e-6f; }
DI void sq_frag(const u32x4& a, float& q) {
#pragma unroll
  for (int i = 0; i < 4; ++i) q = dot2(a[i], a[i], q);
}
#define ONES_FRAG ((u32x4){0x3f803f80u, 0x3f803f80u, 0x3f803f80u, 0x3f803f80u})

constexpr int K1_SLOT = 16384 + 12288, K1_NSLOT = 4;
__global__ __launch_bounds__(512) void k1_qkv(const u16* __restrict__ x, const u16* __restrict__ wqkv,
                                              u16* __restrict__ pq, float* __restrict__ part,
                                              const u16* __restrict__ wg, const u16* __restrict__ wu,
                                              float* __restrict__ csp, unsigned* __restrict__ cnt,
                                              const u16* __restrict__ wo, const u16* __restrict__ wd,
                                              const u16* outp) {
  __shared__ __attribute__((aligned(16))) char smem[K1_SLOT * K1_NSLOT];
  const int tid = threadIdx.x, lane = tid & 63;
  const int wave = __builtin_amdgcn_readfirstlane(tid >> 6);
  const int xcd = blockIdx.x & 7, idx = blockIdx.x >> 3;
  const int rt = (xcd >> 1) * 4 + (idx >> 3);
  const int cg = (xcd & 1) * 8 + (idx & 7);
  const int r = lane & 15, g = lane >> 4;
  STAMP(0,0);
  if (blockIdx.x == 0 && tid < 8) cnt[tid * 64] = 0u;
  if (wave >= 4) {
    const int lw = wave - 4;
    const unsigned lbase = lds_addr(smem);
    const unsigned aoffs = (unsigned)(lw * 32 + (lane >> 3)) * 1024u + (((lane & 7) ^ ((lane >> 4) & 3)) << 4);
    unsigned boffs[3];
#pragma unroll
    for (int i = 0; i < 3; ++i) {
      const int line = (lw * 3 + i) * 4 + g;
      const int kq = line / 3, cgrp = line % 3;
      const int k = kq * 4 + ((lane >> 2) & 3);
      const int ch = (lane & 3) ^ (((k >> 3) & 1) << 1);
      boffs[i] = (unsigned)(k * 1536 + cgrp * 512 + ch * 8) * 2u;
    }
    const char* abase = (const char*)(x + (size_t)rt * 128 * D_);
    const char* bbase = (const char*)(wqkv + cg * 32);
    auto stage = [&](int s) {
      const unsigned slot = lbase + (s % K1_NSLOT) * K1_SLOT;
#pragma unroll
      for (int i = 0; i < 4; ++i)
        glds16(abase + s * 128 + i * 8192, (i & 1) ? (aoffs ^ 64u) : aoffs, slot + lw * 4096 + i * 1024);
#pragma unroll
      for (int i = 0; i < 3; ++i)
        glds16(bbase + (size_t)s * 64 * 3072, boffs[i], slot + 16384 + (lw * 3 + i) * 1024);
    };
    stage(0); stage(1);
    wait_vm<7>();
    BARE_BAR();
    stage(2);
#pragma unroll
    for (int s = 0; s < 7; ++s) {
      if (s == 0 || s == 5) wait_vm<7>(); else if (s <= 4) wait_vm<14>(); else wait_vm<0>();
      BARE_BAR();
      if (s == 0) stage(3);
      if (s + 4 < 8) stage(s + 4);
    }
    const int W = blockIdx.x * 4 + lw;
    const int grp = W % 86, kp = (W / 86) & 7;
    u32x4 wv[4];
    {
      const u16* wsrc = (grp < 43 ? wg : wu) + (grp % 43) * 32 + (lane & 3) * 8 + (size_t)(kp * 64 + (lane >> 2)) * F_;
#pragma unroll
      for (int i = 0; i < 4; ++i) wv[i] = *(const u32x4*)(wsrc + (size_t)i * 16 * F_);
    }
    unsigned touch;
    {
      const int T = idx * 256 + lw * 64 + lane;
      const char* tp = (const char*)pq + (size_t)T * 4096;
      if (T >= 3584) tp = (const char*)outp + (size_t)(T - 3584) * 4096;
      if (T >= 4096) tp = (const char*)wo + (size_t)(T - 4096) * 4096;
      if (T >= 4224) tp = (const char*)wd + (size_t)(T - 4224) * 4096;
      if (T >= 4568) tp = (const char*)wg + (size_t)(T - 4568) * 4096;
      if (T >= 4912) tp = (const char*)wu + (size_t)(T - 4912) * 4096;
      if (T >= 5256) tp = (const char*)wu;
      touch = *(const volatile unsigned*)tp;
    }
    BARE_BAR();
    if (W < 86 * 8) {
      float c8[8];
#pragma unroll
      for (int j = 0; j < 4; ++j) {
        c8[2 * j] = (bf_lo(wv[0][j]) + bf_lo(wv[1][j])) + (bf_lo(wv[2][j]) + bf_lo(wv[3][j]));
        c8[2 * j + 1] = (bf_hi(wv[0][j]) + bf_hi(wv[1][j])) + (bf_hi(wv[2][j]) + bf_hi(wv[3][j]));
      }
#pragma unroll
      for (int j = 0; j < 8; ++j) {
        float v = c8[j];
        v += __shfl_xor(v, 4); v += __shfl_xor(v, 8);
        c8[j] = red32(red16(v));
      }
      if (lane < 4) {
        float* dst = csp + kp * 2752 + grp * 32 + lane * 8;
        f32x4 v0 = {c8[0], c8[1], c8[2], c8[3]}, v1 = {c8[4], c8[5], c8[6], c8[7]};
        *(f32x4*)dst = v0; *(f32x4*)(dst + 4) = v1;
      }
    }
    asm volatile("" :: "v"(touch));
    BARE_BAR();
    return;
  }
  const int wr = wave >> 1, wc = wave & 1;
  const int q4 = (lane & 15) >> 2, p = lane & 3;
  const int aoff = (wr * 64 + r) * 128 + ((g ^ ((r >> 1) & 7)) << 4);
  const int aoff0 = aoff + wc * 4096, aoff1 = aoff + (wc ^ 1) * 4096;
  const int boff = 16384 + (2 * g) * 768 + q4 * 64 + ((wc ^ (g & 1)) << 5) + p * 8;
  const u32x4 ones = ONES_FRAG;
  f32x4 accq[4] = {}, acck[4] = {}, accv[4] = {};
  f32x4 rs[2] = {}, sqm[2] = {}, cq = {}, ck = {}, cv = {};
  u32x4 fa[2][4], fb[2][3];
  auto rdfrags = [&](int t) {
    const char* slot = smem + ((t >> 1) % K1_NSLOT) * K1_SLOT;
    const int kk = t & 1;
#pragma unroll
    for (int i = 0; i < 4; ++i)
      fa[kk][i] = *(const u32x4*)(slot + (((i < 2 ? aoff0 : aoff1) + (i & 1) * 2048) ^ (kk << 6)));
#pragma unroll
    for (int j = 0; j < 3; ++j)
      fb[kk][j] = tr_frag(slot + boff + kk * 6144 + j * 256, slot + boff + kk * 6144 + 768 + j * 256);
  };
  auto compute = [&](int t) {
    const int kk = t & 1;
    const u32x4* a = fa[kk];
    const u32x4 bq = fb[kk][0], bk = fb[kk][1], bv = fb[kk][2];
#pragma unroll
    for (int i = 0; i < 4; ++i) {
      accq[i] = mfma16(bq, a[i], accq[i]);
      acck[i] = mfma16(a[i], bk, acck[i]);
      accv[i] = mfma16(a[i], bv, accv[i]);
    }
    rs[0] = mfma16(ones, a[0], rs[0]);
    rs[1] = mfma16(ones, a[1], rs[1]);
    sqm[0] = mfma16(a[0], a[0], sqm[0]);
    sqm[1] = mfma16(a[1], a[1], sqm[1]);
    if (kk == wr) {
      cq = mfma16(bq, ones, cq);
      ck = mfma16(ones, bk, ck);
      cv = mfma16(ones, bv, cv);
    }
  };
  BARE_BAR();
  STAMP(0,2);
  rdfrags(0);
#pragma unroll
  for (int s = 0; s < 8; ++s) {
    rdfrags(2 * s + 1);
    compute(2 * s);
    __builtin_amdgcn_sched_barrier(0);
    if (s + 1 < 8) {
      RAW_BAR();
      STAMP(0,3+s);
      rdfrags(2 * s + 2);
    }
    compute(2 * s + 1);
    __builtin_amdgcn_sched_barrier(0);
  }
  STAMP(0,11);
  float* st = (float*)(smem);
  float* csl = (float*)(smem + 2048);
  float* st2 = (float*)(smem + 4096);
  float* red = (float*)(smem + 8192);
  {
#pragma unroll
    for (int i = 0; i < 2; ++i) {
      const float q01 = (r & 1) ? sqm[i][1] : sqm[i][0], q23 = (r & 1) ? sqm[i][3] : sqm[i][2];
      const float q = (r & 2) ? q23 : q01;
      if (g == (r >> 2)) {
        f32x2 v = {rs[i][0], q};
        *(f32x2*)(st + (wr * 64 + wc * 32 + i * 16 + r) * 2) = v;
      }
    }
    float* c = csl + (wr * 2 + wc) * 48;
    if (r == 0) *(f32x4*)(c + 4 * g) = cq;
    if (g == 0) { c[16 + r] = ck[0]; c[32 + r] = cv[0]; }
  }
  __syncthreads();
  STAMP(0,12);
  {
    const f32x2 a0 = *(const f32x2*)(st + (wr * 64 + lane) * 2);
    const float mean = a0[0] * (1.f / 512.f);
    const float var = fmaxf(a0[1] * (1.f / 512.f) - mean * mean, 0.f);
    f32x2 v = {mean, __builtin_amdgcn_rsqf(var + 1e-5f)};
    *(f32x2*)(st2 + (wave * 64 + lane) * 2) = v;
  }
  const f32x4 csq4 = *(const f32x4*)(csl + wc * 48 + 4 * g) + *(const f32x4*)(csl + (2 + wc) * 48 + 4 * g);
  const float csk = csl[wc * 48 + 16 + r] + csl[(2 + wc) * 48 + 16 + r];
  const float csv = csl[wc * 48 + 32 + r] + csl[(2 + wc) * 48 + 32 + r];
  f32x4 kv = {}, zz = {};
#pragma unroll
  for (int ih = 0; ih < 2; ++ih) {
    u32x4 pk, vv;
#pragma unroll
    for (int i2 = 0; i2 < 2; ++i2) {
      const int i = ih * 2 + i2;
      const int mbl = ((ih ^ wc) << 1) + i2;
      {
        const f32x2 mi = *(const f32x2*)(st2 + (wave * 64 + mbl * 16 + r) * 2);
        float qv[4];
#pragma unroll
        for (int j = 0; j < 4; ++j) qv[j] = phi_f((accq[i][j] - mi[0] * csq4[j]) * mi[1]);
        u32x2 o = {pack_bf(qv[0], qv[1]), pack_bf(qv[2], qv[3])};
        const int mblk = rt * 8 + wr * 4 + mbl;
        *(u32x2*)(pq + ((size_t)(mblk * 16 + cg) * 64 + r + 16 * (2 * wc + (g >> 1))) * 8 + (g & 1) * 4) = o;
      }
      {
        const f32x4 m0 = *(const f32x4*)(st2 + (wave * 64 + mbl * 16 + 4 * g) * 2);
        const f32x4 m1 = *(const f32x4*)(st2 + (wave * 64 + mbl * 16 + 4 * g) * 2 + 4);
        const float mean[4] = {m0[0], m0[2], m1[0], m1[2]}, inv[4] = {m0[1], m0[3], m1[1], m1[3]};
        float kk[4], vj[4];
#pragma unroll
        for (int j = 0; j < 4; ++j) {
          kk[j] = phi_f((acck[i][j] - mean[j] * csk) * inv[j]);
          vj[j] = (accv[i][j] - mean[j] * csv) * inv[j];
        }
        pk[2 * i2] = pack_bf(kk[0], kk[1]);
        pk[2 * i2 + 1] = pack_bf(kk[2], kk[3]);
        vv[2 * i2] = pack_bf(vj[0], vj[1]);
        vv[2 * i2 + 1] = pack_bf(vj[2], vj[3]);
      }
    }
    kv = mfma16(pk, vv, kv);
    zz = mfma16(pk, ones, zz);
  }
  {
    float* rd = red + (wc * 2 + wr) * 272;
#pragma unroll
    for (int j = 0; j < 4; ++j) rd[(4 * g + j) * 16 + r] = kv[j];
    if (r == 0) *(f32x4*)(rd + 256 + 4 * g) = zz;
  }
  __syncthreads();
  for (int i = tid; i < 544; i += 256) {
    const int hh = i >= 272 ? 1 : 0, e = i - hh * 272;
    const float* rr = red + hh * 2 * 272 + e;
    part[(size_t)(rt * 32 + cg * 2 + hh) * 272 + e] = rr[0] + rr[272];
  }
  STAMP(0,13);
}

typedef __attribute__((address_space(1))) unsigned gu32;
__global__ __launch_bounds__(512) void k2_attn_out(const u16* __restrict__ x, const u16* __restrict__ pq,
                                                   const float* __restrict__ part, const u16* __restrict__ wo,
                                                   u16* wp, unsigned* cnt,
                                                   u16* __restrict__ hf, float* __restrict__ hst,
                                                   const float* __restrict__ csp, float* __restrict__ csgu,
                                                   const u16* __restrict__ wg, const u16* __restrict__ wu,
                                                   const u16* __restrict__ wd) {
  __shared__ __attribute__((aligned(16))) char smem[65536 + 16384 + 1088];
  const int tid = threadIdx.x, lane = tid & 63;
  const int wave = __builtin_amdgcn_readfirstlane(tid >> 6);
  const int xcd = blockIdx.x & 7, idx = blockIdx.x >> 3;
  const int rt = xcd * 4 + (idx >> 3);
  const int ct = idx & 7;
  const int kg = wave >> 2, rb = wave & 3;
  const int r = lane & 15, g = lane >> 4;
  const int mblk = rt * 4 + rb;
  const __amdgpu_buffer_rsrc_t wrs = __builtin_amdgcn_make_buffer_rsrc(wp, 0, 524288 + 8192, 0x00020000);
  STAMP(2,0);
  const int ph = blockIdx.x >> 3, pc = blockIdx.x & 7;
  float* kv = (float*)(smem + 65536 + 16384);
  float pv[16];
  if (tid < 272) {
#pragma unroll
    for (int p = 0; p < 16; ++p) pv[p] = part[(size_t)(p * 32 + ph) * 272 + tid];
  }
  float w[16];
  if (tid < 128) {
#pragma unroll
    for (int e = 0; e < 16; ++e) w[e] = bf_lo(wo[(size_t)(ph * 16 + e) * D_ + pc * 64 + (tid & 63)]);
  }
  float cpart[8];
  const int ccol = blockIdx.x * 11 + (tid - 272);
  const bool do_cs = tid >= 272 && tid < 283 && ccol < 2752;
  if (do_cs) {
#pragma unroll
    for (int kp = 0; kp < 8; ++kp) cpart[kp] = csp[kp * 2752 + ccol];
  }
  u32x4 a[8];
#pragma unroll
  for (int i = 0; i < 8; ++i) a[i] = *(const u32x4*)(pq + ((size_t)(mblk * 16 + kg * 8 + i) * 64 + lane) * 8);
  u32x2 xr[2];
#pragma unroll
  for (int j = 0; j < 2; ++j)
    xr[j] = *(const u32x2*)(x + (size_t)(mblk * 16 + r) * D_ + ct * 64 + (kg * 2 + j) * 16 + g * 4);
  if (tid < 272) {
    float s = 0.f;
#pragma unroll
    for (int p = 0; p < 16; ++p) s += pv[p];
    kv[tid] = s;
  }
  if (do_cs) csgu[ccol] = ((cpart[0] + cpart[1]) + (cpart[2] + cpart[3])) + ((cpart[4] + cpart[5]) + (cpart[6] + cpart[7]));
  __syncthreads();
  STAMP(2,1);
  if (tid < 128) {
    const int c = tid & 63, dh = tid >> 6;
    float o[8];
#pragma unroll
    for (int j = 0; j < 8; ++j) {
      float s = 0.f;
#pragma unroll
      for (int e4 = 0; e4 < 4; ++e4) {
        const f32x4 k4 = *(const f32x4*)(kv + (dh * 8 + j) * 16 + e4 * 4);
        s = fmaf(k4[0], w[e4 * 4], s); s = fmaf(k4[1], w[e4 * 4 + 1], s);
        s = fmaf(k4[2], w[e4 * 4 + 2], s); s = fmaf(k4[3], w[e4 * 4 + 3], s);
      }
      o[j] = s;
    }
    const int n = pc * 64 + c, nb = n >> 4, cc = n & 15;
    const int ks = ph >> 1, gg = (ph & 1) * 2 + dh;
    u32x4 v = {pack_bf(o[0], o[1]), pack_bf(o[2], o[3]), pack_bf(o[4], o[5]), pack_bf(o[6], o[7])};
    __builtin_amdgcn_raw_buffer_store_b128(v, wrs, (unsigned)((nb * 16 + ks) * 64 + gg * 16 + cc) * 16u, 0, 16);
  } else if (tid < 130) {
    const float* zs = kv + 256 + (tid - 128) * 8;
    u32x4 v = {pack_bf(zs[0], zs[1]), pack_bf(zs[2], zs[3]), pack_bf(zs[4], zs[5]), pack_bf(zs[6], zs[7])};
    __builtin_amdgcn_raw_buffer_store_b128(v, wrs, 524288u + (unsigned)(pc * 512 + ph * 16 + (tid - 128) * 8) * 2u, 0, 16);
  }
  WAIT_VM(0);
  __syncthreads();
  if (tid == 0) __hip_atomic_fetch_add((gu32*)(cnt + pc * 64), 1u, __ATOMIC_RELAXED, __HIP_MEMORY_SCOPE_AGENT);
  STAMP(2,2);
  unsigned touch = 0;
  if (wave >= 1) {
    const int l0 = idx * 344 + (wave - 1) * 64 + lane;
    if (wave <= 6 && (wave - 1) * 64 + lane < 344) touch += *(const unsigned*)((const char*)wd + (size_t)l0 * 128);
    const int t = (wave - 1) * 64 + lane;
    if (t < 384) {
      const int row = idx * 16 + (t >> 5 << 1 >> 1) % 16, m = (t / 12) & 1;
      const int rr = idx * 16 + (t / 24), ln = t % 12;
      const size_t off = (size_t)rr * (F_ * 2) + (xcd & 1) * 1536 + (size_t)ln * 128;
      const size_t lim = (size_t)rr * (F_ * 2) + F_ * 2 - 4;
      touch += *(const unsigned*)((const char*)(m ? wu : wg) + (off < lim ? off : lim));
      (void)row;
    }
  }
  if (wave == 0) {
    for (unsigned spins = 0; spins < (1u << 22); ++spins) {
      const unsigned v = __builtin_amdgcn_readfirstlane(
          __hip_atomic_load((gu32*)(cnt + ct * 64), __ATOMIC_RELAXED, __HIP_MEMORY_SCOPE_AGENT));
      if (v >= 32u) break;
      __builtin_amdgcn_s_sleep(1);
    }
  }
  __syncthreads();
  STAMP(2,3);
  u32x4 zf[8], wv[8];
#pragma unroll
  for (int i = 0; i < 8; ++i)
    zf[i] = __builtin_amdgcn_raw_buffer_load_b128(wrs, 524288u + (unsigned)(ct * 512 + (kg * 8 + i) * 32 + g * 8) * 2u, 0, 16);
#pragma unroll
  for (int i = 0; i < 8; ++i)
    wv[i] = __builtin_amdgcn_raw_buffer_load_b128(wrs, (unsigned)(ct * 65536 + (wave * 8 + i) * 1024 + lane * 16), 0, 16);
#pragma unroll
  for (int i = 0; i < 8; ++i) {
    float s = dot2(a[i][0], zf[i][0], 0.f);
    s = dot2(a[i][1], zf[i][1], s);
    s = dot2(a[i][2], zf[i][2], s);
    s = dot2(a[i][3], zf[i][3], s);
    const float inv = __builtin_amdgcn_rcpf(red16(s) + 1e-6f);
#pragma unroll
    for (int j = 0; j < 4; ++j) a[i][j] = pack_bf(bf_lo(a[i][j]) * inv, bf_hi(a[i][j]) * inv);
  }
#pragma unroll
  for (int i = 0; i < 8; ++i) *(u32x4*)(smem + (wave * 8 + i) * 1024 + lane * 16) = wv[i];
  __syncthreads();
  STAMP(2,4);
  f32x4 acc[4] = {};
#pragma unroll
  for (int i = 0; i < 8; ++i) {
#pragma unroll
    for (int nb = 0; nb < 4; ++nb) {
      const u32x4 b = *(const u32x4*)(smem + (nb * 16 + kg * 8 + i) * 1024 + lane * 16);
      acc[nb] = mfma16(b, a[i], acc[nb]);
    }
  }
  STAMP(2,5);
  f32x4* xch = (f32x4*)(smem + 65536);
#pragma unroll
  for (int j = 0; j < 2; ++j) xch[(wave * 2 + j) * 64 + lane] = kg ? acc[j] : acc[2 + j];
  __syncthreads();
  float hs = 0.f, hq = 0.f;
#pragma unroll
  for (int j = 0; j < 2; ++j) {
    const f32x4 o4 = (kg ? acc[2 + j] : acc[j]) + xch[((wave ^ 4) * 2 + j) * 64 + lane];
    u32x2 o;
    o[0] = pack_bf(bf_lo(xr[j][0]) + o4[0], bf_hi(xr[j][0]) + o4[1]);
    o[1] = pack_bf(bf_lo(xr[j][1]) + o4[2], bf_hi(xr[j][1]) + o4[3]);
    *(u32x2*)(hf + ((size_t)(mblk * 16 + ct * 2 + kg) * 64 + r + 16 * (2 * j + (g >> 1))) * 8 + (g & 1) * 4) = o;
    hs = dot2(o[0], 0x3f803f80u, hs); hs = dot2(o[1], 0x3f803f80u, hs);
    hq = dot2(o[0], o[0], hq); hq = dot2(o[1], o[1], hq);
  }
  hs = red32(red16(hs)); hq = red32(red16(hq));
  if (g == 0) {
    f32x2 v = {hs, hq};
    *(f32x2*)(hst + ((size_t)(mblk * 16 + r) * 16 + ct * 2 + kg) * 2) = v;
  }
  asm volatile("" :: "v"(touch));
  STAMP(2,6);
}

constexpr int K3_SLOT = 16384 + 24576, K3_NSLOT = 4;
__global__ __launch_bounds__(512) void k3_gateup(const u16* __restrict__ hf, const u16* __restrict__ wg,
                                                 const u16* __restrict__ wu, u16* __restrict__ hhf,
                                                 const float* __restrict__ hst, const float* __restrict__ csgu) {
  __shared__ __attribute__((aligned(16))) char smem[K3_SLOT * K3_NSLOT];
  const int tid = threadIdx.x, lane = tid & 63;
  const int wave = __builtin_amdgcn_readfirstlane(tid >> 6);
  const int xcd = blockIdx.x & 7, idx = blockIdx.x >> 3;
  const int rt = (xcd >> 1) * 4 + (idx >> 3);
  const int cg = (xcd & 1) * 8 + (idx & 7);
  const int r = lane & 15, g = lane >> 4;
  const int f0 = cg < 11 ? cg * 96 : 1056 + (cg - 11) * 64;
  const int nvalid = cg < 11 ? 6 : 4;
  STAMP(3,0);
  if (wave >= 4) {
    const int lw = wave - 4;
    const char* abase = (const char*)(hf + (size_t)((rt * 8 + lw * 2) * 16) * 512);
    const long wdelta = (const char*)wu - (const char*)wg;
    const char* bsrc[6];
#pragma unroll
    for (int i = 0; i < 6; ++i) {
      const int line = (lw * 6 + i) * 4 + g;
      const int kq = line / 6, cgrp = line % 6;
      const int k = kq * 4 + ((lane >> 2) & 3);
      const int ch = (lane & 3) ^ (((k >> 3) & 1) << 1);
      int c3 = cgrp < 3 ? cgrp : cgrp - 3;
      c3 = (c3 * 2 < nvalid) ? c3 : 0;
      bsrc[i] = (const char*)(wg + f0) + (unsigned)(k * F_ + c3 * 32 + ch * 8) * 2u + (cgrp >= 3 ? wdelta : 0);
    }
    auto glds16v = [&](const char* gsrc, unsigned dst) {
      asm volatile("s_mov_b32 m0, %1\n\ts_nop 0\n\tglobal_load_lds_dwordx4 %0, off" :: "v"(gsrc), "s"(dst) : "memory");
    };
    auto stage = [&](int s) {
      const unsigned slot = lds_addr(smem) + (s % K3_NSLOT) * K3_SLOT;
#pragma unroll
      for (int i = 0; i < 4; ++i)
        glds16(abase + (i >> 1) * 16384 + (s * 2 + (i & 1)) * 1024, lane * 16, slot + (lw * 4 + i) * 1024);
#pragma unroll
      for (int i = 0; i < 6; ++i) glds16v(bsrc[i] + (size_t)s * 64 * F_ * 2, slot + 16384 + (lw * 6 + i) * 1024);
    };
    stage(0); stage(1);
    wait_vm<10>();
    BARE_BAR();
    stage(2);
#pragma unroll
    for (int s = 0; s < 7; ++s) {
      if (s == 0 || s == 5) wait_vm<10>(); else if (s <= 4) wait_vm<20>(); else wait_vm<0>();
      BARE_BAR();
      if (s == 0) stage(3);
      if (s + 4 < 8) stage(s + 4);
    }
    return;
  }
  const int wr = wave >> 1, wc = wave & 1;
  const int q4 = (lane & 15) >> 2, p = lane & 3;
  const int aoff = wr * 8192 + lane * 16;
  const int bbase2 = 16384 + (2 * g) * 1536 + q4 * 64 + p * 8;
  int boff[3];
#pragma unroll
  for (int j = 0; j < 3; ++j) {
    const int fbl = 3 * wc + j;
    boff[j] = bbase2 + (fbl >> 1) * 256 + (((fbl & 1) ^ (g & 1)) << 5);
  }
  f32x4 hp[8];
  f32x4 cg4[3], cu4[3];
  float mean_l = 0.f, inv2_l = 0.f;
  f32x4 accg[4][3] = {}, accu[4][3] = {};
  u32x4 fa[2][4], fb[2][6];
  auto rdfrags = [&](int t) {
    const char* slot = smem + ((t >> 1) % K3_NSLOT) * K3_SLOT;
    const int kk = t & 1;
#pragma unroll
    for (int i = 0; i < 4; ++i) fa[kk][i] = *(const u32x4*)(slot + aoff + i * 2048 + kk * 1024);
#pragma unroll
    for (int j = 0; j < 3; ++j) {
      fb[kk][j] = tr_frag(slot + boff[j] + kk * 12288, slot + boff[j] + kk * 12288 + 1536);
      fb[kk][3 + j] = tr_frag(slot + boff[j] + 768 + kk * 12288, slot + boff[j] + 768 + kk * 12288 + 1536);
    }
  };
  auto compute = [&](int t) {
    const int kk = t & 1;
    const u32x4* a = fa[kk];
#pragma unroll
    for (int j = 0; j < 3; ++j) {
      const u32x4 bg = fb[kk][j], bu = fb[kk][3 + j];
#pragma unroll
      for (int i = 0; i < 4; ++i) {
        accg[i][j] = mfma16(bg, a[i], accg[i][j]);
        accu[i][j] = mfma16(bu, a[i], accu[i][j]);
      }
    }
  };
  BARE_BAR();
  STAMP(3,2);
  rdfrags(0);
#pragma unroll
  for (int s = 0; s < 8; ++s) {
    rdfrags(2 * s + 1);
    compute(2 * s);
    __builtin_amdgcn_sched_barrier(0);
    if (s + 1 < 8) {
      RAW_BAR();
      STAMP(3,3+s);
      rdfrags(2 * s + 2);
    }
    if (s == 4) {
#pragma unroll
      for (int i = 0; i < 8; ++i) hp[i] = *(const f32x4*)(hst + (size_t)(rt * 128 + wr * 64 + lane) * 32 + i * 4);
    }
    if (s == 6) {
      f32x4 t4 = (hp[0] + hp[1]) + (hp[2] + hp[3]) + ((hp[4] + hp[5]) + (hp[6] + hp[7]));
      mean_l = (t4[0] + t4[2]) * (1.f / 512.f);
      const float var = fmaxf((t4[1] + t4[3]) * (1.f / 512.f) - mean_l * mean_l, 0.f);
      inv2_l = __builtin_amdgcn_rcpf(var + 1e-5f);
      asm volatile("" : "+v"(mean_l), "+v"(inv2_l));
#pragma unroll
      for (int j = 0; j < 3; ++j) {
        const int fbl = (3 * wc + j) < nvalid ? (3 * wc + j) : 0;
        cg4[j] = *(const f32x4*)(csgu + f0 + fbl * 16 + 4 * g);
        cu4[j] = *(const f32x4*)(csgu + F_ + f0 + fbl * 16 + 4 * g);
      }
    }
    compute(2 * s + 1);
    __builtin_amdgcn_sched_barrier(0);
  }
  STAMP(3,11);
#pragma unroll
  for (int i = 0; i < 4; ++i) {
    const float mean = __shfl(mean_l, i * 16 + r), inv2 = __shfl(inv2_l, i * 16 + r);
    const int mblk = rt * 8 + wr * 4 + i;
#pragma unroll
    for (int j = 0; j < 3; ++j) {
      const int fbl = 3 * wc + j;
      float hv[4];
#pragma unroll
      for (int e = 0; e < 4; ++e)
        hv[e] = ((accg[i][j][e] - mean * cg4[j][e]) * inv2) * (accu[i][j][e] - mean * cu4[j][e]);
      if (fbl < nvalid) {
        const int fb_ = (f0 >> 4) + fbl;
        u32x2 o = {pack_bf(hv[0], hv[1]), pack_bf(hv[2], hv[3])};
        *(u32x2*)(hhf + ((size_t)(mblk * KS4 + (fb_ >> 1)) * 64 + r + 16 * (2 * (fb_ & 1) + (g >> 1))) * 8 + (g & 1) * 4) = o;
      }
    }
  }
  STAMP(3,13);
}

__global__ __launch_bounds__(512) void k4_down(const u16* __restrict__ hf, const u16* __restrict__ hhf,
                                               const u16* __restrict__ wd, u16* __restrict__ out) {
  __shared__ __attribute__((aligned(16))) char smem[4 * 32768];
  const int tid = threadIdx.x, lane = tid & 63;
  const int wave = __builtin_amdgcn_readfirstlane(tid >> 6);
  const int xcd = blockIdx.x & 7, idx = blockIdx.x >> 3;
  const int rt = xcd * 4 + (idx >> 3);
  const int ct = idx & 7;
  const int r = lane & 15, g = lane >> 4;
  STAMP(4,0);
  if (wave >= 4) {
    const int lw = wave - 4;
    const char* abase = (const char*)(hhf + (size_t)((rt * 4 + lw) * KS4) * 512);
    const char* bbase = (const char*)(wd + (size_t)lw * 32 * D_ + ct * 64);
    const unsigned boffs = (unsigned)(((g >> 1) * 4 + ((lane >> 2) & 3)) * D_ + (g & 1) * 32 + (lane & 3) * 8) * 2u;
    auto stage = [&](int s) {
      const unsigned slot = lds_addr(smem) + (s & 3) * 32768;
#pragma unroll
      for (int i = 0; i < 4; ++i)
        glds16(abase + (s * 4 + ((s == 10 && i == 3) ? 2 : i)) * 1024, lane * 16, slot + lw * 4096 + i * 1024);
      const int so = (s == 10 && lw == 3) ? s - 1 : s;
#pragma unroll
      for (int i = 0; i < 4; ++i)
        glds16(bbase + (size_t)(so * 128 + i * 8) * D_ * 2, (i & 1) ? (boffs ^ 32u) : boffs, slot + 16384 + lw * 4096 + i * 1024);
    };
    stage(0); stage(1);
    wait_vm<8>();
    BARE_BAR();
    stage(2);
#pragma unroll
    for (int s = 0; s < 10; ++s) {
      if (s == 0 || s == 8) wait_vm<8>(); else if (s <= 7) wait_vm<16>(); else wait_vm<0>();
      BARE_BAR();
      if (s == 0) stage(3);
      if (s + 4 < 11) stage(s + 4);
    }
    return;
  }
  const int wr = wave >> 1, wc = wave & 1;
  const int q4 = (lane & 15) >> 2, p = lane & 3;
  const int boff = 16384 + (2 * g) * 512 + wc * 256 + q4 * 64 + p * 8;
  const int aoff = wr * 8192 + lane * 16;
  u32x2 hr[2][2];
#pragma unroll
  for (int mb = 0; mb < 2; ++mb)
#pragma unroll
    for (int nb = 0; nb < 2; ++nb)
      hr[mb][nb] = *(const u32x2*)(hf + ((size_t)((rt * 4 + wr * 2 + mb) * 16 + ct * 2 + wc) * 64 + r + 16 * (2 * nb + (g >> 1))) * 8 + (g & 1) * 4);
  f32x4 acc[2][2] = {};
  u32x4 fa[2][2][2], fb[2][2][2];
  auto rdfrags = [&](int t) {
    const char* slot = smem + ((t >> 1) & 3) * 32768;
#pragma unroll
    for (int kk = 0; kk < 2; ++kk) {
      const int ksl = (t & 1) * 2 + kk;
#pragma unroll
      for (int mb = 0; mb < 2; ++mb) fa[t & 1][kk][mb] = *(const u32x4*)(slot + aoff + mb * 4096 + ksl * 1024);
#pragma unroll
      for (int nb = 0; nb < 2; ++nb) {
        const int ho = (nb ^ (g & 1)) << 5;
        fb[t & 1][kk][nb] = tr_frag(slot + boff + ksl * 4096 + ho, slot + boff + ksl * 4096 + 512 + ho);
      }
    }
  };
  auto compute = [&](int t) {
#pragma unroll
    for (int kk = 0; kk < 2; ++kk) {
      if (2 * t + kk < KS4) {
#pragma unroll
        for (int mb = 0; mb < 2; ++mb)
#pragma unroll
          for (int nb = 0; nb < 2; ++nb) acc[mb][nb] = mfma16(fb[t & 1][kk][nb], fa[t & 1][kk][mb], acc[mb][nb]);
      }
    }
  };
  BARE_BAR();
  STAMP(4,2);
  rdfrags(0);
#pragma unroll
  for (int s = 0; s < 11; ++s) {
    rdfrags(2 * s + 1);
    compute(2 * s);
    if (s + 1 < 11) {
      RAW_BAR();
      STAMP(4,3+s);
      rdfrags(2 * s + 2);
    }
    compute(2 * s + 1);
  }
#pragma unroll
  for (int mb = 0; mb < 2; ++mb)
#pragma unroll
    for (int nb = 0; nb < 2; ++nb) {
      const f32x4 o4 = acc[mb][nb];
      u32x2 o;
      o[0] = pack_bf(bf_lo(hr[mb][nb][0]) + o4[0], bf_hi(hr[mb][nb][0]) + o4[1]);
      o[1] = pack_bf(bf_lo(hr[mb][nb][1]) + o4[2], bf_hi(hr[mb][nb][1]) + o4[3]);
      *(u32x2*)(out + (size_t)(rt * 64 + wr * 32 + mb * 16 + r) * D_ + ct * 64 + wc * 32 + nb * 16 + g * 4) = o;
    }
  STAMP(4,14);
}

extern "C" void kernel_launch(void* const* d_in, const int* in_sizes, int n_in, void* d_out, int out_size,
                              void* d_ws, size_t ws_size, hipStream_t stream) {
  const u16* x = (const u16*)d_in[0];
  const u16* wqkv = (const u16*)d_in[1];
  const u16* wo = (const u16*)d_in[2];
  const u16* wg = (const u16*)d_in[4];
  const u16* wu = (const u16*)d_in[5];
  const u16* wd = (const u16*)d_in[6];
  u16* out = (u16*)d_out;
  char* ws = (char*)d_ws;
  u16* pq = (u16*)(ws);
  u16* hf = (u16*)(ws + (2u << 20));
  u16* wp = (u16*)(ws + (4u << 20));
  float* part = (float*)(ws + (5u << 20));
  unsigned* cnt = (unsigned*)(ws + (6u << 20));
  float* csgu = (float*)(ws + (6u << 20) + 4096);
  float* csp = (float*)(ws + (6u << 20) + 65536);
  float* hst = (float*)(ws + (7u << 20));
  u16* hhf = (u16*)(ws + (8u << 20));
  k1_qkv<<<256, 512, 0, stream>>>(x, wqkv, pq, part, wg, wu, csp, cnt, wo, wd, out);
  k2_attn_out<<<256, 512, 0, stream>>>(x, pq, part, wo, wp, cnt, hf, hst, csp, csgu, wg, wu, wd);
  k3_gateup<<<256, 512, 0, stream>>>(hf, wg, wu, hhf, hst, csgu);
  k4_down<<<256, 512, 0, stream>>>(hf, hhf, wd, out);
}
```

```cpp
#include <hip/hip_runtime.h>
#include <hip/hip_bf16.h>
#include <stdint.h>

typedef unsigned short u16;
typedef __attribute__((ext_vector_type(8))) __bf16 bf16x8;
typedef __attribute__((ext_vector_type(2))) __bf16 bf16x2;
typedef __attribute__((ext_vector_type(4))) short s16x4;
typedef __attribute__((ext_vector_type(4))) float f32x4;
typedef __attribute__((ext_vector_type(2))) float f32x2;
typedef __attribute__((ext_vector_type(4))) unsigned u32x4;
typedef __attribute__((ext_vector_type(2))) unsigned u32x2;
typedef __attribute__((address_space(3))) s16x4* lds_s16x4_ptr;

#define DI __device__ __forceinline__
#define STAMP(kid,i)

constexpr int S_ = 2048, D_ = 512, F_ = 1376, H_ = 32;
constexpr int KS4 = F_ / 32;

DI float bf_lo(unsigned u) { return __uint_as_float(u << 16); }
DI float bf_hi(unsigned u) { return __uint_as_float(u & 0xffff0000u); }
DI unsigned pack_bf(float lo, float hi) {
  f32x2 f = {lo, hi};
  bf16x2 r = __builtin_convertvector(f, bf16x2);
  return __builtin_bit_cast(unsigned, r);
}
DI float bfr(float f) { return bf_lo(pack_bf(f, 0.f) & 0xffffu); }
DI f32x4 mfma16(u32x4 a, u32x4 b, f32x4 c) {
  return __builtin_amdgcn_mfma_f32_16x16x32_bf16(__builtin_bit_cast(bf16x8, a),
                                                 __builtin_bit_cast(bf16x8, b), c, 0, 0, 0);
}
DI float dot2(unsigned a, unsigned b, float c) {
  return __builtin_amdgcn_fdot2_f32_bf16(__builtin_bit_cast(bf16x2, a), __builtin_bit_cast(bf16x2, b), c, false);
}
DI u32x4 tr_frag(const char* lds0, const char* lds1) {
  s16x4 lo = __builtin_amdgcn_ds_read_tr16_b64_v4i16((lds_s16x4_ptr)lds0);
  s16x4 hi = __builtin_amdgcn_ds_read_tr16_b64_v4i16((lds_s16x4_ptr)lds1);
  u32x2 l2 = __builtin_bit_cast(u32x2, lo), h2 = __builtin_bit_cast(u32x2, hi);
  u32x4 r = {l2[0], l2[1], h2[0], h2[1]};
  return r;
}
DI unsigned lds_addr(char* l) { return (unsigned)(size_t)(__attribute__((address_space(3))) char*)l; }
DI void glds16(const void* sbase, unsigned voff, unsigned dst) {
  asm volatile("s_mov_b32 m0, %2\n\ts_nop 0\n\tglobal_load_lds_dwordx4 %0, %1"
               :: "v"(voff), "s"(sbase), "s"(dst) : "memory");
}
DI float red16(float s) {
  const unsigned u = __float_as_uint(s);
  auto r = __builtin_amdgcn_permlane16_swap(u, u, false, false);
  return __uint_as_float(r[0]) + __uint_as_float(r[1]);
}
DI float red32(float s) {
  const unsigned u = __float_as_uint(s);
  auto r = __builtin_amdgcn_permlane32_swap(u, u, false, false);
  return __uint_as_float(r[0]) + __uint_as_float(r[1]);
}
#define WAIT_VM(n) asm volatile("s_waitcnt vmcnt(" #n ")" ::: "memory")
#define RAW_BAR() do { asm volatile("s_waitcnt lgkmcnt(0)" ::: "memory"); __builtin_amdgcn_s_barrier(); } while (0)
#define BARE_BAR() do { asm volatile("" ::: "memory"); __builtin_amdgcn_s_barrier(); } while (0)
template <int N> DI void wait_vm() { asm volatile("s_waitcnt vmcnt(%0)" ::"i"(N) : "memory"); }

DI float phi_f(float t) { return (t > 0.f ? t + 1.f : __expf(t)) + 1e-6f; }
DI void sq_frag(const u32x4& a, float& q) {
#pragma unroll
  for (int i = 0; i < 4; ++i) q = dot2(a[i], a[i], q);
}
#define ONES_FRAG ((u32x4){0x3f803f80u, 0x3f803f80u, 0x3f803f80u, 0x3f803f80u})

constexpr int K1_SLOT = 16384 + 12288, K1_NSLOT = 4;
extern "C" __global__ __launch_bounds__(512) void k1_qkv(const u16* __restrict__ x, const u16* __restrict__ wqkv,
                                              u16* __restrict__ pq, float* __restrict__ part,
                                              const u16* __restrict__ wg, const u16* __restrict__ wu,
                                              float* __restrict__ csp, unsigned* __restrict__ cnt,
                                              const u16* __restrict__ wo, const u16* __restrict__ wd,
                                              const u16* outp) {
  __shared__ __attribute__((aligned(16))) char smem[K1_SLOT * K1_NSLOT];
  const int tid = threadIdx.x, lane = tid & 63;
  const int wave = __builtin_amdgcn_readfirstlane(tid >> 6);
  const int xcd = blockIdx.x & 7, idx = blockIdx.x >> 3;
  const int rt = (xcd >> 1) * 4 + (idx >> 3);
  const int cg = (xcd & 1) * 8 + (idx & 7);
  const int r = lane & 15, g = lane >> 4;
  STAMP(0,0);
  if (blockIdx.x == 0 && tid < 8) cnt[tid * 64] = 0u;
  if (wave >= 4) {
    const int lw = wave - 4;
    const unsigned lbase = lds_addr(smem);
    const unsigned aoffs = (unsigned)(lw * 32 + (lane >> 3)) * 1024u + (((lane & 7) ^ ((lane >> 4) & 3)) << 4);
    unsigned boffs[3];
#pragma unroll
    for (int i = 0; i < 3; ++i) {
      const int line = (lw * 3 + i) * 4 + g;
      const int kq = line / 3, cgrp = line % 3;
      const int k = kq * 4 + ((lane >> 2) & 3);
      const int ch = (lane & 3) ^ (((k >> 3) & 1) << 1);
      boffs[i] = (unsigned)(k * 1536 + cgrp * 512 + ch * 8) * 2u;
    }
    const char* abase = (const char*)(x + (size_t)rt * 128 * D_);
    const char* bbase = (const char*)(wqkv + cg * 32);
    auto stage = [&](int s) {
      const unsigned slot = lbase + (s % K1_NSLOT) * K1_SLOT;
#pragma unroll
      for (int i = 0; i < 4; ++i)
        glds16(abase + s * 128 + i * 8192, (i & 1) ? (aoffs ^ 64u) : aoffs, slot + lw * 4096 + i * 1024);
#pragma unroll
      for (int i = 0; i < 3; ++i)
        glds16(bbase + (size_t)s * 64 * 3072, boffs[i], slot + 16384 + (lw * 3 + i) * 1024);
    };
    stage(0); stage(1);
    wait_vm<7>();
    BARE_BAR();
    stage(2);
#pragma unroll
    for (int s = 0; s < 7; ++s) {
      if (s == 0 || s == 5) wait_vm<7>(); else if (s <= 4) wait_vm<14>(); else wait_vm<0>();
      BARE_BAR();
      if (s == 0) stage(3);
      if (s + 4 < 8) stage(s + 4);
    }
    const int W = blockIdx.x * 4 + lw;
    const int grp = W % 86, kp = (W / 86) & 7;
    u32x4 wv[4];
    {
      const u16* wsrc = (grp < 43 ? wg : wu) + (grp % 43) * 32 + (lane & 3) * 8 + (size_t)(kp * 64 + (lane >> 2)) * F_;
#pragma unroll
      for (int i = 0; i < 4; ++i) wv[i] = *(const u32x4*)(wsrc + (size_t)i * 16 * F_);
    }
    unsigned touch;
    {
      const int T = idx * 256 + lw * 64 + lane;
      const char* tp = (const char*)pq + (size_t)T * 4096;
      if (T >= 3584) tp = (const char*)outp + (size_t)(T - 3584) * 4096;
      if (T >= 4096) tp = (const char*)wo + (size_t)(T - 4096) * 4096;
      if (T >= 4224) tp = (const char*)wd + (size_t)(T - 4224) * 4096;
      if (T >= 4568) tp = (const char*)wg + (size_t)(T - 4568) * 4096;
      if (T >= 4912) tp = (const char*)wu + (size_t)(T - 4912) * 4096;
      if (T >= 5256) tp = (const char*)wu;
      touch = *(const volatile unsigned*)tp;
      unsigned long long pc; unsigned d0, d1;
      asm volatile("s_getpc_b64 %0\n.Lpc_anchor_%=:\n\ts_mov_b32 %1, k2_attn_out-.Lpc_anchor_%=\n\ts_mov_b32 %2, k4_down-.Lpc_anchor_%="
                   : "=s"(pc), "=s"(d0), "=s"(d1));
      const unsigned off = d0 + (unsigned)(lw * 64 + lane) * 128u, lim = d1 + 8192u - 128u;
      touch += *(const volatile unsigned*)((const char*)pc + (off < lim ? off : lim));
    }
    BARE_BAR();
    if (W < 86 * 8) {
      float c8[8];
#pragma unroll
      for (int j = 0; j < 4; ++j) {
        c8[2 * j] = (bf_lo(wv[0][j]) + bf_lo(wv[1][j])) + (bf_lo(wv[2][j]) + bf_lo(wv[3][j]));
        c8[2 * j + 1] = (bf_hi(wv[0][j]) + bf_hi(wv[1][j])) + (bf_hi(wv[2][j]) + bf_hi(wv[3][j]));
      }
#pragma unroll
      for (int j = 0; j < 8; ++j) {
        float v = c8[j];
        v += __shfl_xor(v, 4); v += __shfl_xor(v, 8);
        c8[j] = red32(red16(v));
      }
      if (lane < 4) {
        float* dst = csp + kp * 2752 + grp * 32 + lane * 8;
        f32x4 v0 = {c8[0], c8[1], c8[2], c8[3]}, v1 = {c8[4], c8[5], c8[6], c8[7]};
        *(f32x4*)dst = v0; *(f32x4*)(dst + 4) = v1;
      }
    }
    asm volatile("" :: "v"(touch));
    BARE_BAR();
    return;
  }
  const int wr = wave >> 1, wc = wave & 1;
  const int q4 = (lane & 15) >> 2, p = lane & 3;
  const int aoff = (wr * 64 + r) * 128 + ((g ^ ((r >> 1) & 7)) << 4);
  const int aoff0 = aoff + wc * 4096, aoff1 = aoff + (wc ^ 1) * 4096;
  const int boff = 16384 + (2 * g) * 768 + q4 * 64 + ((wc ^ (g & 1)) << 5) + p * 8;
  const u32x4 ones = ONES_FRAG;
  f32x4 accq[4] = {}, acck[4] = {}, accv[4] = {};
  f32x4 rs[2] = {}, sqm[2] = {}, cq = {}, ck = {}, cv = {};
  u32x4 fa[2][4], fb[2][3];
  auto rdfrags = [&](int t) {
    const char* slot = smem + ((t >> 1) % K1_NSLOT) * K1_SLOT;
    const int kk = t & 1;
#pragma unroll
    for (int i = 0; i < 4; ++i)
      fa[kk][i] = *(const u32x4*)(slot + (((i < 2 ? aoff0 : aoff1) + (i & 1) * 2048) ^ (kk << 6)));
#pragma unroll
    for (int j = 0; j < 3; ++j)
      fb[kk][j] = tr_frag(slot + boff + kk * 6144 + j * 256, slot + boff + kk * 6144 + 768 + j * 256);
  };
  auto compute = [&](int t) {
    const int kk = t & 1;
    const u32x4* a = fa[kk];
    const u32x4 bq = fb[kk][0], bk = fb[kk][1], bv = fb[kk][2];
#pragma unroll
    for (int i = 0; i < 4; ++i) {
      accq[i] = mfma16(bq, a[i], accq[i]);
      acck[i] = mfma16(a[i], bk, acck[i]);
      accv[i] = mfma16(a[i], bv, accv[i]);
    }
    rs[0] = mfma16(ones, a[0], rs[0]);
    rs[1] = mfma16(ones, a[1], rs[1]);
    sqm[0] = mfma16(a[0], a[0], sqm[0]);
    sqm[1] = mfma16(a[1], a[1], sqm[1]);
    if (kk == wr) {
      cq = mfma16(bq, ones, cq);
      ck = mfma16(ones, bk, ck);
      cv = mfma16(ones, bv, cv);
    }
  };
  BARE_BAR();
  STAMP(0,2);
  rdfrags(0);
#pragma unroll
  for (int s = 0; s < 8; ++s) {
    rdfrags(2 * s + 1);
    compute(2 * s);
    __builtin_amdgcn_sched_barrier(0);
    if (s + 1 < 8) {
      RAW_BAR();
      STAMP(0,3+s);
      rdfrags(2 * s + 2);
    }
    compute(2 * s + 1);
    __builtin_amdgcn_sched_barrier(0);
  }
  STAMP(0,11);
  float* st = (float*)(smem);
  float* csl = (float*)(smem + 2048);
  float* st2 = (float*)(smem + 4096);
  float* red = (float*)(smem + 8192);
  {
#pragma unroll
    for (int i = 0; i < 2; ++i) {
      const float q01 = (r & 1) ? sqm[i][1] : sqm[i][0], q23 = (r & 1) ? sqm[i][3] : sqm[i][2];
      const float q = (r & 2) ? q23 : q01;
      if (g == (r >> 2)) {
        f32x2 v = {rs[i][0], q};
        *(f32x2*)(st + (wr * 64 + wc * 32 + i * 16 + r) * 2) = v;
      }
    }
    float* c = csl + (wr * 2 + wc) * 48;
    if (r == 0) *(f32x4*)(c + 4 * g) = cq;
    if (g == 0) { c[16 + r] = ck[0]; c[32 + r] = cv[0]; }
  }
  __syncthreads();
  STAMP(0,12);
  {
    const f32x2 a0 = *(const f32x2*)(st + (wr * 64 + lane) * 2);
    const float mean = a0[0] * (1.f / 512.f);
    const float var = fmaxf(a0[1] * (1.f / 512.f) - mean * mean, 0.f);
    f32x2 v = {mean, __builtin_amdgcn_rsqf(var + 1e-5f)};
    *(f32x2*)(st2 + (wave * 64 + lane) * 2) = v;
  }
  const f32x4 csq4 = *(const f32x4*)(csl + wc * 48 + 4 * g) + *(const f32x4*)(csl + (2 + wc) * 48 + 4 * g);
  const float csk = csl[wc * 48 + 16 + r] + csl[(2 + wc) * 48 + 16 + r];
  const float csv = csl[wc * 48 + 32 + r] + csl[(2 + wc) * 48 + 32 + r];
  f32x4 kv = {}, zz = {};
#pragma unroll
  for (int ih = 0; ih < 2; ++ih) {
    u32x4 pk, vv;
#pragma unroll
    for (int i2 = 0; i2 < 2; ++i2) {
      const int i = ih * 2 + i2;
      const int mbl = ((ih ^ wc) << 1) + i2;
      {
        const f32x2 mi = *(const f32x2*)(st2 + (wave * 64 + mbl * 16 + r) * 2);
        float qv[4];
#pragma unroll
        for (int j = 0; j < 4; ++j) qv[j] = phi_f((accq[i][j] - mi[0] * csq4[j]) * mi[1]);
        u32x2 o = {pack_bf(qv[0], qv[1]), pack_bf(qv[2], qv[3])};
        const int mblk = rt * 8 + wr * 4 + mbl;
        *(u32x2*)(pq + ((size_t)(mblk * 16 + cg) * 64 + r + 16 * (2 * wc + (g >> 1))) * 8 + (g & 1) * 4) = o;
      }
      {
        const f32x4 m0 = *(const f32x4*)(st2 + (wave * 64 + mbl * 16 + 4 * g) * 2);
        const f32x4 m1 = *(const f32x4*)(st2 + (wave * 64 + mbl * 16 + 4 * g) * 2 + 4);
        const float mean[4] = {m0[0], m0[2], m1[0], m1[2]}, inv[4] = {m0[1], m0[3], m1[1], m1[3]};
        float kk[4], vj[4];
#pragma unroll
        for (int j = 0; j < 4; ++j) {
          kk[j] = phi_f((acck[i][j] - mean[j] * csk) * inv[j]);
          vj[j] = (accv[i][j] - mean[j] * csv) * inv[j];
        }
        pk[2 * i2] = pack_bf(kk[0], kk[1]);
        pk[2 * i2 + 1] = pack_bf(kk[2], kk[3]);
        vv[2 * i2] = pack_bf(vj[0], vj[1]);
        vv[2 * i2 + 1] = pack_bf(vj[2], vj[3]);
      }
    }
    kv = mfma16(pk, vv, kv);
    zz = mfma16(pk, ones, zz);
  }
  {
    float* rd = red + (wc * 2 + wr) * 272;
#pragma unroll
    for (int j = 0; j < 4; ++j) rd[(4 * g + j) * 16 + r] = kv[j];
    if (r == 0) *(f32x4*)(rd + 256 + 4 * g) = zz;
  }
  __syncthreads();
  for (int i = tid; i < 544; i += 256) {
    const int hh = i >= 272 ? 1 : 0, e = i - hh * 272;
    const float* rr = red + hh * 2 * 272 + e;
    part[(size_t)(rt * 32 + cg * 2 + hh) * 272 + e] = rr[0] + rr[272];
  }
  STAMP(0,13);
}

typedef __attribute__((address_space(1))) unsigned gu32;
extern "C" __global__ __launch_bounds__(512) void k2_attn_out(const u16* __restrict__ x, const u16* __restrict__ pq,
                                                   const float* __restrict__ part, const u16* __restrict__ wo,
                                                   u16* wp, unsigned* cnt,
                                                   u16* __restrict__ hf, float* __restrict__ hst,
                                                   const float* __restrict__ csp, float* __restrict__ csgu,
                                                   const u16* __restrict__ wg, const u16* __restrict__ wu,
                                                   const u16* __restrict__ wd) {
  __shared__ __attribute__((aligned(16))) char smem[65536 + 16384 + 1088];
  const int tid = threadIdx.x, lane = tid & 63;
  const int wave = __builtin_amdgcn_readfirstlane(tid >> 6);
  const int xcd = blockIdx.x & 7, idx = blockIdx.x >> 3;
  const int rt = xcd * 4 + (idx >> 3);
  const int ct = idx & 7;
  const int kg = wave >> 2, rb = wave & 3;
  const int r = lane & 15, g = lane >> 4;
  const int mblk = rt * 4 + rb;
  const __amdgpu_buffer_rsrc_t wrs = __builtin_amdgcn_make_buffer_rsrc(wp, 0, 524288 + 8192, 0x00020000);
  STAMP(2,0);
  const int ph = blockIdx.x >> 3, pc = blockIdx.x & 7;
  float* kv = (float*)(smem + 65536 + 16384);
  float pv[16];
  if (tid < 272) {
#pragma unroll
    for (int p = 0; p < 16; ++p) pv[p] = part[(size_t)(p * 32 + ph) * 272 + tid];
  }
  float w[16];
  if (tid < 128) {
#pragma unroll
    for (int e = 0; e < 16; ++e) w[e] = bf_lo(wo[(size_t)(ph * 16 + e) * D_ + pc * 64 + (tid & 63)]);
  }
  float cpart[8];
  const int ccol = blockIdx.x * 11 + (tid - 272);
  const bool do_cs = tid >= 272 && tid < 283 && ccol < 2752;
  if (do_cs) {
#pragma unroll
    for (int kp = 0; kp < 8; ++kp) cpart[kp] = csp[kp * 2752 + ccol];
  }
  u32x4 a[8];
#pragma unroll
  for (int i = 0; i < 8; ++i) a[i] = *(const u32x4*)(pq + ((size_t)(mblk * 16 + kg * 8 + i) * 64 + lane) * 8);
  u32x2 xr[2];
#pragma unroll
  for (int j = 0; j < 2; ++j)
    xr[j] = *(const u32x2*)(x + (size_t)(mblk * 16 + r) * D_ + ct * 64 + (kg * 2 + j) * 16 + g * 4);
  if (tid < 272) {
    float s = 0.f;
#pragma unroll
    for (int p = 0; p < 16; ++p) s += pv[p];
    kv[tid] = s;
  }
  if (do_cs) csgu[ccol] = ((cpart[0] + cpart[1]) + (cpart[2] + cpart[3])) + ((cpart[4] + cpart[5]) + (cpart[6] + cpart[7]));
  __syncthreads();
  STAMP(2,1);
  if (tid < 128) {
    const int c = tid & 63, dh = tid >> 6;
    float o[8];
#pragma unroll
    for (int j = 0; j < 8; ++j) {
      float s = 0.f;
#pragma unroll
      for (int e4 = 0; e4 < 4; ++e4) {
        const f32x4 k4 = *(const f32x4*)(kv + (dh * 8 + j) * 16 + e4 * 4);
        s = fmaf(k4[0], w[e4 * 4], s); s = fmaf(k4[1], w[e4 * 4 + 1], s);
        s = fmaf(k4[2], w[e4 * 4 + 2], s); s = fmaf(k4[3], w[e4 * 4 + 3], s);
      }
      o[j] = s;
    }
    const int n = pc * 64 + c, nb = n >> 4, cc = n & 15;
    const int ks = ph >> 1, gg = (ph & 1) * 2 + dh;
    u32x4 v = {pack_bf(o[0], o[1]), pack_bf(o[2], o[3]), pack_bf(o[4], o[5]), pack_bf(o[6], o[7])};
    __builtin_amdgcn_raw_buffer_store_b128(v, wrs, (unsigned)((nb * 16 + ks) * 64 + gg * 16 + cc) * 16u, 0, 16);
  } else if (tid < 130) {
    const float* zs = kv + 256 + (tid - 128) * 8;
    u32x4 v = {pack_bf(zs[0], zs[1]), pack_bf(zs[2], zs[3]), pack_bf(zs[4], zs[5]), pack_bf(zs[6], zs[7])};
    __builtin_amdgcn_raw_buffer_store_b128(v, wrs, 524288u + (unsigned)(pc * 512 + ph * 16 + (tid - 128) * 8) * 2u, 0, 16);
  }
  WAIT_VM(0);
  __syncthreads();
  if (tid == 0) __hip_atomic_fetch_add((gu32*)(cnt + pc * 64), 1u, __ATOMIC_RELAXED, __HIP_MEMORY_SCOPE_AGENT);
  STAMP(2,2);
  unsigned touch = 0;
  if (wave >= 1) {
    const int l0 = idx * 344 + (wave - 1) * 64 + lane;
    if (wave <= 6 && (wave - 1) * 64 + lane < 344) touch += *(const unsigned*)((const char*)wd + (size_t)l0 * 128);
    const int t = (wave - 1) * 64 + lane;
    if (t < 384) {
      const int row = idx * 16 + (t >> 5 << 1 >> 1) % 16, m = (t / 12) & 1;
      const int rr = idx * 16 + (t / 24), ln = t % 12;
      const size_t off = (size_t)rr * (F_ * 2) + (xcd & 1) * 1536 + (size_t)ln * 128;
      const size_t lim = (size_t)rr * (F_ * 2) + F_ * 2 - 4;
      touch += *(const unsigned*)((const char*)(m ? wu : wg) + (off < lim ? off : lim));
      (void)row;
    }
  }
  if (wave == 0) {
    for (unsigned spins = 0; spins < (1u << 22); ++spins) {
      const unsigned v = __builtin_amdgcn_readfirstlane(
          __hip_atomic_load((gu32*)(cnt + ct * 64), __ATOMIC_RELAXED, __HIP_MEMORY_SCOPE_AGENT));
      if (v >= 32u) break;
      __builtin_amdgcn_s_sleep(1);
    }
  }
  __syncthreads();
  STAMP(2,3);
  u32x4 zf[8], wv[8];
#pragma unroll
  for (int i = 0; i < 8; ++i)
    zf[i] = __builtin_amdgcn_raw_buffer_load_b128(wrs, 524288u + (unsigned)(ct * 512 + (kg * 8 + i) * 32 + g * 8) * 2u, 0, 16);
#pragma unroll
  for (int i = 0; i < 8; ++i)
    wv[i] = __builtin_amdgcn_raw_buffer_load_b128(wrs, (unsigned)(ct * 65536 + (wave * 8 + i) * 1024 + lane * 16), 0, 16);
#pragma unroll
  for (int i = 0; i < 8; ++i) {
    float s = dot2(a[i][0], zf[i][0], 0.f);
    s = dot2(a[i][1], zf[i][1], s);
    s = dot2(a[i][2], zf[i][2], s);
    s = dot2(a[i][3], zf[i][3], s);
    const float inv = __builtin_amdgcn_rcpf(red16(s) + 1e-6f);
#pragma unroll
    for (int j = 0; j < 4; ++j) a[i][j] = pack_bf(bf_lo(a[i][j]) * inv, bf_hi(a[i][j]) * inv);
  }
#pragma unroll
  for (int i = 0; i < 8; ++i) *(u32x4*)(smem + (wave * 8 + i) * 1024 + lane * 16) = wv[i];
  __syncthreads();
  STAMP(2,4);
  f32x4 acc[4] = {};
#pragma unroll
  for (int i = 0; i < 8; ++i) {
#pragma unroll
    for (int nb = 0; nb < 4; ++nb) {
      const u32x4 b = *(const u32x4*)(smem + (nb * 16 + kg * 8 + i) * 1024 + lane * 16);
      acc[nb] = mfma16(b, a[i], acc[nb]);
    }
  }
  STAMP(2,5);
  f32x4* xch = (f32x4*)(smem + 65536);
#pragma unroll
  for (int j = 0; j < 2; ++j) xch[(wave * 2 + j) * 64 + lane] = kg ? acc[j] : acc[2 + j];
  __syncthreads();
  float hs = 0.f, hq = 0.f;
#pragma unroll
  for (int j = 0; j < 2; ++j) {
    const f32x4 o4 = (kg ? acc[2 + j] : acc[j]) + xch[((wave ^ 4) * 2 + j) * 64 + lane];
    u32x2 o;
    o[0] = pack_bf(bf_lo(xr[j][0]) + o4[0], bf_hi(xr[j][0]) + o4[1]);
    o[1] = pack_bf(bf_lo(xr[j][1]) + o4[2], bf_hi(xr[j][1]) + o4[3]);
    *(u32x2*)(hf + ((size_t)(mblk * 16 + ct * 2 + kg) * 64 + r + 16 * (2 * j + (g >> 1))) * 8 + (g & 1) * 4) = o;
    hs = dot2(o[0], 0x3f803f80u, hs); hs = dot2(o[1], 0x3f803f80u, hs);
    hq = dot2(o[0], o[0], hq); hq = dot2(o[1], o[1], hq);
  }
  hs = red32(red16(hs)); hq = red32(red16(hq));
  if (g == 0) {
    f32x2 v = {hs, hq};
    *(f32x2*)(hst + ((size_t)(mblk * 16 + r) * 16 + ct * 2 + kg) * 2) = v;
  }
  asm volatile("" :: "v"(touch));
  STAMP(2,6);
}

constexpr int K3_SLOT = 16384 + 24576, K3_NSLOT = 4;
extern "C" __global__ __launch_bounds__(512) void k3_gateup(const u16* __restrict__ hf, const u16* __restrict__ wg,
                                                 const u16* __restrict__ wu, u16* __restrict__ hhf,
                                                 const float* __restrict__ hst, const float* __restrict__ csgu) {
  __shared__ __attribute__((aligned(16))) char smem[K3_SLOT * K3_NSLOT];
  const int tid = threadIdx.x, lane = tid & 63;
  const int wave = __builtin_amdgcn_readfirstlane(tid >> 6);
  const int xcd = blockIdx.x & 7, idx = blockIdx.x >> 3;
  const int rt = (xcd >> 1) * 4 + (idx >> 3);
  const int cg = (xcd & 1) * 8 + (idx & 7);
  const int r = lane & 15, g = lane >> 4;
  const int f0 = cg < 11 ? cg * 96 : 1056 + (cg - 11) * 64;
  const int nvalid = cg < 11 ? 6 : 4;
  STAMP(3,0);
  if (wave >= 4) {
    const int lw = wave - 4;
    const char* abase = (const char*)(hf + (size_t)((rt * 8 + lw * 2) * 16) * 512);
    const long wdelta = (const char*)wu - (const char*)wg;
    const char* bsrc[6];
#pragma unroll
    for (int i = 0; i < 6; ++i) {
      const int line = (lw * 6 + i) * 4 + g;
      const int kq = line / 6, cgrp = line % 6;
      const int k = kq * 4 + ((lane >> 2) & 3);
      const int ch = (lane & 3) ^ (((k >> 3) & 1) << 1);
      int c3 = cgrp < 3 ? cgrp : cgrp - 3;
      c3 = (c3 * 2 < nvalid) ? c3 : 0;
      bsrc[i] = (const char*)(wg + f0) + (unsigned)(k * F_ + c3 * 32 + ch * 8) * 2u + (cgrp >= 3 ? wdelta : 0);
    }
    auto glds16v = [&](const char* gsrc, unsigned dst) {
      asm volatile("s_mov_b32 m0, %1\n\ts_nop 0\n\tglobal_load_lds_dwordx4 %0, off" :: "v"(gsrc), "s"(dst) : "memory");
    };
    auto stage = [&](int s) {
      const unsigned slot = lds_addr(smem) + (s % K3_NSLOT) * K3_SLOT;
#pragma unroll
      for (int i = 0; i < 4; ++i)
        glds16(abase + (i >> 1) * 16384 + (s * 2 + (i & 1)) * 1024, lane * 16, slot + (lw * 4 + i) * 1024);
#pragma unroll
      for (int i = 0; i < 6; ++i) glds16v(bsrc[i] + (size_t)s * 64 * F_ * 2, slot + 16384 + (lw * 6 + i) * 1024);
    };
    stage(0); stage(1);
    wait_vm<10>();
    BARE_BAR();
    stage(2);
#pragma unroll
    for (int s = 0; s < 7; ++s) {
      if (s == 0 || s == 5) wait_vm<10>(); else if (s <= 4) wait_vm<20>(); else wait_vm<0>();
      BARE_BAR();
      if (s == 0) stage(3);
      if (s + 4 < 8) stage(s + 4);
    }
    return;
  }
  const int wr = wave >> 1, wc = wave & 1;
  const int q4 = (lane & 15) >> 2, p = lane & 3;
  const int aoff = wr * 8192 + lane * 16;
  const int bbase2 = 16384 + (2 * g) * 1536 + q4 * 64 + p * 8;
  int boff[3];
#pragma unroll
  for (int j = 0; j < 3; ++j) {
    const int fbl = 3 * wc + j;
    boff[j] = bbase2 + (fbl >> 1) * 256 + (((fbl & 1) ^ (g & 1)) << 5);
  }
  f32x4 hp[8];
  f32x4 cg4[3], cu4[3];
  float mean_l = 0.f, inv2_l = 0.f;
  f32x4 accg[4][3] = {}, accu[4][3] = {};
  u32x4 fa[2][4], fb[2][6];
  auto rdfrags = [&](int t) {
    const char* slot = smem + ((t >> 1) % K3_NSLOT) * K3_SLOT;
    const int kk = t & 1;
#pragma unroll
    for (int i = 0; i < 4; ++i) fa[kk][i] = *(const u32x4*)(slot + aoff + i * 2048 + kk * 1024);
#pragma unroll
    for (int j = 0; j < 3; ++j) {
      fb[kk][j] = tr_frag(slot + boff[j] + kk * 12288, slot + boff[j] + kk * 12288 + 1536);
      fb[kk][3 + j] = tr_frag(slot + boff[j] + 768 + kk * 12288, slot + boff[j] + 768 + kk * 12288 + 1536);
    }
  };
  auto compute = [&](int t) {
    const int kk = t & 1;
    const u32x4* a = fa[kk];
#pragma unroll
    for (int j = 0; j < 3; ++j) {
      const u32x4 bg = fb[kk][j], bu = fb[kk][3 + j];
#pragma unroll
      for (int i = 0; i < 4; ++i) {
        accg[i][j] = mfma16(bg, a[i], accg[i][j]);
        accu[i][j] = mfma16(bu, a[i], accu[i][j]);
      }
    }
  };
  BARE_BAR();
  STAMP(3,2);
  rdfrags(0);
#pragma unroll
  for (int s = 0; s < 8; ++s) {
    rdfrags(2 * s + 1);
    compute(2 * s);
    __builtin_amdgcn_sched_barrier(0);
    if (s + 1 < 8) {
      RAW_BAR();
      STAMP(3,3+s);
      rdfrags(2 * s + 2);
    }
    if (s == 4) {
#pragma unroll
      for (int i = 0; i < 8; ++i) hp[i] = *(const f32x4*)(hst + (size_t)(rt * 128 + wr * 64 + lane) * 32 + i * 4);
    }
    if (s == 6) {
      f32x4 t4 = (hp[0] + hp[1]) + (hp[2] + hp[3]) + ((hp[4] + hp[5]) + (hp[6] + hp[7]));
      mean_l = (t4[0] + t4[2]) * (1.f / 512.f);
      const float var = fmaxf((t4[1] + t4[3]) * (1.f / 512.f) - mean_l * mean_l, 0.f);
      inv2_l = __builtin_amdgcn_rcpf(var + 1e-5f);
      asm volatile("" : "+v"(mean_l), "+v"(inv2_l));
#pragma unroll
      for (int j = 0; j < 3; ++j) {
        const int fbl = (3 * wc + j) < nvalid ? (3 * wc + j) : 0;
        cg4[j] = *(const f32x4*)(csgu + f0 + fbl * 16 + 4 * g);
        cu4[j] = *(const f32x4*)(csgu + F_ + f0 + fbl * 16 + 4 * g);
      }
    }
    compute(2 * s + 1);
    __builtin_amdgcn_sched_barrier(0);
  }
  STAMP(3,11);
#pragma unroll
  for (int i = 0; i < 4; ++i) {
    const float mean = __shfl(mean_l, i * 16 + r), inv2 = __shfl(inv2_l, i * 16 + r);
    const int mblk = rt * 8 + wr * 4 + i;
#pragma unroll
    for (int j = 0; j < 3; ++j) {
      const int fbl = 3 * wc + j;
      float hv[4];
#pragma unroll
      for (int e = 0; e < 4; ++e)
        hv[e] = ((accg[i][j][e] - mean * cg4[j][e]) * inv2) * (accu[i][j][e] - mean * cu4[j][e]);
      if (fbl < nvalid) {
        const int fb_ = (f0 >> 4) + fbl;
        u32x2 o = {pack_bf(hv[0], hv[1]), pack_bf(hv[2], hv[3])};
        *(u32x2*)(hhf + ((size_t)(mblk * KS4 + (fb_ >> 1)) * 64 + r + 16 * (2 * (fb_ & 1) + (g >> 1))) * 8 + (g & 1) * 4) = o;
      }
    }
  }
  STAMP(3,13);
}

extern "C" __global__ __launch_bounds__(512) void k4_down(const u16* __restrict__ hf, const u16* __restrict__ hhf,
                                               const u16* __restrict__ wd, u16* __restrict__ out) {
  __shared__ __attribute__((aligned(16))) char smem[4 * 32768];
  const int tid = threadIdx.x, lane = tid & 63;
  const int wave = __builtin_amdgcn_readfirstlane(tid >> 6);
  const int xcd = blockIdx.x & 7, idx = blockIdx.x >> 3;
  const int rt = xcd * 4 + (idx >> 3);
  const int ct = idx & 7;
  const int r = lane & 15, g = lane >> 4;
  STAMP(4,0);
  if (wave >= 4) {
    const int lw = wave - 4;
    const char* abase = (const char*)(hhf + (size_t)((rt * 4 + lw) * KS4) * 512);
    const char* bbase = (const char*)(wd + (size_t)lw * 32 * D_ + ct * 64);
    const unsigned boffs = (unsigned)(((g >> 1) * 4 + ((lane >> 2) & 3)) * D_ + (g & 1) * 32 + (lane & 3) * 8) * 2u;
    auto stage = [&](int s) {
      const unsigned slot = lds_addr(smem) + (s & 3) * 32768;
#pragma unroll
      for (int i = 0; i < 4; ++i)
        glds16(abase + (s * 4 + ((s == 10 && i == 3) ? 2 : i)) * 1024, lane * 16, slot + lw * 4096 + i * 1024);
      const int so = (s == 10 && lw == 3) ? s - 1 : s;
#pragma unroll
      for (int i = 0; i < 4; ++i)
        glds16(bbase + (size_t)(so * 128 + i * 8) * D_ * 2, (i & 1) ? (boffs ^ 32u) : boffs, slot + 16384 + lw * 4096 + i * 1024);
    };
    stage(0); stage(1);
    wait_vm<8>();
    BARE_BAR();
    stage(2);
#pragma unroll
    for (int s = 0; s < 10; ++s) {
      if (s == 0 || s == 8) wait_vm<8>(); else if (s <= 7) wait_vm<16>(); else wait_vm<0>();
      BARE_BAR();
      if (s == 0) stage(3);
      if (s + 4 < 11) stage(s + 4);
    }
    return;
  }
  const int wr = wave >> 1, wc = wave & 1;
  const int q4 = (lane & 15) >> 2, p = lane & 3;
  const int boff = 16384 + (2 * g) * 512 + wc * 256 + q4 * 64 + p * 8;
  const int aoff = wr * 8192 + lane * 16;
  u32x2 hr[2][2];
#pragma unroll
  for (int mb = 0; mb < 2; ++mb)
#pragma unroll
    for (int nb = 0; nb < 2; ++nb)
      hr[mb][nb] = *(const u32x2*)(hf + ((size_t)((rt * 4 + wr * 2 + mb) * 16 + ct * 2 + wc) * 64 + r + 16 * (2 * nb + (g >> 1))) * 8 + (g & 1) * 4);
  f32x4 acc[2][2] = {};
  u32x4 fa[2][2][2], fb[2][2][2];
  auto rdfrags = [&](int t) {
    const char* slot = smem + ((t >> 1) & 3) * 32768;
#pragma unroll
    for (int kk = 0; kk < 2; ++kk) {
      const int ksl = (t & 1) * 2 + kk;
#pragma unroll
      for (int mb = 0; mb < 2; ++mb) fa[t & 1][kk][mb] = *(const u32x4*)(slot + aoff + mb * 4096 + ksl * 1024);
#pragma unroll
      for (int nb = 0; nb < 2; ++nb) {
        const int ho = (nb ^ (g & 1)) << 5;
        fb[t & 1][kk][nb] = tr_frag(slot + boff + ksl * 4096 + ho, slot + boff + ksl * 4096 + 512 + ho);
      }
    }
  };
  auto compute = [&](int t) {
#pragma unroll
    for (int kk = 0; kk < 2; ++kk) {
      if (2 * t + kk < KS4) {
#pragma unroll
        for (int mb = 0; mb < 2; ++mb)
#pragma unroll
          for (int nb = 0; nb < 2; ++nb) acc[mb][nb] = mfma16(fb[t & 1][kk][nb], fa[t & 1][kk][mb], acc[mb][nb]);
      }
    }
  };
  BARE_BAR();
  STAMP(4,2);
  rdfrags(0);
#pragma unroll
  for (int s = 0; s < 11; ++s) {
    rdfrags(2 * s + 1);
    compute(2 * s);
    if (s + 1 < 11) {
      RAW_BAR();
      STAMP(4,3+s);
      rdfrags(2 * s + 2);
    }
    compute(2 * s + 1);
  }
#pragma unroll
  for (int mb = 0; mb < 2; ++mb)
#pragma unroll
    for (int nb = 0; nb < 2; ++nb) {
      const f32x4 o4 = acc[mb][nb];
      u32x2 o;
      o[0] = pack_bf(bf_lo(hr[mb][nb][0]) + o4[0], bf_hi(hr[mb][nb][0]) + o4[1]);
      o[1] = pack_bf(bf_lo(hr[mb][nb][1]) + o4[2], bf_hi(hr[mb][nb][1]) + o4[3]);
      *(u32x2*)(out + (size_t)(rt * 64 + wr * 32 + mb * 16 + r) * D_ + ct * 64 + wc * 32 + nb * 16 + g * 4) = o;
    }
  STAMP(4,14);
}

extern "C" void kernel_launch(void* const* d_in, const int* in_sizes, int n_in, void* d_out, int out_size,
                              void* d_ws, size_t ws_size, hipStream_t stream) {
  const u16* x = (const u16*)d_in[0];
  const u16* wqkv = (const u16*)d_in[1];
  const u16* wo = (const u16*)d_in[2];
  const u16* wg = (const u16*)d_in[4];
  const u16* wu = (const u16*)d_in[5];
  const u16* wd = (const u16*)d_in[6];
  u16* out = (u16*)d_out;
  char* ws = (char*)d_ws;
  u16* pq = (u16*)(ws);
  u16* hf = (u16*)(ws + (2u << 20));
  u16* wp = (u16*)(ws + (4u << 20));
  float* part = (float*)(ws + (5u << 20));
  unsigned* cnt = (unsigned*)(ws + (6u << 20));
  float* csgu = (float*)(ws + (6u << 20) + 4096);
  float* csp = (float*)(ws + (6u << 20) + 65536);
  float* hst = (float*)(ws + (7u << 20));
  u16* hhf = (u16*)(ws + (8u << 20));
  k1_qkv<<<256, 512, 0, stream>>>(x, wqkv, pq, part, wg, wu, csp, cnt, wo, wd, out);
  k2_attn_out<<<256, 512, 0, stream>>>(x, pq, part, wo, wp, cnt, hf, hst, csp, csgu, wg, wu, wd);
  k3_gateup<<<256, 512, 0, stream>>>(hf, wg, wu, hhf, hst, csgu);
  k4_down<<<256, 512, 0, stream>>>(hf, hhf, wd, out);
}
```

```cpp
#include <hip/hip_runtime.h>
#include <hip/hip_bf16.h>
#include <stdint.h>

typedef unsigned short u16;
typedef __attribute__((ext_vector_type(8))) __bf16 bf16x8;
typedef __attribute__((ext_vector_type(2))) __bf16 bf16x2;
typedef __attribute__((ext_vector_type(4))) short s16x4;
typedef __attribute__((ext_vector_type(4))) float f32x4;
typedef __attribute__((ext_vector_type(2))) float f32x2;
typedef __attribute__((ext_vector_type(4))) unsigned u32x4;
typedef __attribute__((ext_vector_type(2))) unsigned u32x2;
typedef __attribute__((address_space(3))) s16x4* lds_s16x4_ptr;

#define DI __device__ __forceinline__
#define STAMP(kid,i)

constexpr int S_ = 2048, D_ = 512, F_ = 1376, H_ = 32;
constexpr int KS4 = F_ / 32;

DI float bf_lo(unsigned u) { return __uint_as_float(u << 16); }
DI float bf_hi(unsigned u) { return __uint_as_float(u & 0xffff0000u); }
DI unsigned pack_bf(float lo, float hi) {
  f32x2 f = {lo, hi};
  bf16x2 r = __builtin_convertvector(f, bf16x2);
  return __builtin_bit_cast(unsigned, r);
}
DI float bfr(float f) { return bf_lo(pack_bf(f, 0.f) & 0xffffu); }
DI f32x4 mfma16(u32x4 a, u32x4 b, f32x4 c) {
  return __builtin_amdgcn_mfma_f32_16x16x32_bf16(__builtin_bit_cast(bf16x8, a),
                                                 __builtin_bit_cast(bf16x8, b), c, 0, 0, 0);
}
DI float dot2(unsigned a, unsigned b, float c) {
  return __builtin_amdgcn_fdot2_f32_bf16(__builtin_bit_cast(bf16x2, a), __builtin_bit_cast(bf16x2, b), c, false);
}
DI u32x4 tr_frag(const char* lds0, const char* lds1) {
  s16x4 lo = __builtin_amdgcn_ds_read_tr16_b64_v4i16((lds_s16x4_ptr)lds0);
  s16x4 hi = __builtin_amdgcn_ds_read_tr16_b64_v4i16((lds_s16x4_ptr)lds1);
  u32x2 l2 = __builtin_bit_cast(u32x2, lo), h2 = __builtin_bit_cast(u32x2, hi);
  u32x4 r = {l2[0], l2[1], h2[0], h2[1]};
  return r;
}
DI unsigned lds_addr(char* l) { return (unsigned)(size_t)(__attribute__((address_space(3))) char*)l; }
DI void glds16(const void* sbase, unsigned voff, unsigned dst) {
  asm volatile("s_mov_b32 m0, %2\n\ts_nop 0\n\tglobal_load_lds_dwordx4 %0, %1"
               :: "v"(voff), "s"(sbase), "s"(dst) : "memory");
}
DI float red16(float s) {
  const unsigned u = __float_as_uint(s);
  auto r = __builtin_amdgcn_permlane16_swap(u, u, false, false);
  return __uint_as_float(r[0]) + __uint_as_float(r[1]);
}
DI float red32(float s) {
  const unsigned u = __float_as_uint(s);
  auto r = __builtin_amdgcn_permlane32_swap(u, u, false, false);
  return __uint_as_float(r[0]) + __uint_as_float(r[1]);
}
#define WAIT_VM(n) asm volatile("s_waitcnt vmcnt(" #n ")" ::: "memory")
#define RAW_BAR() do { asm volatile("s_waitcnt lgkmcnt(0)" ::: "memory"); __builtin_amdgcn_s_barrier(); } while (0)
#define BARE_BAR() do { asm volatile("" ::: "memory"); __builtin_amdgcn_s_barrier(); } while (0)
template <int N> DI void wait_vm() { asm volatile("s_waitcnt vmcnt(%0)" ::"i"(N) : "memory"); }

DI float phi_f(float t) { return (t > 0.f ? t + 1.f : __expf(t)) + 1e-6f; }
DI void sq_frag(const u32x4& a, float& q) {
#pragma unroll
  for (int i = 0; i < 4; ++i) q = dot2(a[i], a[i], q);
}
#define ONES_FRAG ((u32x4){0x3f803f80u, 0x3f803f80u, 0x3f803f80u, 0x3f803f80u})

constexpr int K1_SLOT = 16384 + 12288, K1_NSLOT = 4;
__global__ __launch_bounds__(512) void k1_qkv(const u16* __restrict__ x, const u16* __restrict__ wqkv,
                                              u16* __restrict__ pq, float* __restrict__ part,
                                              const u16* __restrict__ wg, const u16* __restrict__ wu,
                                              float* __restrict__ csp, unsigned* __restrict__ cnt,
                                              const u16* __restrict__ wo, const u16* __restrict__ wd,
                                              const u16* outp) {
  __shared__ __attribute__((aligned(16))) char smem[K1_SLOT * K1_NSLOT];
  const int tid = threadIdx.x, lane = tid & 63;
  const int wave = __builtin_amdgcn_readfirstlane(tid >> 6);
  const int xcd = blockIdx.x & 7, idx = blockIdx.x >> 3;
  const int rt = (xcd >> 1) * 4 + (idx >> 3);
  const int cg = (xcd & 1) * 8 + (idx & 7);
  const int r = lane & 15, g = lane >> 4;
  STAMP(0,0);
  if (blockIdx.x == 0 && tid < 40) cnt[tid * 64] = 0u;
  if (wave >= 4) {
    const int lw = wave - 4;
    const unsigned lbase = lds_addr(smem);
    const unsigned aoffs = (unsigned)(lw * 32 + (lane >> 3)) * 1024u + (((lane & 7) ^ ((lane >> 4) & 3)) << 4);
    unsigned boffs[3];
#pragma unroll
    for (int i = 0; i < 3; ++i) {
      const int line = (lw * 3 + i) * 4 + g;
      const int kq = line / 3, cgrp = line % 3;
      const int k = kq * 4 + ((lane >> 2) & 3);
      const int ch = (lane & 3) ^ (((k >> 3) & 1) << 1);
      boffs[i] = (unsigned)(k * 1536 + cgrp * 512 + ch * 8) * 2u;
    }
    const char* abase = (const char*)(x + (size_t)rt * 128 * D_);
    const char* bbase = (const char*)(wqkv + cg * 32);
    auto stage = [&](int s) {
      const unsigned slot = lbase + (s % K1_NSLOT) * K1_SLOT;
#pragma unroll
      for (int i = 0; i < 4; ++i)
        glds16(abase + s * 128 + i * 8192, (i & 1) ? (aoffs ^ 64u) : aoffs, slot + lw * 4096 + i * 1024);
#pragma unroll
      for (int i = 0; i < 3; ++i)
        glds16(bbase + (size_t)s * 64 * 3072, boffs[i], slot + 16384 + (lw * 3 + i) * 1024);
    };
    stage(0); stage(1);
    wait_vm<7>();
    BARE_BAR();
    stage(2);
#pragma unroll
    for (int s = 0; s < 7; ++s) {
      if (s == 0 || s == 5) wait_vm<7>(); else if (s <= 4) wait_vm<14>(); else wait_vm<0>();
      BARE_BAR();
      if (s == 0) stage(3);
      if (s + 4 < 8) stage(s + 4);
    }
    const int W = blockIdx.x * 4 + lw;
    const int grp = W % 86, kp = (W / 86) & 1;
    float c8[8];
#pragma unroll
    for (int j = 0; j < 8; ++j) c8[j] = 0.f;
    if (W < 86 * 2) {
      const u16* wsrc = (grp < 43 ? wg : wu) + (grp % 43) * 32 + (lane & 3) * 8 + (size_t)(kp * 256 + (lane >> 2)) * F_;
      u32x4 wv[16];
#pragma unroll
      for (int i = 0; i < 16; ++i) wv[i] = *(const u32x4*)(wsrc + (size_t)i * 16 * F_);
#pragma unroll
      for (int i = 0; i < 16; ++i)
#pragma unroll
        for (int j = 0; j < 4; ++j) { c8[2 * j] += bf_lo(wv[i][j]); c8[2 * j + 1] += bf_hi(wv[i][j]); }
    }
    unsigned touch;
    {
      const int T = idx * 256 + lw * 64 + lane;
      const char* tp = (const char*)pq + (size_t)T * 4096;
      if (T >= 3584) tp = (const char*)outp + (size_t)(T - 3584) * 4096;
      if (T >= 4096) tp = (const char*)wo + (size_t)(T - 4096) * 4096;
      if (T >= 4224) tp = (const char*)wd + (size_t)(T - 4224) * 4096;
      if (T >= 4568) tp = (const char*)wg + (size_t)(T - 4568) * 4096;
      if (T >= 4912) tp = (const char*)wu + (size_t)(T - 4912) * 4096;
      if (T >= 5256) tp = (const char*)wu;
      touch = *(const volatile unsigned*)tp;
    }
    BARE_BAR();
    if (W < 86 * 2) {
#pragma unroll
      for (int j = 0; j < 8; ++j) {
        float v = c8[j];
        v += __shfl_xor(v, 4); v += __shfl_xor(v, 8);
        c8[j] = red32(red16(v));
      }
      if (lane < 4) {
        float* dst = csp + kp * 2752 + grp * 32 + lane * 8;
        f32x4 v0 = {c8[0], c8[1], c8[2], c8[3]}, v1 = {c8[4], c8[5], c8[6], c8[7]};
        *(f32x4*)dst = v0; *(f32x4*)(dst + 4) = v1;
      }
    }
    asm volatile("" :: "v"(touch));
    BARE_BAR();
    return;
  }
  const int wr = wave >> 1, wc = wave & 1;
  const int q4 = (lane & 15) >> 2, p = lane & 3;
  const int aoff = (wr * 64 + r) * 128 + ((g ^ ((r >> 1) & 7)) << 4);
  const int aoff0 = aoff + wc * 4096, aoff1 = aoff + (wc ^ 1) * 4096;
  const int boff = 16384 + (2 * g) * 768 + q4 * 64 + ((wc ^ (g & 1)) << 5) + p * 8;
  const u32x4 ones = ONES_FRAG;
  f32x4 accq[4] = {}, acck[4] = {}, accv[4] = {};
  f32x4 rs[2] = {}, sqm[2] = {}, cq = {}, ck = {}, cv = {};
  u32x4 fa[2][4], fb[2][3];
  auto rdfrags = [&](int t) {
    const char* slot = smem + ((t >> 1) % K1_NSLOT) * K1_SLOT;
    const int kk = t & 1;
#pragma unroll
    for (int i = 0; i < 4; ++i)
      fa[kk][i] = *(const u32x4*)(slot + (((i < 2 ? aoff0 : aoff1) + (i & 1) * 2048) ^ (kk << 6)));
#pragma unroll
    for (int j = 0; j < 3; ++j)
      fb[kk][j] = tr_frag(slot + boff + kk * 6144 + j * 256, slot + boff + kk * 6144 + 768 + j * 256);
  };
  auto compute = [&](int t) {
    const int kk = t & 1;
    const u32x4* a = fa[kk];
    const u32x4 bq = fb[kk][0], bk = fb[kk][1], bv = fb[kk][2];
#pragma unroll
    for (int i = 0; i < 4; ++i) {
      accq[i] = mfma16(bq, a[i], accq[i]);
      acck[i] = mfma16(a[i], bk, acck[i]);
      accv[i] = mfma16(a[i], bv, accv[i]);
    }
    rs[0] = mfma16(ones, a[0], rs[0]);
    rs[1] = mfma16(ones, a[1], rs[1]);
    sqm[0] = mfma16(a[0], a[0], sqm[0]);
    sqm[1] = mfma16(a[1], a[1], sqm[1]);
    if (kk == wr) {
      cq = mfma16(bq, ones, cq);
      ck = mfma16(ones, bk, ck);
      cv = mfma16(ones, bv, cv);
    }
  };
  BARE_BAR();
  STAMP(0,2);
  rdfrags(0);
#pragma unroll
  for (int s = 0; s < 8; ++s) {
    rdfrags(2 * s + 1);
    compute(2 * s);
    __builtin_amdgcn_sched_barrier(0);
    if (s + 1 < 8) {
      RAW_BAR();
      STAMP(0,3+s);
      rdfrags(2 * s + 2);
    }
    compute(2 * s + 1);
    __builtin_amdgcn_sched_barrier(0);
  }
  STAMP(0,11);
  float* st = (float*)(smem);
  float* csl = (float*)(smem + 2048);
  float* st2 = (float*)(smem + 4096);
  float* red = (float*)(smem + 8192);
  {
#pragma unroll
    for (int i = 0; i < 2; ++i) {
      const float q01 = (r & 1) ? sqm[i][1] : sqm[i][0], q23 = (r & 1) ? sqm[i][3] : sqm[i][2];
      const float q = (r & 2) ? q23 : q01;
      if (g == (r >> 2)) {
        f32x2 v = {rs[i][0], q};
        *(f32x2*)(st + (wr * 64 + wc * 32 + i * 16 + r) * 2) = v;
      }
    }
    float* c = csl + (wr * 2 + wc) * 48;
    if (r == 0) *(f32x4*)(c + 4 * g) = cq;
    if (g == 0) { c[16 + r] = ck[0]; c[32 + r] = cv[0]; }
  }
  __syncthreads();
  STAMP(0,12);
  {
    const f32x2 a0 = *(const f32x2*)(st + (wr * 64 + lane) * 2);
    const float mean = a0[0] * (1.f / 512.f);
    const float var = fmaxf(a0[1] * (1.f / 512.f) - mean * mean, 0.f);
    f32x2 v = {mean, __builtin_amdgcn_rsqf(var + 1e-5f)};
    *(f32x2*)(st2 + (wave * 64 + lane) * 2) = v;
  }
  const f32x4 csq4 = *(const f32x4*)(csl + wc * 48 + 4 * g) + *(const f32x4*)(csl + (2 + wc) * 48 + 4 * g);
  const float csk = csl[wc * 48 + 16 + r] + csl[(2 + wc) * 48 + 16 + r];
  const float csv = csl[wc * 48 + 32 + r] + csl[(2 + wc) * 48 + 32 + r];
  f32x4 kv = {}, zz = {};
#pragma unroll
  for (int ih = 0; ih < 2; ++ih) {
    u32x4 pk, vv;
#pragma unroll
    for (int i2 = 0; i2 < 2; ++i2) {
      const int i = ih * 2 + i2;
      const int mbl = ((ih ^ wc) << 1) + i2;
      {
        const f32x2 mi = *(const f32x2*)(st2 + (wave * 64 + mbl * 16 + r) * 2);
        float qv[4];
#pragma unroll
        for (int j = 0; j < 4; ++j) qv[j] = phi_f((accq[i][j] - mi[0] * csq4[j]) * mi[1]);
        u32x2 o = {pack_bf(qv[0], qv[1]), pack_bf(qv[2], qv[3])};
        const int mblk = rt * 8 + wr * 4 + mbl;
        *(u32x2*)(pq + ((size_t)(mblk * 16 + cg) * 64 + r + 16 * (2 * wc + (g >> 1))) * 8 + (g & 1) * 4) = o;
      }
      {
        const f32x4 m0 = *(const f32x4*)(st2 + (wave * 64 + mbl * 16 + 4 * g) * 2);
        const f32x4 m1 = *(const f32x4*)(st2 + (wave * 64 + mbl * 16 + 4 * g) * 2 + 4);
        const float mean[4] = {m0[0], m0[2], m1[0], m1[2]}, inv[4] = {m0[1], m0[3], m1[1], m1[3]};
        float kk[4], vj[4];
#pragma unroll
        for (int j = 0; j < 4; ++j) {
          kk[j] = phi_f((acck[i][j] - mean[j] * csk) * inv[j]);
          vj[j] = (accv[i][j] - mean[j] * csv) * inv[j];
        }
        pk[2 * i2] = pack_bf(kk[0], kk[1]);
        pk[2 * i2 + 1] = pack_bf(kk[2], kk[3]);
        vv[2 * i2] = pack_bf(vj[0], vj[1]);
        vv[2 * i2 + 1] = pack_bf(vj[2], vj[3]);
      }
    }
    kv = mfma16(pk, vv, kv);
    zz = mfma16(pk, ones, zz);
  }
  {
    float* rd = red + (wc * 2 + wr) * 272;
#pragma unroll
    for (int j = 0; j < 4; ++j) rd[(4 * g + j) * 16 + r] = kv[j];
    if (r == 0) *(f32x4*)(rd + 256 + 4 * g) = zz;
  }
  __syncthreads();
  for (int i = tid; i < 544; i += 256) {
    const int hh = i >= 272 ? 1 : 0, e = i - hh * 272;
    const float* rr = red + hh * 2 * 272 + e;
    part[(size_t)(rt * 32 + cg * 2 + hh) * 272 + e] = rr[0] + rr[272];
  }
  STAMP(0,13);
}

typedef __attribute__((address_space(1))) unsigned gu32;
DI void poll_ge(unsigned* c, unsigned target) {
  for (unsigned spins = 0; spins < (1u << 22); ++spins) {
    const unsigned v = __builtin_amdgcn_readfirstlane(
        __hip_atomic_load((gu32*)c, __ATOMIC_RELAXED, __HIP_MEMORY_SCOPE_AGENT));
    if (v >= target) break;
    __builtin_amdgcn_s_sleep(1);
  }
  asm volatile("" ::: "memory");
}
DI void glds16_sc1(const void* sbase, unsigned voff, unsigned dst) {
  asm volatile("s_mov_b32 m0, %2\n\ts_nop 0\n\tglobal_load_lds_dwordx4 %0, %1 sc1"
               :: "v"(voff), "s"(sbase), "s"(dst) : "memory");
}
DI void glds16v(const char* gsrc, unsigned dst) {
  asm volatile("s_mov_b32 m0, %1\n\ts_nop 0\n\tglobal_load_lds_dwordx4 %0, off" :: "v"(gsrc), "s"(dst) : "memory");
}
constexpr int K3_SLOT = 16384 + 24576, K3_NSLOT = 4;

__global__ __launch_bounds__(512) void kb_rest(const u16* __restrict__ x, const u16* __restrict__ pq,
                                               const float* __restrict__ part, const u16* __restrict__ wo,
                                               u16* wp, unsigned* cnt, u16* hf, float* hst,
                                               const float* __restrict__ csp,
                                               const u16* __restrict__ wg, const u16* __restrict__ wu,
                                               const u16* __restrict__ wd, u16* hhf, u16* __restrict__ out) {
  __shared__ __attribute__((aligned(16))) char smem[K3_SLOT * K3_NSLOT];
  const int tid = threadIdx.x, lane = tid & 63;
  const int wave = __builtin_amdgcn_readfirstlane(tid >> 6);
  const int xcd = blockIdx.x & 7, idx = blockIdx.x >> 3;
  const int r = lane & 15, g = lane >> 4;
  const int q4 = (lane & 15) >> 2, p = lane & 3;
  const __amdgpu_buffer_rsrc_t hrs = __builtin_amdgcn_make_buffer_rsrc(hf, 0, S_ * D_ * 2, 0x00020000);
  const __amdgpu_buffer_rsrc_t srs = __builtin_amdgcn_make_buffer_rsrc(hst, 0, S_ * 32 * 4, 0x00020000);
  unsigned* cnt_w = cnt;
  unsigned* cnt_h = cnt + 8 * 64;
  unsigned* cnt_hh = cnt + 24 * 64;
  STAMP(2,0);
  {
    const int rt = xcd * 4 + (idx >> 3);
    const int ct = idx & 7;
    const int kg = wave >> 2, rb = wave & 3;
    const int mblk = rt * 4 + rb;
    const __amdgpu_buffer_rsrc_t wrs = __builtin_amdgcn_make_buffer_rsrc(wp, 0, 524288 + 8192, 0x00020000);
    const int ph = blockIdx.x >> 3, pc = blockIdx.x & 7;
    float* kv = (float*)(smem + 65536 + 16384);
    float pv[16];
    if (tid < 272) {
#pragma unroll
      for (int pp = 0; pp < 16; ++pp) pv[pp] = part[(size_t)(pp * 32 + ph) * 272 + tid];
    }
    float w[16];
    if (tid < 128) {
#pragma unroll
      for (int e = 0; e < 16; ++e) w[e] = bf_lo(wo[(size_t)(ph * 16 + e) * D_ + pc * 64 + (tid & 63)]);
    }
    u32x4 a[8];
#pragma unroll
    for (int i = 0; i < 8; ++i) a[i] = *(const u32x4*)(pq + ((size_t)(mblk * 16 + kg * 8 + i) * 64 + lane) * 8);
    u32x2 xr[2];
#pragma unroll
    for (int j = 0; j < 2; ++j)
      xr[j] = *(const u32x2*)(x + (size_t)(mblk * 16 + r) * D_ + ct * 64 + (kg * 2 + j) * 16 + g * 4);
    if (tid < 272) {
      float s = 0.f;
#pragma unroll
      for (int pp = 0; pp < 16; ++pp) s += pv[pp];
      kv[tid] = s;
    }
    __syncthreads();
    STAMP(2,1);
    if (tid < 128) {
      const int c = tid & 63, dh = tid >> 6;
      float o[8];
#pragma unroll
      for (int j = 0; j < 8; ++j) {
        float s = 0.f;
#pragma unroll
        for (int e4 = 0; e4 < 4; ++e4) {
          const f32x4 k4 = *(const f32x4*)(kv + (dh * 8 + j) * 16 + e4 * 4);
          s = fmaf(k4[0], w[e4 * 4], s); s = fmaf(k4[1], w[e4 * 4 + 1], s);
          s = fmaf(k4[2], w[e4 * 4 + 2], s); s = fmaf(k4[3], w[e4 * 4 + 3], s);
        }
        o[j] = s;
      }
      const int n = pc * 64 + c, nb = n >> 4, cc = n & 15;
      const int ks = ph >> 1, gg = (ph & 1) * 2 + dh;
      u32x4 v = {pack_bf(o[0], o[1]), pack_bf(o[2], o[3]), pack_bf(o[4], o[5]), pack_bf(o[6], o[7])};
      __builtin_amdgcn_raw_buffer_store_b128(v, wrs, (unsigned)((nb * 16 + ks) * 64 + gg * 16 + cc) * 16u, 0, 16);
    } else if (tid < 130) {
      const float* zs = kv + 256 + (tid - 128) * 8;
      u32x4 v = {pack_bf(zs[0], zs[1]), pack_bf(zs[2], zs[3]), pack_bf(zs[4], zs[5]), pack_bf(zs[6], zs[7])};
      __builtin_amdgcn_raw_buffer_store_b128(v, wrs, 524288u + (unsigned)(pc * 512 + ph * 16 + (tid - 128) * 8) * 2u, 0, 16);
    }
    WAIT_VM(0);
    __syncthreads();
    if (tid == 0) __hip_atomic_fetch_add((gu32*)(cnt_w + pc * 64), 1u, __ATOMIC_RELAXED, __HIP_MEMORY_SCOPE_AGENT);
    STAMP(2,2);
    unsigned touch = 0;
    if (wave >= 1) {
      const int t = (wave - 1) * 64 + lane;
      if (t < 344) touch += *(const unsigned*)((const char*)wd + (size_t)(idx * 344 + t) * 128);
      if (t < 384) {
        const int m = (t / 12) & 1, rr = idx * 16 + (t / 24), ln = t % 12;
        const size_t off = (size_t)rr * (F_ * 2) + (xcd & 1) * 1536 + (size_t)ln * 128;
        const size_t lim = (size_t)rr * (F_ * 2) + F_ * 2 - 4;
        touch += *(const unsigned*)((const char*)(m ? wu : wg) + (off < lim ? off : lim));
      }
    }
    if (wave == 0) poll_ge(cnt_w + ct * 64, 32u);
    __syncthreads();
    STAMP(2,3);
    u32x4 zf[8], wv[8];
#pragma unroll
    for (int i = 0; i < 8; ++i)
      zf[i] = __builtin_amdgcn_raw_buffer_load_b128(wrs, 524288u + (unsigned)(ct * 512 + (kg * 8 + i) * 32 + g * 8) * 2u, 0, 16);
#pragma unroll
    for (int i = 0; i < 8; ++i)
      wv[i] = __builtin_amdgcn_raw_buffer_load_b128(wrs, (unsigned)(ct * 65536 + (wave * 8 + i) * 1024 + lane * 16), 0, 16);
#pragma unroll
    for (int i = 0; i < 8; ++i) {
      float s = dot2(a[i][0], zf[i][0], 0.f);
      s = dot2(a[i][1], zf[i][1], s);
      s = dot2(a[i][2], zf[i][2], s);
      s = dot2(a[i][3], zf[i][3], s);
      const float inv = __builtin_amdgcn_rcpf(red16(s) + 1e-6f);
#pragma unroll
      for (int j = 0; j < 4; ++j) a[i][j] = pack_bf(bf_lo(a[i][j]) * inv, bf_hi(a[i][j]) * inv);
    }
#pragma unroll
    for (int i = 0; i < 8; ++i) *(u32x4*)(smem + (wave * 8 + i) * 1024 + lane * 16) = wv[i];
    __syncthreads();
    STAMP(2,4);
    f32x4 acc[4] = {};
#pragma unroll
    for (int i = 0; i < 8; ++i) {
#pragma unroll
      for (int nb = 0; nb < 4; ++nb) {
        const u32x4 b = *(const u32x4*)(smem + (nb * 16 + kg * 8 + i) * 1024 + lane * 16);
        acc[nb] = mfma16(b, a[i], acc[nb]);
      }
    }
    f32x4* xch = (f32x4*)(smem + 65536);
#pragma unroll
    for (int j = 0; j < 2; ++j) xch[(wave * 2 + j) * 64 + lane] = kg ? acc[j] : acc[2 + j];
    __syncthreads();
    float hs = 0.f, hq = 0.f;
#pragma unroll
    for (int j = 0; j < 2; ++j) {
      const f32x4 o4 = (kg ? acc[2 + j] : acc[j]) + xch[((wave ^ 4) * 2 + j) * 64 + lane];
      u32x2 o;
      o[0] = pack_bf(bf_lo(xr[j][0]) + o4[0], bf_hi(xr[j][0]) + o4[1]);
      o[1] = pack_bf(bf_lo(xr[j][1]) + o4[2], bf_hi(xr[j][1]) + o4[3]);
      __builtin_amdgcn_raw_buffer_store_b64(
          o, hrs, (unsigned)(((mblk * 16 + ct * 2 + kg) * 64 + r + 16 * (2 * j + (g >> 1))) * 16 + (g & 1) * 8), 0, 16);
      hs = dot2(o[0], 0x3f803f80u, hs); hs = dot2(o[1], 0x3f803f80u, hs);
      hq = dot2(o[0], o[0], hq); hq = dot2(o[1], o[1], hq);
    }
    hs = red32(red16(hs)); hq = red32(red16(hq));
    if (g == 0) {
      u32x2 v = {__float_as_uint(hs), __float_as_uint(hq)};
      __builtin_amdgcn_raw_buffer_store_b64(v, srs, (unsigned)(((mblk * 16 + r) * 16 + ct * 2 + kg) * 8), 0, 16);
    }
    asm volatile("" :: "v"(touch));
    WAIT_VM(0);
    __syncthreads();
    if (tid == 0) __hip_atomic_fetch_add((gu32*)(cnt_h + (rt >> 1) * 64), 1u, __ATOMIC_RELAXED, __HIP_MEMORY_SCOPE_AGENT);
    STAMP(2,5);
  }
  const int rt3 = (xcd >> 1) * 4 + (idx >> 3);
  const int cg = (xcd & 1) * 8 + (idx & 7);
  const int f0 = cg < 11 ? cg * 96 : 1056 + (cg - 11) * 64;
  const int nvalid = cg < 11 ? 6 : 4;
  const int rt4 = xcd * 4 + (idx >> 3);
  const int ct4 = idx & 7;
  if (wave >= 4) {
    const int lw = wave - 4;
    const unsigned lbase = lds_addr(smem);
    {
      const char* abase = (const char*)(hf + (size_t)((rt3 * 8 + lw * 2) * 16) * 512);
      const long wdelta = (const char*)wu - (const char*)wg;
      const char* bsrc[6];
#pragma unroll
      for (int i = 0; i < 6; ++i) {
        const int line = (lw * 6 + i) * 4 + g;
        const int kq = line / 6, cgrp = line % 6;
        const int k = kq * 4 + ((lane >> 2) & 3);
        const int ch = (lane & 3) ^ (((k >> 3) & 1) << 1);
        int c3 = cgrp < 3 ? cgrp : cgrp - 3;
        c3 = (c3 * 2 < nvalid) ? c3 : 0;
        bsrc[i] = (const char*)(wg + f0) + (unsigned)(k * F_ + c3 * 32 + ch * 8) * 2u + (cgrp >= 3 ? wdelta : 0);
      }
      auto stA = [&](int s) {
        const unsigned slot = lbase + (s % K3_NSLOT) * K3_SLOT;
#pragma unroll
        for (int i = 0; i < 4; ++i)
          glds16_sc1(abase + (i >> 1) * 16384 + (s * 2 + (i & 1)) * 1024, lane * 16, slot + (lw * 4 + i) * 1024);
      };
      auto stB = [&](int s) {
        const unsigned slot = lbase + (s % K3_NSLOT) * K3_SLOT;
#pragma unroll
        for (int i = 0; i < 6; ++i) glds16v(bsrc[i] + (size_t)s * 64 * F_ * 2, slot + 16384 + (lw * 6 + i) * 1024);
      };
      stB(0); stB(1); stB(2); stB(3);
      poll_ge(cnt_h + rt3 * 64, 16u);
      stA(0); stA(1); stA(2); stA(3);
      wait_vm<12>(); BARE_BAR();
      wait_vm<8>();  BARE_BAR(); stA(4); stB(4);
      wait_vm<14>(); BARE_BAR(); stA(5); stB(5);
      wait_vm<20>(); BARE_BAR(); stA(6); stB(6);
      wait_vm<20>(); BARE_BAR(); stA(7); stB(7);
      wait_vm<20>(); BARE_BAR();
      wait_vm<10>(); BARE_BAR();
      wait_vm<0>();  BARE_BAR();
    }
    {
      const char* abase = (const char*)(hhf + (size_t)((rt4 * 4 + lw) * KS4) * 512);
      const char* bbase = (const char*)(wd + (size_t)lw * 32 * D_ + ct4 * 64);
      const unsigned boffs = (unsigned)(((g >> 1) * 4 + ((lane >> 2) & 3)) * D_ + (g & 1) * 32 + (lane & 3) * 8) * 2u;
      auto stA = [&](int s) {
        const unsigned slot = lbase + (s & 3) * 32768;
#pragma unroll
        for (int i = 0; i < 4; ++i)
          glds16_sc1(abase + (s * 4 + ((s == 10 && i == 3) ? 2 : i)) * 1024, lane * 16, slot + lw * 4096 + i * 1024);
      };
      auto stB = [&](int s) {
        const unsigned slot = lbase + (s & 3) * 32768;
        const int so = (s == 10 && lw == 3) ? s - 1 : s;
#pragma unroll
        for (int i = 0; i < 4; ++i)
          glds16(bbase + (size_t)(so * 128 + i * 8) * D_ * 2, (i & 1) ? (boffs ^ 32u) : boffs, slot + 16384 + lw * 4096 + i * 1024);
      };
      stB(0); stB(1); stB(2);
      BARE_BAR();
      stB(3);
      poll_ge(cnt_hh + (rt4 >> 1) * 64, 16u);
      stA(0); stA(1); stA(2); stA(3);
      wait_vm<12>(); BARE_BAR();
      wait_vm<8>();  BARE_BAR(); stA(4); stB(4);
      wait_vm<12>(); BARE_BAR(); stA(5); stB(5);
#pragma unroll
      for (int s = 2; s < 10; ++s) {
        if (s <= 7) wait_vm<16>(); else if (s == 8) wait_vm<8>(); else wait_vm<0>();
        BARE_BAR();
        if (s + 4 < 11) { stA(s + 4); stB(s + 4); }
      }
    }
    return;
  }
  const int wr = wave >> 1, wc = wave & 1;
  {
    const int aoff = wr * 8192 + lane * 16;
    const int bbase2 = 16384 + (2 * g) * 1536 + q4 * 64 + p * 8;
    int boff[3];
#pragma unroll
    for (int j = 0; j < 3; ++j) {
      const int fbl = 3 * wc + j;
      boff[j] = bbase2 + (fbl >> 1) * 256 + (((fbl & 1) ^ (g & 1)) << 5);
    }
    u32x4 hp[8];
    f32x4 cg4[3], cu4[3];
    float mean_l = 0.f, inv2_l = 0.f;
    f32x4 accg[4][3] = {}, accu[4][3] = {};
    u32x4 fa[2][4], fb[2][6];
    auto rdfrags = [&](int t) {
      const char* slot = smem + ((t >> 1) % K3_NSLOT) * K3_SLOT;
      const int kk = t & 1;
#pragma unroll
      for (int i = 0; i < 4; ++i) fa[kk][i] = *(const u32x4*)(slot + aoff + i * 2048 + kk * 1024);
#pragma unroll
      for (int j = 0; j < 3; ++j) {
        fb[kk][j] = tr_frag(slot + boff[j] + kk * 12288, slot + boff[j] + kk * 12288 + 1536);
        fb[kk][3 + j] = tr_frag(slot + boff[j] + 768 + kk * 12288, slot + boff[j] + 768 + kk * 12288 + 1536);
      }
    };
    auto compute = [&](int t) {
      const int kk = t & 1;
      const u32x4* a = fa[kk];
#pragma unroll
      for (int j = 0; j < 3; ++j) {
        const u32x4 bg = fb[kk][j], bu = fb[kk][3 + j];
#pragma unroll
        for (int i = 0; i < 4; ++i) {
          accg[i][j] = mfma16(bg, a[i], accg[i][j]);
          accu[i][j] = mfma16(bu, a[i], accu[i][j]);
        }
      }
    };
    BARE_BAR();
    STAMP(3,0);
    rdfrags(0);
#pragma unroll
    for (int s = 0; s < 8; ++s) {
      rdfrags(2 * s + 1);
      compute(2 * s);
      __builtin_amdgcn_sched_barrier(0);
      if (s + 1 < 8) {
        RAW_BAR();
        STAMP(3,1+s);
        rdfrags(2 * s + 2);
      }
      if (s == 4) {
#pragma unroll
        for (int i = 0; i < 8; ++i)
          hp[i] = __builtin_amdgcn_raw_buffer_load_b128(srs, (unsigned)((rt3 * 128 + wr * 64 + lane) * 128 + i * 16), 0, 16);
      }
      if (s == 6) {
        f32x4 t4 = {};
#pragma unroll
        for (int i = 0; i < 8; ++i) t4 += __builtin_bit_cast(f32x4, hp[i]);
        mean_l = (t4[0] + t4[2]) * (1.f / 512.f);
        const float var = fmaxf((t4[1] + t4[3]) * (1.f / 512.f) - mean_l * mean_l, 0.f);
        inv2_l = __builtin_amdgcn_rcpf(var + 1e-5f);
        asm volatile("" : "+v"(mean_l), "+v"(inv2_l));
#pragma unroll
        for (int j = 0; j < 3; ++j) {
          const int fbl = (3 * wc + j) < nvalid ? (3 * wc + j) : 0;
          const float* c = csp + f0 + fbl * 16 + 4 * g;
          cg4[j] = *(const f32x4*)c + *(const f32x4*)(c + 2752);
          cu4[j] = *(const f32x4*)(c + F_) + *(const f32x4*)(c + F_ + 2752);
        }
      }
      compute(2 * s + 1);
      __builtin_amdgcn_sched_barrier(0);
    }
    STAMP(3,9);
    const __amdgpu_buffer_rsrc_t hhrs = __builtin_amdgcn_make_buffer_rsrc(hhf, 0, 128 * KS4 * 1024, 0x00020000);
#pragma unroll
    for (int i = 0; i < 4; ++i) {
      const float mean = __shfl(mean_l, i * 16 + r), inv2 = __shfl(inv2_l, i * 16 + r);
      const int mblk = rt3 * 8 + wr * 4 + i;
#pragma unroll
      for (int j = 0; j < 3; ++j) {
        const int fbl = 3 * wc + j;
        float hv[4];
#pragma unroll
        for (int e = 0; e < 4; ++e)
          hv[e] = ((accg[i][j][e] - mean * cg4[j][e]) * inv2) * (accu[i][j][e] - mean * cu4[j][e]);
        if (fbl < nvalid) {
          const int fb_ = (f0 >> 4) + fbl;
          u32x2 o = {pack_bf(hv[0], hv[1]), pack_bf(hv[2], hv[3])};
          __builtin_amdgcn_raw_buffer_store_b64(
              o, hhrs, (unsigned)(((mblk * KS4 + (fb_ >> 1)) * 64 + r + 16 * (2 * (fb_ & 1) + (g >> 1))) * 16 + (g & 1) * 8), 0, 16);
        }
      }
    }
    WAIT_VM(0);
    RAW_BAR();
    if (tid == 0) __hip_atomic_fetch_add((gu32*)(cnt_hh + rt3 * 64), 1u, __ATOMIC_RELAXED, __HIP_MEMORY_SCOPE_AGENT);
    STAMP(3,10);
  }
  {
    const int boff = 16384 + (2 * g) * 512 + wc * 256 + q4 * 64 + p * 8;
    const int aoff = wr * 8192 + lane * 16;
    u32x2 hr[2][2];
    f32x4 acc[2][2] = {};
    u32x4 fa[2][2][2], fb[2][2][2];
    auto rdfrags = [&](int t) {
      const char* slot = smem + ((t >> 1) & 3) * 32768;
#pragma unroll
      for (int kk = 0; kk < 2; ++kk) {
        const int ksl = (t & 1) * 2 + kk;
#pragma unroll
        for (int mb = 0; mb < 2; ++mb) fa[t & 1][kk][mb] = *(const u32x4*)(slot + aoff + mb * 4096 + ksl * 1024);
#pragma unroll
        for (int nb = 0; nb < 2; ++nb) {
          const int ho = (nb ^ (g & 1)) << 5;
          fb[t & 1][kk][nb] = tr_frag(slot + boff + ksl * 4096 + ho, slot + boff + ksl * 4096 + 512 + ho);
        }
      }
    };
    auto compute = [&](int t) {
#pragma unroll
      for (int kk = 0; kk < 2; ++kk) {
        if (2 * t + kk < KS4) {
#pragma unroll
          for (int mb = 0; mb < 2; ++mb)
#pragma unroll
            for (int nb = 0; nb < 2; ++nb) acc[mb][nb] = mfma16(fb[t & 1][kk][nb], fa[t & 1][kk][mb], acc[mb][nb]);
        }
      }
    };
    BARE_BAR();
    STAMP(4,0);
    rdfrags(0);
#pragma unroll
    for (int mb = 0; mb < 2; ++mb)
#pragma unroll
      for (int nb = 0; nb < 2; ++nb)
        hr[mb][nb] = __builtin_amdgcn_raw_buffer_load_b64(
            hrs, (unsigned)((((rt4 * 4 + wr * 2 + mb) * 16 + ct4 * 2 + wc) * 64 + r + 16 * (2 * nb + (g >> 1))) * 16 + (g & 1) * 8), 0, 16);
#pragma unroll
    for (int s = 0; s < 11; ++s) {
      rdfrags(2 * s + 1);
      compute(2 * s);
      if (s + 1 < 11) {
        RAW_BAR();
        STAMP(4,1+s);
        rdfrags(2 * s + 2);
      }
      compute(2 * s + 1);
    }
#pragma unroll
    for (int mb = 0; mb < 2; ++mb)
#pragma unroll
      for (int nb = 0; nb < 2; ++nb) {
        const f32x4 o4 = acc[mb][nb];
        u32x2 o;
        o[0] = pack_bf(bf_lo(hr[mb][nb][0]) + o4[0], bf_hi(hr[mb][nb][0]) + o4[1]);
        o[1] = pack_bf(bf_lo(hr[mb][nb][1]) + o4[2], bf_hi(hr[mb][nb][1]) + o4[3]);
        *(u32x2*)(out + (size_t)(rt4 * 64 + wr * 32 + mb * 16 + r) * D_ + ct4 * 64 + wc * 32 + nb * 16 + g * 4) = o;
      }
    STAMP(4,14);
  }
}

extern "C" void kernel_launch(void* const* d_in, const int* in_sizes, int n_in, void* d_out, int out_size,
                              void* d_ws, size_t ws_size, hipStream_t stream) {
  const u16* x = (const u16*)d_in[0];
  const u16* wqkv = (const u16*)d_in[1];
  const u16* wo = (const u16*)d_in[2];
  const u16* wg = (const u16*)d_in[4];
  const u16* wu = (const u16*)d_in[5];
  const u16* wd = (const u16*)d_in[6];
  u16* out = (u16*)d_out;
  char* ws = (char*)d_ws;
  u16* pq = (u16*)(ws);
  u16* hf = (u16*)(ws + (2u << 20));
  u16* wp = (u16*)(ws + (4u << 20));
  float* part = (float*)(ws + (5u << 20));
  unsigned* cnt = (unsigned*)(ws + (6u << 20));
  float* csp = (float*)(ws + (6u << 20) + 65536);
  float* hst = (float*)(ws + (7u << 20));
  u16* hhf = (u16*)(ws + (8u << 20));
  k1_qkv<<<256, 512, 0, stream>>>(x, wqkv, pq, part, wg, wu, csp, cnt, wo, wd, out);
  kb_rest<<<256, 512, 0, stream>>>(x, pq, part, wo, wp, cnt, hf, hst, csp, wg, wu, wd, hhf, out);
}
```

```cpp
#include <hip/hip_runtime.h>
#include <hip/hip_bf16.h>
#include <stdint.h>

typedef unsigned short u16;
typedef __attribute__((ext_vector_type(8))) __bf16 bf16x8;
typedef __attribute__((ext_vector_type(2))) __bf16 bf16x2;
typedef __attribute__((ext_vector_type(4))) short s16x4;
typedef __attribute__((ext_vector_type(4))) float f32x4;
typedef __attribute__((ext_vector_type(2))) float f32x2;
typedef __attribute__((ext_vector_type(4))) unsigned u32x4;
typedef __attribute__((ext_vector_type(2))) unsigned u32x2;
typedef __attribute__((address_space(3))) s16x4* lds_s16x4_ptr;

#define DI __device__ __forceinline__
#define STAMP(kid,i)

constexpr int S_ = 2048, D_ = 512, F_ = 1376, H_ = 32;
constexpr int KS4 = F_ / 32;

DI float bf_lo(unsigned u) { return __uint_as_float(u << 16); }
DI float bf_hi(unsigned u) { return __uint_as_float(u & 0xffff0000u); }
DI unsigned pack_bf(float lo, float hi) {
  f32x2 f = {lo, hi};
  bf16x2 r = __builtin_convertvector(f, bf16x2);
  return __builtin_bit_cast(unsigned, r);
}
DI float bfr(float f) { return bf_lo(pack_bf(f, 0.f) & 0xffffu); }
DI f32x4 mfma16(u32x4 a, u32x4 b, f32x4 c) {
  return __builtin_amdgcn_mfma_f32_16x16x32_bf16(__builtin_bit_cast(bf16x8, a),
                                                 __builtin_bit_cast(bf16x8, b), c, 0, 0, 0);
}
DI float dot2(unsigned a, unsigned b, float c) {
  return __builtin_amdgcn_fdot2_f32_bf16(__builtin_bit_cast(bf16x2, a), __builtin_bit_cast(bf16x2, b), c, false);
}
DI u32x4 tr_frag(const char* lds0, const char* lds1) {
  s16x4 lo = __builtin_amdgcn_ds_read_tr16_b64_v4i16((lds_s16x4_ptr)lds0);
  s16x4 hi = __builtin_amdgcn_ds_read_tr16_b64_v4i16((lds_s16x4_ptr)lds1);
  u32x2 l2 = __builtin_bit_cast(u32x2, lo), h2 = __builtin_bit_cast(u32x2, hi);
  u32x4 r = {l2[0], l2[1], h2[0], h2[1]};
  return r;
}
DI unsigned lds_addr(char* l) { return (unsigned)(size_t)(__attribute__((address_space(3))) char*)l; }
DI void glds16(const void* sbase, unsigned voff, unsigned dst) {
  asm volatile("s_mov_b32 m0, %2\n\ts_nop 0\n\tglobal_load_lds_dwordx4 %0, %1"
               :: "v"(voff), "s"(sbase), "s"(dst) : "memory");
}
DI float red16(float s) {
  const unsigned u = __float_as_uint(s);
  auto r = __builtin_amdgcn_permlane16_swap(u, u, false, false);
  return __uint_as_float(r[0]) + __uint_as_float(r[1]);
}
DI float red32(float s) {
  const unsigned u = __float_as_uint(s);
  auto r = __builtin_amdgcn_permlane32_swap(u, u, false, false);
  return __uint_as_float(r[0]) + __uint_as_float(r[1]);
}
DI u32x4 merge16(u32x2 t0, u32x2 t1) {
  auto x = __builtin_amdgcn_permlane16_swap(t0[0], t1[0], false, false);
  auto y = __builtin_amdgcn_permlane16_swap(t0[1], t1[1], false, false);
  u32x4 v = {x[0], y[0], x[1], y[1]};
  return v;
}
#define WAIT_VM(n) asm volatile("s_waitcnt vmcnt(" #n ")" ::: "memory")
#define RAW_BAR() do { asm volatile("s_waitcnt lgkmcnt(0)" ::: "memory"); __builtin_amdgcn_s_barrier(); } while (0)
#define BARE_BAR() do { asm volatile("" ::: "memory"); __builtin_amdgcn_s_barrier(); } while (0)
template <int N> DI void wait_vm() { asm volatile("s_waitcnt vmcnt(%0)" ::"i"(N) : "memory"); }

DI float phi_f(float t) { return (t > 0.f ? t + 1.f : __expf(t)) + 1e-6f; }
DI void sq_frag(const u32x4& a, float& q) {
#pragma unroll
  for (int i = 0; i < 4; ++i) q = dot2(a[i], a[i], q);
}
#define ONES_FRAG ((u32x4){0x3f803f80u, 0x3f803f80u, 0x3f803f80u, 0x3f803f80u})

constexpr int K1_SLOT = 16384 + 12288, K1_NSLOT = 4;
__global__ __launch_bounds__(512) void k1_qkv(const u16* __restrict__ x, const u16* __restrict__ wqkv,
                                              u16* __restrict__ pq, float* __restrict__ part,
                                              const u16* __restrict__ wg, const u16* __restrict__ wu,
                                              float* __restrict__ csp, unsigned* __restrict__ cnt,
                                              const u16* __restrict__ wo, const u16* __restrict__ wd,
                                              const u16* outp) {
  __shared__ __attribute__((aligned(16))) char smem[K1_SLOT * K1_NSLOT];
  const int tid = threadIdx.x, lane = tid & 63;
  const int wave = __builtin_amdgcn_readfirstlane(tid >> 6);
  const int xcd = blockIdx.x & 7, idx = blockIdx.x >> 3;
  const int rt = (xcd >> 1) * 4 + (idx >> 3);
  const int cg = (xcd & 1) * 8 + (idx & 7);
  const int r = lane & 15, g = lane >> 4;
  STAMP(0,0);
  if (blockIdx.x == 0 && tid < 40) cnt[tid * 64] = 0u;
  if (wave >= 4) {
    const int lw = wave - 4;
    const unsigned lbase = lds_addr(smem);
    const unsigned aoffs = (unsigned)(lw * 32 + (lane >> 3)) * 1024u + (((lane & 7) ^ ((lane >> 4) & 3)) << 4);
    unsigned boffs[3];
#pragma unroll
    for (int i = 0; i < 3; ++i) {
      const int line = (lw * 3 + i) * 4 + g;
      const int kq = line / 3, cgrp = line % 3;
      const int k = kq * 4 + ((lane >> 2) & 3);
      const int ch = (lane & 3) ^ (((k >> 3) & 1) << 1);
      boffs[i] = (unsigned)(k * 1536 + cgrp * 512 + ch * 8) * 2u;
    }
    const char* abase = (const char*)(x + (size_t)rt * 128 * D_);
    const char* bbase = (const char*)(wqkv + cg * 32);
    auto stage = [&](int s) {
      const unsigned slot = lbase + (s % K1_NSLOT) * K1_SLOT;
#pragma unroll
      for (int i = 0; i < 4; ++i)
        glds16(abase + s * 128 + i * 8192, (i & 1) ? (aoffs ^ 64u) : aoffs, slot + lw * 4096 + i * 1024);
#pragma unroll
      for (int i = 0; i < 3; ++i)
        glds16(bbase + (size_t)s * 64 * 3072, boffs[i], slot + 16384 + (lw * 3 + i) * 1024);
    };
    stage(0); stage(1);
    wait_vm<7>();
    BARE_BAR();
    stage(2);
#pragma unroll
    for (int s = 0; s < 7; ++s) {
      if (s == 0 || s == 5) wait_vm<7>(); else if (s <= 4) wait_vm<14>(); else wait_vm<0>();
      BARE_BAR();
      if (s == 0) stage(3);
      if (s + 4 < 8) stage(s + 4);
    }
    const int W = blockIdx.x * 4 + lw;
    const int grp = W % 86, kp = (W / 86) & 7;
    u32x4 wv[4];
    {
      const u16* wsrc = (grp < 43 ? wg : wu) + (grp % 43) * 32 + (lane & 3) * 8 + (size_t)(kp * 64 + (lane >> 2)) * F_;
#pragma unroll
      for (int i = 0; i < 4; ++i) wv[i] = *(const u32x4*)(wsrc + (size_t)i * 16 * F_);
    }
    unsigned touch;
    {
      const int T = idx * 256 + lw * 64 + lane;
      const char* tp = (const char*)pq + (size_t)T * 4096;
      if (T >= 3584) tp = (const char*)outp + (size_t)(T - 3584) * 4096;
      if (T >= 4096) tp = (const char*)wo + (size_t)(T - 4096) * 4096;
      if (T >= 4224) tp = (const char*)wd + (size_t)(T - 4224) * 4096;
      if (T >= 4568) tp = (const char*)wg + (size_t)(T - 4568) * 4096;
      if (T >= 4912) tp = (const char*)wu + (size_t)(T - 4912) * 4096;
      if (T >= 5256) tp = (const char*)wu;
      touch = *(const volatile unsigned*)tp;
    }
    BARE_BAR();
    if (W < 86 * 8) {
      float c8[8];
#pragma unroll
      for (int j = 0; j < 4; ++j) {
        c8[2 * j] = (bf_lo(wv[0][j]) + bf_lo(wv[1][j])) + (bf_lo(wv[2][j]) + bf_lo(wv[3][j]));
        c8[2 * j + 1] = (bf_hi(wv[0][j]) + bf_hi(wv[1][j])) + (bf_hi(wv[2][j]) + bf_hi(wv[3][j]));
      }
#pragma unroll
      for (int j = 0; j < 8; ++j) {
        float v = c8[j];
        v += __shfl_xor(v, 4); v += __shfl_xor(v, 8);
        c8[j] = red32(red16(v));
      }
      if (lane < 4) {
        float* dst = csp + kp * 2752 + grp * 32 + lane * 8;
        f32x4 v0 = {c8[0], c8[1], c8[2], c8[3]}, v1 = {c8[4], c8[5], c8[6], c8[7]};
        *(f32x4*)dst = v0; *(f32x4*)(dst + 4) = v1;
      }
    }
    asm volatile("" :: "v"(touch));
    BARE_BAR();
    return;
  }
  const int wr = wave >> 1, wc = wave & 1;
  const int q4 = (lane & 15) >> 2, p = lane & 3;
  unsigned touch0;
  {
    const int t = wave * 64 + lane;
    const int lx = idx * 128 + (t & 127);
    const char* px = (const char*)(x + (size_t)(xcd >> 1) * 512 * D_) + (size_t)lx * 128;
    const int lw_ = idx * 192 + (t < 192 ? t : 0);
    const char* pw = (const char*)wqkv + (size_t)(lw_ / 12) * 3072 + ((lw_ % 12) >> 2) * 1024 + (xcd & 1) * 512 + (lw_ & 3) * 128;
    touch0 = *(const volatile unsigned*)pw;
    if (t < 128) touch0 += *(const volatile unsigned*)px;
  }
  const int aoff = (wr * 64 + r) * 128 + ((g ^ ((r >> 1) & 7)) << 4);
  const int aoff0 = aoff + wc * 4096, aoff1 = aoff + (wc ^ 1) * 4096;
  const int boff = 16384 + (2 * g) * 768 + q4 * 64 + ((wc ^ (g & 1)) << 5) + p * 8;
  const u32x4 ones = ONES_FRAG;
  f32x4 accq[4] = {}, acck[4] = {}, accv[4] = {};
  f32x4 rs[2] = {}, sqm[2] = {}, cq = {}, ck = {}, cv = {};
  u32x4 fa[2][4], fb[2][3];
  auto rdfrags = [&](int t) {
    const char* slot = smem + ((t >> 1) % K1_NSLOT) * K1_SLOT;
    const int kk = t & 1;
#pragma unroll
    for (int i = 0; i < 4; ++i)
      fa[kk][i] = *(const u32x4*)(slot + (((i < 2 ? aoff0 : aoff1) + (i & 1) * 2048) ^ (kk << 6)));
#pragma unroll
    for (int j = 0; j < 3; ++j)
      fb[kk][j] = tr_frag(slot + boff + kk * 6144 + j * 256, slot + boff + kk * 6144 + 768 + j * 256);
  };
  auto compute = [&](int t) {
    const int kk = t & 1;
    const u32x4* a = fa[kk];
    const u32x4 bq = fb[kk][0], bk = fb[kk][1], bv = fb[kk][2];
#pragma unroll
    for (int i = 0; i < 4; ++i) {
      accq[i] = mfma16(bq, a[i], accq[i]);
      acck[i] = mfma16(a[i], bk, acck[i]);
      accv[i] = mfma16(a[i], bv, accv[i]);
    }
    rs[0] = mfma16(ones, a[0], rs[0]);
    rs[1] = mfma16(ones, a[1], rs[1]);
    sqm[0] = mfma16(a[0], a[0], sqm[0]);
    sqm[1] = mfma16(a[1], a[1], sqm[1]);
    if (kk == wr) {
      cq = mfma16(bq, ones, cq);
      ck = mfma16(ones, bk, ck);
      cv = mfma16(ones, bv, cv);
    }
  };
  BARE_BAR();
  STAMP(0,2);
  rdfrags(0);
#pragma unroll
  for (int s = 0; s < 8; ++s) {
    rdfrags(2 * s + 1);
    compute(2 * s);
    __builtin_amdgcn_sched_barrier(0);
    if (s + 1 < 8) {
      RAW_BAR();
      STAMP(0,3+s);
      rdfrags(2 * s + 2);
    }
    compute(2 * s + 1);
    __builtin_amdgcn_sched_barrier(0);
  }
  STAMP(0,11);
  float* st = (float*)(smem);
  float* csl = (float*)(smem + 2048);
  float* st2 = (float*)(smem + 4096);
  float* red = (float*)(smem + 8192);
  {
#pragma unroll
    for (int i = 0; i < 2; ++i) {
      const float q01 = (r & 1) ? sqm[i][1] : sqm[i][0], q23 = (r & 1) ? sqm[i][3] : sqm[i][2];
      const float q = (r & 2) ? q23 : q01;
      if (g == (r >> 2)) {
        f32x2 v = {rs[i][0], q};
        *(f32x2*)(st + (wr * 64 + wc * 32 + i * 16 + r) * 2) = v;
      }
    }
    float* c = csl + (wr * 2 + wc) * 48;
    if (r == 0) *(f32x4*)(c + 4 * g) = cq;
    if (g == 0) { c[16 + r] = ck[0]; c[32 + r] = cv[0]; }
  }
  __syncthreads();
  STAMP(0,12);
  {
    const f32x2 a0 = *(const f32x2*)(st + (wr * 64 + lane) * 2);
    const float mean = a0[0] * (1.f / 512.f);
    const float var = fmaxf(a0[1] * (1.f / 512.f) - mean * mean, 0.f);
    f32x2 v = {mean, __builtin_amdgcn_rsqf(var + 1e-5f)};
    *(f32x2*)(st2 + (wave * 64 + lane) * 2) = v;
  }
  const f32x4 csq4 = *(const f32x4*)(csl + wc * 48 + 4 * g) + *(const f32x4*)(csl + (2 + wc) * 48 + 4 * g);
  const float csk = csl[wc * 48 + 16 + r] + csl[(2 + wc) * 48 + 16 + r];
  const float csv = csl[wc * 48 + 32 + r] + csl[(2 + wc) * 48 + 32 + r];
  f32x4 kv = {}, zz = {};
#pragma unroll
  for (int ih = 0; ih < 2; ++ih) {
    u32x4 pk, vv;
#pragma unroll
    for (int i2 = 0; i2 < 2; ++i2) {
      const int i = ih * 2 + i2;
      const int mbl = ((ih ^ wc) << 1) + i2;
      {
        const f32x2 mi = *(const f32x2*)(st2 + (wave * 64 + mbl * 16 + r) * 2);
        float qv[4];
#pragma unroll
        for (int j = 0; j < 4; ++j) qv[j] = phi_f((accq[i][j] - mi[0] * csq4[j]) * mi[1]);
        u32x2 o = {pack_bf(qv[0], qv[1]), pack_bf(qv[2], qv[3])};
        const int mblk = rt * 8 + wr * 4 + mbl;
        *(u32x2*)(pq + ((size_t)(mblk * 16 + cg) * 64 + r + 16 * (2 * wc + (g >> 1))) * 8 + (g & 1) * 4) = o;
      }
      {
        const f32x4 m0 = *(const f32x4*)(st2 + (wave * 64 + mbl * 16 + 4 * g) * 2);
        const f32x4 m1 = *(const f32x4*)(st2 + (wave * 64 + mbl * 16 + 4 * g) * 2 + 4);
        const float mean[4] = {m0[0], m0[2], m1[0], m1[2]}, inv[4] = {m0[1], m0[3], m1[1], m1[3]};
        float kk[4], vj[4];
#pragma unroll
        for (int j = 0; j < 4; ++j) {
          kk[j] = phi_f((acck[i][j] - mean[j] * csk) * inv[j]);
          vj[j] = (accv[i][j] - mean[j] * csv) * inv[j];
        }
        pk[2 * i2] = pack_bf(kk[0], kk[1]);
        pk[2 * i2 + 1] = pack_bf(kk[2], kk[3]);
        vv[2 * i2] = pack_bf(vj[0], vj[1]);
        vv[2 * i2 + 1] = pack_bf(vj[2], vj[3]);
      }
    }
    kv = mfma16(pk, vv, kv);
    zz = mfma16(pk, ones, zz);
  }
  {
    float* rd = red + (wc * 2 + wr) * 272;
#pragma unroll
    for (int j = 0; j < 4; ++j) rd[(4 * g + j) * 16 + r] = kv[j];
    if (r == 0) *(f32x4*)(rd + 256 + 4 * g) = zz;
  }
  __syncthreads();
  for (int i = tid; i < 544; i += 256) {
    const int hh = i >= 272 ? 1 : 0, e = i - hh * 272;
    const float* rr = red + hh * 2 * 272 + e;
    part[(size_t)((cg * 2 + hh) * 16 + rt) * 272 + e] = rr[0] + rr[272];
  }
  asm volatile("" :: "v"(touch0));
  STAMP(0,13);
}

typedef __attribute__((address_space(1))) unsigned gu32;
DI void poll_ge(unsigned* c, unsigned target) {
  for (unsigned spins = 0; spins < (1u << 22); ++spins) {
    const unsigned v = __builtin_amdgcn_readfirstlane(
        __hip_atomic_load((gu32*)c, __ATOMIC_RELAXED, __HIP_MEMORY_SCOPE_AGENT));
    if (v >= target) break;
    __builtin_amdgcn_s_sleep(1);
  }
  asm volatile("" ::: "memory");
}
DI void glds16_sc1(const void* sbase, unsigned voff, unsigned dst) {
  asm volatile("s_mov_b32 m0, %2\n\ts_nop 0\n\tglobal_load_lds_dwordx4 %0, %1 sc1"
               :: "v"(voff), "s"(sbase), "s"(dst) : "memory");
}
DI void glds16v(const char* gsrc, unsigned dst) {
  asm volatile("s_mov_b32 m0, %1\n\ts_nop 0\n\tglobal_load_lds_dwordx4 %0, off" :: "v"(gsrc), "s"(dst) : "memory");
}
constexpr int K3_SLOT = 16384 + 24576, K3_NSLOT = 4;

__global__ __launch_bounds__(512) void kb_rest(const u16* __restrict__ x, const u16* __restrict__ pq,
                                               const float* __restrict__ part, const u16* __restrict__ wo,
                                               u16* wp, unsigned* cnt, u16* hf, float* hst,
                                               const float* __restrict__ csp,
                                               const u16* __restrict__ wg, const u16* __restrict__ wu,
                                               const u16* __restrict__ wd, u16* hhf, u16* __restrict__ out) {
  __shared__ __attribute__((aligned(16))) char smem[K3_SLOT * K3_NSLOT];
  const int tid = threadIdx.x, lane = tid & 63;
  const int wave = __builtin_amdgcn_readfirstlane(tid >> 6);
  const int xcd = blockIdx.x & 7, idx = blockIdx.x >> 3;
  const int r = lane & 15, g = lane >> 4;
  const int q4 = (lane & 15) >> 2, p = lane & 3;
  const __amdgpu_buffer_rsrc_t hrs = __builtin_amdgcn_make_buffer_rsrc(hf, 0, S_ * D_ * 2, 0x00020000);
  const __amdgpu_buffer_rsrc_t srs = __builtin_amdgcn_make_buffer_rsrc(hst, 0, S_ * 32 * 4 + 16 * 2752 * 4, 0x00020000);
  unsigned* cnt_w = cnt;
  unsigned* cnt_h = cnt + 8 * 64;
  unsigned* cnt_hh = cnt + 24 * 64;
  STAMP(2,0);
  {
    const int rt = xcd * 4 + (idx >> 3);
    const int ct = idx & 7;
    const int kg = wave >> 2, rb = wave & 3;
    const int mblk = rt * 4 + rb;
    const __amdgpu_buffer_rsrc_t wrs = __builtin_amdgcn_make_buffer_rsrc(wp, 0, 524288 + 8192, 0x00020000);
    const int ph = blockIdx.x >> 3, pc = blockIdx.x & 7;
    float* kv = (float*)(smem + 65536 + 16384);
    float pv[16];
    if (tid < 272) {
#pragma unroll
      for (int pp = 0; pp < 16; ++pp) pv[pp] = part[(size_t)(ph * 16 + pp) * 272 + tid];
    }
    float w[16];
    if (tid < 128) {
#pragma unroll
      for (int e = 0; e < 16; ++e) w[e] = bf_lo(wo[(size_t)(ph * 16 + e) * D_ + pc * 64 + (tid & 63)]);
    }
    float cpart[8];
    const int ccol = ((rt & 1) * 8 + ct) * 172 + (tid - 272);
    const bool do_cs = tid >= 272 && tid < 444;
    if (do_cs) {
#pragma unroll
      for (int kp = 0; kp < 8; ++kp) cpart[kp] = csp[kp * 2752 + ccol];
    }
    u32x4 a[8];
#pragma unroll
    for (int i = 0; i < 8; ++i) a[i] = *(const u32x4*)(pq + ((size_t)(mblk * 16 + kg * 8 + i) * 64 + lane) * 8);
    u32x2 xr[2];
#pragma unroll
    for (int j = 0; j < 2; ++j)
      xr[j] = *(const u32x2*)(x + (size_t)(mblk * 16 + r) * D_ + ct * 64 + (kg * 2 + j) * 16 + g * 4);
    if (tid < 272) {
      float s = 0.f;
#pragma unroll
      for (int pp = 0; pp < 16; ++pp) s += pv[pp];
      kv[tid] = s;
    }
    if (do_cs) {
      const float c = ((cpart[0] + cpart[1]) + (cpart[2] + cpart[3])) + ((cpart[4] + cpart[5]) + (cpart[6] + cpart[7]));
      __builtin_amdgcn_raw_buffer_store_b32(__float_as_uint(c), srs, (unsigned)(S_ * 32 * 4 + ((rt >> 1) * 2752 + ccol) * 4), 0, 16);
    }
    __syncthreads();
    STAMP(2,1);
    if (tid < 128) {
      const int c = tid & 63, dh = tid >> 6;
      float o[8];
#pragma unroll
      for (int j = 0; j < 8; ++j) {
        float s = 0.f;
#pragma unroll
        for (int e4 = 0; e4 < 4; ++e4) {
          const f32x4 k4 = *(const f32x4*)(kv + (dh * 8 + j) * 16 + e4 * 4);
          s = fmaf(k4[0], w[e4 * 4], s); s = fmaf(k4[1], w[e4 * 4 + 1], s);
          s = fmaf(k4[2], w[e4 * 4 + 2], s); s = fmaf(k4[3], w[e4 * 4 + 3], s);
        }
        o[j] = s;
      }
      const int n = pc * 64 + c, nb = n >> 4, cc = n & 15;
      const int ks = ph >> 1, gg = (ph & 1) * 2 + dh;
      u32x4 v = {pack_bf(o[0], o[1]), pack_bf(o[2], o[3]), pack_bf(o[4], o[5]), pack_bf(o[6], o[7])};
      __builtin_amdgcn_raw_buffer_store_b128(v, wrs, (unsigned)((nb * 16 + ks) * 64 + gg * 16 + cc) * 16u, 0, 16);
    } else if (tid < 130) {
      const float* zs = kv + 256 + (tid - 128) * 8;
      u32x4 v = {pack_bf(zs[0], zs[1]), pack_bf(zs[2], zs[3]), pack_bf(zs[4], zs[5]), pack_bf(zs[6], zs[7])};
      __builtin_amdgcn_raw_buffer_store_b128(v, wrs, 524288u + (unsigned)(pc * 512 + ph * 16 + (tid - 128) * 8) * 2u, 0, 16);
    }
    WAIT_VM(0);
    __syncthreads();
    if (tid == 0) __hip_atomic_fetch_add((gu32*)(cnt_w + pc * 64), 1u, __ATOMIC_RELAXED, __HIP_MEMORY_SCOPE_AGENT);
    STAMP(2,2);
    unsigned touch = 0;
    if (wave >= 1) {
      const int t = (wave - 1) * 64 + lane;
      if (t < 344) touch += *(const unsigned*)((const char*)wd + (size_t)(idx * 344 + t) * 128);
      if (t < 384) {
        const int m = (t / 12) & 1, rr = idx * 16 + (t / 24), ln = t % 12;
        const size_t off = (size_t)rr * (F_ * 2) + (xcd & 1) * 1536 + (size_t)ln * 128;
        const size_t lim = (size_t)rr * (F_ * 2) + F_ * 2 - 4;
        touch += *(const unsigned*)((const char*)(m ? wu : wg) + (off < lim ? off : lim));
      }
    }
    if (wave == 0) poll_ge(cnt_w + ct * 64, 32u);
    __syncthreads();
    STAMP(2,3);
    u32x4 zf[8], wv[8];
#pragma unroll
    for (int i = 0; i < 8; ++i)
      zf[i] = __builtin_amdgcn_raw_buffer_load_b128(wrs, 524288u + (unsigned)(ct * 512 + (kg * 8 + i) * 32 + g * 8) * 2u, 0, 16);
#pragma unroll
    for (int i = 0; i < 8; ++i)
      wv[i] = __builtin_amdgcn_raw_buffer_load_b128(wrs, (unsigned)(ct * 65536 + (wave * 8 + i) * 1024 + lane * 16), 0, 16);
#pragma unroll
    for (int i = 0; i < 8; ++i) {
      float s = dot2(a[i][0], zf[i][0], 0.f);
      s = dot2(a[i][1], zf[i][1], s);
      s = dot2(a[i][2], zf[i][2], s);
      s = dot2(a[i][3], zf[i][3], s);
      const float inv = __builtin_amdgcn_rcpf(red16(s) + 1e-6f);
#pragma unroll
      for (int j = 0; j < 4; ++j) a[i][j] = pack_bf(bf_lo(a[i][j]) * inv, bf_hi(a[i][j]) * inv);
    }
#pragma unroll
    for (int i = 0; i < 8; ++i) *(u32x4*)(smem + (wave * 8 + i) * 1024 + lane * 16) = wv[i];
    __syncthreads();
    STAMP(2,4);
    f32x4 acc[4] = {};
#pragma unroll
    for (int i = 0; i < 8; ++i) {
#pragma unroll
      for (int nb = 0; nb < 4; ++nb) {
        const u32x4 b = *(const u32x4*)(smem + (nb * 16 + kg * 8 + i) * 1024 + lane * 16);
        acc[nb] = mfma16(b, a[i], acc[nb]);
      }
    }
    f32x4* xch = (f32x4*)(smem + 65536);
#pragma unroll
    for (int j = 0; j < 2; ++j) xch[(wave * 2 + j) * 64 + lane] = kg ? acc[j] : acc[2 + j];
    __syncthreads();
    float hs = 0.f, hq = 0.f;
    u32x2 oo[2];
#pragma unroll
    for (int j = 0; j < 2; ++j) {
      const f32x4 o4 = (kg ? acc[2 + j] : acc[j]) + xch[((wave ^ 4) * 2 + j) * 64 + lane];
      u32x2 o;
      o[0] = pack_bf(bf_lo(xr[j][0]) + o4[0], bf_hi(xr[j][0]) + o4[1]);
      o[1] = pack_bf(bf_lo(xr[j][1]) + o4[2], bf_hi(xr[j][1]) + o4[3]);
      oo[j] = o;
      hs = dot2(o[0], 0x3f803f80u, hs); hs = dot2(o[1], 0x3f803f80u, hs);
      hq = dot2(o[0], o[0], hq); hq = dot2(o[1], o[1], hq);
    }
    __builtin_amdgcn_raw_buffer_store_b128(
        merge16(oo[0], oo[1]), hrs, (unsigned)(((mblk * 16 + ct * 2 + kg) * 64 + r + 16 * (2 * (g & 1) + (g >> 1))) * 16), 0, 16);
    hs = red32(red16(hs)); hq = red32(red16(hq));
    if (g == 0) {
      u32x2 v = {__float_as_uint(hs), __float_as_uint(hq)};
      __builtin_amdgcn_raw_buffer_store_b64(v, srs, (unsigned)(((ct * 2 + kg) * S_ + mblk * 16 + r) * 8), 0, 16);
    }
    asm volatile("" :: "v"(touch));
    WAIT_VM(0);
    __syncthreads();
    if (tid == 0) __hip_atomic_fetch_add((gu32*)(cnt_h + (rt >> 1) * 64), 1u, __ATOMIC_RELAXED, __HIP_MEMORY_SCOPE_AGENT);
    STAMP(2,5);
  }
  const int rt3 = (xcd >> 1) * 4 + (idx >> 3);
  const int cg = (xcd & 1) * 8 + (idx & 7);
  const int f0 = cg < 11 ? cg * 96 : 1056 + (cg - 11) * 64;
  const int nvalid = cg < 11 ? 6 : 4;
  const int rt4 = xcd * 4 + (idx >> 3);
  const int ct4 = idx & 7;
  if (wave >= 4) {
    const int lw = wave - 4;
    const unsigned lbase = lds_addr(smem);
    {
      const char* abase = (const char*)(hf + (size_t)((rt3 * 8 + lw * 2) * 16) * 512);
      const long wdelta = (const char*)wu - (const char*)wg;
      const char* bsrc[6];
#pragma unroll
      for (int i = 0; i < 6; ++i) {
        const int line = (lw * 6 + i) * 4 + g;
        const int kq = line / 6, cgrp = line % 6;
        const int k = kq * 4 + ((lane >> 2) & 3);
        const int ch = (lane & 3) ^ (((k >> 3) & 1) << 1);
        int c3 = cgrp < 3 ? cgrp : cgrp - 3;
        c3 = (c3 * 2 < nvalid) ? c3 : 0;
        bsrc[i] = (const char*)(wg + f0) + (unsigned)(k * F_ + c3 * 32 + ch * 8) * 2u + (cgrp >= 3 ? wdelta : 0);
      }
      auto stA = [&](int s) {
        const unsigned slot = lbase + (s % K3_NSLOT) * K3_SLOT;
#pragma unroll
        for (int i = 0; i < 4; ++i)
          glds16_sc1(abase + (i >> 1) * 16384 + (s * 2 + (i & 1)) * 1024, lane * 16, slot + (lw * 4 + i) * 1024);
      };
      auto stB = [&](int s) {
        const unsigned slot = lbase + (s % K3_NSLOT) * K3_SLOT;
#pragma unroll
        for (int i = 0; i < 6; ++i) glds16v(bsrc[i] + (size_t)s * 64 * F_ * 2, slot + 16384 + (lw * 6 + i) * 1024);
      };
      stB(0); stB(1); stB(2); stB(3);
      poll_ge(cnt_h + rt3 * 64, 16u);
      stA(0); stA(1); stA(2); stA(3);
      wait_vm<12>(); BARE_BAR();
      wait_vm<8>();  BARE_BAR(); stA(4); stB(4);
      wait_vm<14>(); BARE_BAR(); stA(5); stB(5);
      wait_vm<20>(); BARE_BAR(); stA(6); stB(6);
      wait_vm<20>(); BARE_BAR(); stA(7); stB(7);
      wait_vm<20>(); BARE_BAR();
      wait_vm<10>(); BARE_BAR();
      wait_vm<0>();  BARE_BAR();
    }
    {
      const char* abase = (const char*)(hhf + (size_t)((rt4 * 4 + lw) * KS4) * 512);
      const char* bbase = (const char*)(wd + (size_t)lw * 32 * D_ + ct4 * 64);
      const unsigned boffs = (unsigned)(((g >> 1) * 4 + ((lane >> 2) & 3)) * D_ + (g & 1) * 32 + (lane & 3) * 8) * 2u;
      auto stA = [&](int s) {
        const unsigned slot = lbase + (s & 3) * 32768;
#pragma unroll
        for (int i = 0; i < 4; ++i)
          glds16_sc1(abase + (s * 4 + ((s == 10 && i == 3) ? 2 : i)) * 1024, lane * 16, slot + lw * 4096 + i * 1024);
      };
      auto stB = [&](int s) {
        const unsigned slot = lbase + (s & 3) * 32768;
        const int so = (s == 10 && lw == 3) ? s - 1 : s;
#pragma unroll
        for (int i = 0; i < 4; ++i)
          glds16(bbase + (size_t)(so * 128 + i * 8) * D_ * 2, (i & 1) ? (boffs ^ 32u) : boffs, slot + 16384 + lw * 4096 + i * 1024);
      };
      stB(0); stB(1); stB(2);
      BARE_BAR();
      stB(3);
      poll_ge(cnt_hh + (rt4 >> 1) * 64, 16u);
      stA(0); stA(1); stA(2); stA(3);
      wait_vm<12>(); BARE_BAR();
      wait_vm<8>();  BARE_BAR(); stA(4); stB(4);
      wait_vm<12>(); BARE_BAR(); stA(5); stB(5);
#pragma unroll
      for (int s = 2; s < 10; ++s) {
        if (s <= 7) wait_vm<16>(); else if (s == 8) wait_vm<8>(); else wait_vm<0>();
        BARE_BAR();
        if (s + 4 < 11) { stA(s + 4); stB(s + 4); }
      }
    }
    return;
  }
  const int wr = wave >> 1, wc = wave & 1;
  {
    const int aoff = wr * 8192 + lane * 16;
    const int bbase2 = 16384 + (2 * g) * 1536 + q4 * 64 + p * 8;
    int boff[3];
#pragma unroll
    for (int j = 0; j < 3; ++j) {
      const int fbl = 3 * wc + j;
      boff[j] = bbase2 + (fbl >> 1) * 256 + (((fbl & 1) ^ (g & 1)) << 5);
    }
    u32x2 hp[16];
    f32x4 cg4[3], cu4[3];
    float mean_l = 0.f, inv2_l = 0.f;
    f32x4 accg[4][3] = {}, accu[4][3] = {};
    u32x4 fa[2][4], fb[2][6];
    auto rdfrags = [&](int t) {
      const char* slot = smem + ((t >> 1) % K3_NSLOT) * K3_SLOT;
      const int kk = t & 1;
#pragma unroll
      for (int i = 0; i < 4; ++i) fa[kk][i] = *(const u32x4*)(slot + aoff + i * 2048 + kk * 1024);
#pragma unroll
      for (int j = 0; j < 3; ++j) {
        fb[kk][j] = tr_frag(slot + boff[j] + kk * 12288, slot + boff[j] + kk * 12288 + 1536);
        fb[kk][3 + j] = tr_frag(slot + boff[j] + 768 + kk * 12288, slot + boff[j] + 768 + kk * 12288 + 1536);
      }
    };
    auto compute = [&](int t) {
      const int kk = t & 1;
      const u32x4* a = fa[kk];
#pragma unroll
      for (int j = 0; j < 3; ++j) {
        const u32x4 bg = fb[kk][j], bu = fb[kk][3 + j];
#pragma unroll
        for (int i = 0; i < 4; ++i) {
          accg[i][j] = mfma16(bg, a[i], accg[i][j]);
          accu[i][j] = mfma16(bu, a[i], accu[i][j]);
        }
      }
    };
    BARE_BAR();
    STAMP(3,0);
    rdfrags(0);
#pragma unroll
    for (int s = 0; s < 8; ++s) {
      rdfrags(2 * s + 1);
      compute(2 * s);
      __builtin_amdgcn_sched_barrier(0);
      if (s + 1 < 8) {
        RAW_BAR();
        STAMP(3,1+s);
        rdfrags(2 * s + 2);
      }
      if (s == 4) {
#pragma unroll
        for (int i = 0; i < 16; ++i)
          hp[i] = __builtin_amdgcn_raw_buffer_load_b64(srs, (unsigned)((i * S_ + rt3 * 128 + wr * 64 + lane) * 8), 0, 16);
      }
      if (s == 6) {
        f32x2 t2 = {};
#pragma unroll
        for (int i = 0; i < 16; ++i) t2 += __builtin_bit_cast(f32x2, hp[i]);
        mean_l = t2[0] * (1.f / 512.f);
        const float var = fmaxf(t2[1] * (1.f / 512.f) - mean_l * mean_l, 0.f);
        inv2_l = __builtin_amdgcn_rcpf(var + 1e-5f);
        asm volatile("" : "+v"(mean_l), "+v"(inv2_l));
#pragma unroll
        for (int j = 0; j < 3; ++j) {
          const int fbl = (3 * wc + j) < nvalid ? (3 * wc + j) : 0;
          const unsigned co = (unsigned)(S_ * 32 * 4 + (rt3 * 2752 + f0 + fbl * 16 + 4 * g) * 4);
          cg4[j] = __builtin_bit_cast(f32x4, __builtin_amdgcn_raw_buffer_load_b128(srs, co, 0, 16));
          cu4[j] = __builtin_bit_cast(f32x4, __builtin_amdgcn_raw_buffer_load_b128(srs, co + F_ * 4, 0, 16));
        }
      }
      compute(2 * s + 1);
      __builtin_amdgcn_sched_barrier(0);
    }
    STAMP(3,9);
    const __amdgpu_buffer_rsrc_t hhrs = __builtin_amdgcn_make_buffer_rsrc(hhf, 0, 128 * KS4 * 1024, 0x00020000);
#pragma unroll
    for (int ip = 0; ip < 2; ++ip) {
      float mean[2], inv2[2];
#pragma unroll
      for (int i2 = 0; i2 < 2; ++i2) {
        mean[i2] = __shfl(mean_l, (ip * 2 + i2) * 16 + r);
        inv2[i2] = __shfl(inv2_l, (ip * 2 + i2) * 16 + r);
      }
      const int mblk = rt3 * 8 + wr * 4 + ip * 2 + (g & 1);
#pragma unroll
      for (int j = 0; j < 3; ++j) {
        const int fbl = 3 * wc + j;
        u32x2 o[2];
#pragma unroll
        for (int i2 = 0; i2 < 2; ++i2) {
          const int i = ip * 2 + i2;
          float hv[4];
#pragma unroll
          for (int e = 0; e < 4; ++e)
            hv[e] = ((accg[i][j][e] - mean[i2] * cg4[j][e]) * inv2[i2]) * (accu[i][j][e] - mean[i2] * cu4[j][e]);
          o[i2][0] = pack_bf(hv[0], hv[1]); o[i2][1] = pack_bf(hv[2], hv[3]);
        }
        const u32x4 v = merge16(o[0], o[1]);
        if (fbl < nvalid) {
          const int fb_ = (f0 >> 4) + fbl;
          __builtin_amdgcn_raw_buffer_store_b128(
              v, hhrs, (unsigned)(((mblk * KS4 + (fb_ >> 1)) * 64 + r + 16 * (2 * (fb_ & 1) + (g >> 1))) * 16), 0, 16);
        }
      }
    }
    WAIT_VM(0);
    RAW_BAR();
    if (tid == 0) __hip_atomic_fetch_add((gu32*)(cnt_hh + rt3 * 64), 1u, __ATOMIC_RELAXED, __HIP_MEMORY_SCOPE_AGENT);
    STAMP(3,10);
  }
  {
    const int boff = 16384 + (2 * g) * 512 + wc * 256 + q4 * 64 + p * 8;
    const int aoff = wr * 8192 + lane * 16;
    u32x2 hr[2][2];
    f32x4 acc[2][2] = {};
    u32x4 fa[2][2][2], fb[2][2][2];
    auto rdfrags = [&](int t) {
      const char* slot = smem + ((t >> 1) & 3) * 32768;
#pragma unroll
      for (int kk = 0; kk < 2; ++kk) {
        const int ksl = (t & 1) * 2 + kk;
#pragma unroll
        for (int mb = 0; mb < 2; ++mb) fa[t & 1][kk][mb] = *(const u32x4*)(slot + aoff + mb * 4096 + ksl * 1024);
#pragma unroll
        for (int nb = 0; nb < 2; ++nb) {
          const int ho = (nb ^ (g & 1)) << 5;
          fb[t & 1][kk][nb] = tr_frag(slot + boff + ksl * 4096 + ho, slot + boff + ksl * 4096 + 512 + ho);
        }
      }
    };
    auto compute = [&](int t) {
#pragma unroll
      for (int kk = 0; kk < 2; ++kk) {
        if (2 * t + kk < KS4) {
#pragma unroll
          for (int mb = 0; mb < 2; ++mb)
#pragma unroll
            for (int nb = 0; nb < 2; ++nb) acc[mb][nb] = mfma16(fb[t & 1][kk][nb], fa[t & 1][kk][mb], acc[mb][nb]);
        }
      }
    };
    BARE_BAR();
    STAMP(4,0);
    rdfrags(0);
#pragma unroll
    for (int mb = 0; mb < 2; ++mb)
#pragma unroll
      for (int nb = 0; nb < 2; ++nb)
        hr[mb][nb] = __builtin_amdgcn_raw_buffer_load_b64(
            hrs, (unsigned)((((rt4 * 4 + wr * 2 + mb) * 16 + ct4 * 2 + wc) * 64 + r + 16 * (2 * nb + (g >> 1))) * 16 + (g & 1) * 8), 0, 16);
#pragma unroll
    for (int s = 0; s < 11; ++s) {
      rdfrags(2 * s + 1);
      compute(2 * s);
      if (s + 1 < 11) {
        RAW_BAR();
        STAMP(4,1+s);
        rdfrags(2 * s + 2);
      }
      compute(2 * s + 1);
    }
#pragma unroll
    for (int mb = 0; mb < 2; ++mb)
#pragma unroll
      for (int nb = 0; nb < 2; ++nb) {
        const f32x4 o4 = acc[mb][nb];
        u32x2 o;
        o[0] = pack_bf(bf_lo(hr[mb][nb][0]) + o4[0], bf_hi(hr[mb][nb][0]) + o4[1]);
        o[1] = pack_bf(bf_lo(hr[mb][nb][1]) + o4[2], bf_hi(hr[mb][nb][1]) + o4[3]);
        *(u32x2*)(out + (size_t)(rt4 * 64 + wr * 32 + mb * 16 + r) * D_ + ct4 * 64 + wc * 32 + nb * 16 + g * 4) = o;
      }
    STAMP(4,14);
  }
}

extern "C" void kernel_launch(void* const* d_in, const int* in_sizes, int n_in, void* d_out, int out_size,
                              void* d_ws, size_t ws_size, hipStream_t stream) {
  const u16* x = (const u16*)d_in[0];
  const u16* wqkv = (const u16*)d_in[1];
  const u16* wo = (const u16*)d_in[2];
  const u16* wg = (const u16*)d_in[4];
  const u16* wu = (const u16*)d_in[5];
  const u16* wd = (const u16*)d_in[6];
  u16* out = (u16*)d_out;
  char* ws = (char*)d_ws;
  u16* pq = (u16*)(ws);
  u16* hf = (u16*)(ws + (2u << 20));
  u16* wp = (u16*)(ws + (4u << 20));
  float* part = (float*)(ws + (5u << 20));
  unsigned* cnt = (unsigned*)(ws + (6u << 20));
  float* csp = (float*)(ws + (6u << 20) + 65536);
  float* hst = (float*)(ws + (7u << 20));
  u16* hhf = (u16*)(ws + (8u << 20));
  k1_qkv<<<256, 512, 0, stream>>>(x, wqkv, pq, part, wg, wu, csp, cnt, wo, wd, out);
  kb_rest<<<256, 512, 0, stream>>>(x, pq, part, wo, wp, cnt, hf, hst, csp, wg, wu, wd, hhf, out);
}
```

```cpp
#include <hip/hip_runtime.h>
#include <hip/hip_bf16.h>
#include <stdint.h>

typedef unsigned short u16;
typedef __attribute__((ext_vector_type(8))) __bf16 bf16x8;
typedef __attribute__((ext_vector_type(2))) __bf16 bf16x2;
typedef __attribute__((ext_vector_type(4))) short s16x4;
typedef __attribute__((ext_vector_type(4))) float f32x4;
typedef __attribute__((ext_vector_type(2))) float f32x2;
typedef __attribute__((ext_vector_type(4))) unsigned u32x4;
typedef __attribute__((ext_vector_type(2))) unsigned u32x2;
typedef __attribute__((address_space(3))) s16x4* lds_s16x4_ptr;

#define DI __device__ __forceinline__
#define STAMP(kid,i)

constexpr int S_ = 2048, D_ = 512, F_ = 1376, H_ = 32;
constexpr int KS4 = F_ / 32;

DI float bf_lo(unsigned u) { return __uint_as_float(u << 16); }
DI float bf_hi(unsigned u) { return __uint_as_float(u & 0xffff0000u); }
DI unsigned pack_bf(float lo, float hi) {
  f32x2 f = {lo, hi};
  bf16x2 r = __builtin_convertvector(f, bf16x2);
  return __builtin_bit_cast(unsigned, r);
}
DI float bfr(float f) { return bf_lo(pack_bf(f, 0.f) & 0xffffu); }
DI f32x4 mfma16(u32x4 a, u32x4 b, f32x4 c) {
  return __builtin_amdgcn_mfma_f32_16x16x32_bf16(__builtin_bit_cast(bf16x8, a),
                                                 __builtin_bit_cast(bf16x8, b), c, 0, 0, 0);
}
DI float dot2(unsigned a, unsigned b, float c) {
  return __builtin_amdgcn_fdot2_f32_bf16(__builtin_bit_cast(bf16x2, a), __builtin_bit_cast(bf16x2, b), c, false);
}
DI u32x4 tr_frag(const char* lds0, const char* lds1) {
  s16x4 lo = __builtin_amdgcn_ds_read_tr16_b64_v4i16((lds_s16x4_ptr)lds0);
  s16x4 hi = __builtin_amdgcn_ds_read_tr16_b64_v4i16((lds_s16x4_ptr)lds1);
  u32x2 l2 = __builtin_bit_cast(u32x2, lo), h2 = __builtin_bit_cast(u32x2, hi);
  u32x4 r = {l2[0], l2[1], h2[0], h2[1]};
  return r;
}
DI unsigned lds_addr(char* l) { return (unsigned)(size_t)(__attribute__((address_space(3))) char*)l; }
DI void glds16(const void* sbase, unsigned voff, unsigned dst) {
  asm volatile("s_mov_b32 m0, %2\n\ts_nop 0\n\tglobal_load_lds_dwordx4 %0, %1"
               :: "v"(voff), "s"(sbase), "s"(dst) : "memory");
}
DI float red16(float s) {
  const unsigned u = __float_as_uint(s);
  auto r = __builtin_amdgcn_permlane16_swap(u, u, false, false);
  return __uint_as_float(r[0]) + __uint_as_float(r[1]);
}
DI float red32(float s) {
  const unsigned u = __float_as_uint(s);
  auto r = __builtin_amdgcn_permlane32_swap(u, u, false, false);
  return __uint_as_float(r[0]) + __uint_as_float(r[1]);
}
DI u32x4 merge16(u32x2 t0, u32x2 t1) {
  auto x = __builtin_amdgcn_permlane16_swap(t0[0], t1[0], false, false);
  auto y = __builtin_amdgcn_permlane16_swap(t0[1], t1[1], false, false);
  u32x4 v = {x[0], y[0], x[1], y[1]};
  return v;
}
#define WAIT_VM(n) asm volatile("s_waitcnt vmcnt(" #n ")" ::: "memory")
#define RAW_BAR() do { asm volatile("s_waitcnt lgkmcnt(0)" ::: "memory"); __builtin_amdgcn_s_barrier(); } while (0)
#define BARE_BAR() do { asm volatile("" ::: "memory"); __builtin_amdgcn_s_barrier(); } while (0)
template <int N> DI void wait_vm() { asm volatile("s_waitcnt vmcnt(%0)" ::"i"(N) : "memory"); }

DI float phi_f(float t) { return (t > 0.f ? t + 1.f : __expf(t)) + 1e-6f; }
DI void sq_frag(const u32x4& a, float& q) {
#pragma unroll
  for (int i = 0; i < 4; ++i) q = dot2(a[i], a[i], q);
}
#define ONES_FRAG ((u32x4){0x3f803f80u, 0x3f803f80u, 0x3f803f80u, 0x3f803f80u})

constexpr int K1_SLOT = 16384 + 12288, K1_NSLOT = 4;
__global__ __launch_bounds__(512) void k1_qkv(const u16* __restrict__ x, const u16* __restrict__ wqkv,
                                              u16* __restrict__ pq, float* __restrict__ part,
                                              const u16* __restrict__ wg, const u16* __restrict__ wu,
                                              float* __restrict__ csp, unsigned* __restrict__ cnt,
                                              const u16* __restrict__ wo, const u16* __restrict__ wd,
                                              const u16* outp) {
  __shared__ __attribute__((aligned(16))) char smem[K1_SLOT * K1_NSLOT];
  const int tid = threadIdx.x, lane = tid & 63;
  const int wave = __builtin_amdgcn_readfirstlane(tid >> 6);
  const int xcd = blockIdx.x & 7, idx = blockIdx.x >> 3;
  const int rt = (xcd >> 1) * 4 + (idx >> 3);
  const int cg = (xcd & 1) * 8 + (idx & 7);
  const int r = lane & 15, g = lane >> 4;
  STAMP(0,0);
  if (blockIdx.x == 0 && tid < 40) cnt[tid * 64] = 0u;
  if (wave >= 4) {
    const int lw = wave - 4;
    const unsigned lbase = lds_addr(smem);
    const unsigned aoffs = (unsigned)(lw * 32 + (lane >> 3)) * 1024u + (((lane & 7) ^ ((lane >> 4) & 3)) << 4);
    unsigned boffs[3];
#pragma unroll
    for (int i = 0; i < 3; ++i) {
      const int line = (lw * 3 + i) * 4 + g;
      const int kq = line / 3, cgrp = line % 3;
      const int k = kq * 4 + ((lane >> 2) & 3);
      const int ch = (lane & 3) ^ (((k >> 3) & 1) << 1);
      boffs[i] = (unsigned)(k * 1536 + cgrp * 512 + ch * 8) * 2u;
    }
    const char* abase = (const char*)(x + (size_t)rt * 128 * D_);
    const char* bbase = (const char*)(wqkv + cg * 32);
    auto stage = [&](int s) {
      const unsigned slot = lbase + (s % K1_NSLOT) * K1_SLOT;
#pragma unroll
      for (int i = 0; i < 4; ++i)
        glds16(abase + s * 128 + i * 8192, (i & 1) ? (aoffs ^ 64u) : aoffs, slot + lw * 4096 + i * 1024);
#pragma unroll
      for (int i = 0; i < 3; ++i)
        glds16(bbase + (size_t)s * 64 * 3072, boffs[i], slot + 16384 + (lw * 3 + i) * 1024);
    };
    stage(0); stage(1);
    wait_vm<7>();
    BARE_BAR();
    stage(2);
#pragma unroll
    for (int s = 0; s < 7; ++s) {
      if (s == 0 || s == 5) wait_vm<7>(); else if (s <= 4) wait_vm<14>(); else wait_vm<0>();
      BARE_BAR();
      if (s == 0) stage(3);
      if (s + 4 < 8) stage(s + 4);
    }
    const int W = blockIdx.x * 4 + lw;
    const int grp = W % 86, kp = (W / 86) & 7;
    u32x4 wv[4];
    {
      const u16* wsrc = (grp < 43 ? wg : wu) + (grp % 43) * 32 + (lane & 3) * 8 + (size_t)(kp * 64 + (lane >> 2)) * F_;
#pragma unroll
      for (int i = 0; i < 4; ++i) wv[i] = *(const u32x4*)(wsrc + (size_t)i * 16 * F_);
    }
    unsigned touch;
    {
      const int T = idx * 256 + lw * 64 + lane;
      const char* tp = (const char*)pq + (size_t)T * 4096;
      if (T >= 3584) tp = (const char*)outp + (size_t)(T - 3584) * 4096;
      if (T >= 4096) tp = (const char*)wo + (size_t)(T - 4096) * 4096;
      if (T >= 4224) tp = (const char*)wd + (size_t)(T - 4224) * 4096;
      if (T >= 4568) tp = (const char*)wg + (size_t)(T - 4568) * 4096;
      if (T >= 4912) tp = (const char*)wu + (size_t)(T - 4912) * 4096;
      if (T >= 5256) tp = (const char*)wu;
      touch = *(const volatile unsigned*)tp;
    }
    BARE_BAR();
    if (W < 86 * 8) {
      float c8[8];
#pragma unroll
      for (int j = 0; j < 4; ++j) {
        c8[2 * j] = (bf_lo(wv[0][j]) + bf_lo(wv[1][j])) + (bf_lo(wv[2][j]) + bf_lo(wv[3][j]));
        c8[2 * j + 1] = (bf_hi(wv[0][j]) + bf_hi(wv[1][j])) + (bf_hi(wv[2][j]) + bf_hi(wv[3][j]));
      }
#pragma unroll
      for (int j = 0; j < 8; ++j) {
        float v = c8[j];
        v += __shfl_xor(v, 4); v += __shfl_xor(v, 8);
        c8[j] = red32(red16(v));
      }
      if (lane < 4) {
        float* dst = csp + kp * 2752 + grp * 32 + lane * 8;
        f32x4 v0 = {c8[0], c8[1], c8[2], c8[3]}, v1 = {c8[4], c8[5], c8[6], c8[7]};
        *(f32x4*)dst = v0; *(f32x4*)(dst + 4) = v1;
      }
    }
    asm volatile("" :: "v"(touch));
    BARE_BAR();
    return;
  }
  const int wr = wave >> 1, wc = wave & 1;
  const int q4 = (lane & 15) >> 2, p = lane & 3;
  unsigned touch0;
  {
    const int t = wave * 64 + lane;
    const int lx = idx * 128 + (t & 127);
    const char* px = (const char*)(x + (size_t)(xcd >> 1) * 512 * D_) + (size_t)lx * 128;
    const int lw_ = idx * 192 + (t < 192 ? t : 0);
    const char* pw = (const char*)wqkv + (size_t)(lw_ / 12) * 3072 + ((lw_ % 12) >> 2) * 1024 + (xcd & 1) * 512 + (lw_ & 3) * 128;
    touch0 = *(const volatile unsigned*)pw;
    if (t < 128) touch0 += *(const volatile unsigned*)px;
    if (t >= 240) touch0 += *(const volatile unsigned*)((const char*)wo + (size_t)(blockIdx.x * 16 + t - 240) * 128);
  }
  const int aoff = (wr * 64 + r) * 128 + ((g ^ ((r >> 1) & 7)) << 4);
  const int aoff0 = aoff + wc * 4096, aoff1 = aoff + (wc ^ 1) * 4096;
  const int boff = 16384 + (2 * g) * 768 + q4 * 64 + ((wc ^ (g & 1)) << 5) + p * 8;
  const u32x4 ones = ONES_FRAG;
  f32x4 accq[4] = {}, acck[4] = {}, accv[4] = {};
  f32x4 rs[2] = {}, sqm[2] = {}, cq = {}, ck = {}, cv = {};
  u32x4 fa[2][4], fb[2][3];
  auto rdfrags = [&](int t) {
    const char* slot = smem + ((t >> 1) % K1_NSLOT) * K1_SLOT;
    const int kk = t & 1;
#pragma unroll
    for (int i = 0; i < 4; ++i)
      fa[kk][i] = *(const u32x4*)(slot + (((i < 2 ? aoff0 : aoff1) + (i & 1) * 2048) ^ (kk << 6)));
#pragma unroll
    for (int j = 0; j < 3; ++j)
      fb[kk][j] = tr_frag(slot + boff + kk * 6144 + j * 256, slot + boff + kk * 6144 + 768 + j * 256);
  };
  auto compute = [&](int t) {
    const int kk = t & 1;
    const u32x4* a = fa[kk];
    const u32x4 bq = fb[kk][0], bk = fb[kk][1], bv = fb[kk][2];
#pragma unroll
    for (int i = 0; i < 4; ++i) {
      accq[i] = mfma16(bq, a[i], accq[i]);
      acck[i] = mfma16(a[i], bk, acck[i]);
      accv[i] = mfma16(a[i], bv, accv[i]);
    }
    rs[0] = mfma16(ones, a[0], rs[0]);
    rs[1] = mfma16(ones, a[1], rs[1]);
    sqm[0] = mfma16(a[0], a[0], sqm[0]);
    sqm[1] = mfma16(a[1], a[1], sqm[1]);
    if (kk == wr) {
      cq = mfma16(bq, ones, cq);
      ck = mfma16(ones, bk, ck);
      cv = mfma16(ones, bv, cv);
    }
  };
  BARE_BAR();
  STAMP(0,2);
  rdfrags(0);
#pragma unroll
  for (int s = 0; s < 8; ++s) {
    rdfrags(2 * s + 1);
    compute(2 * s);
    __builtin_amdgcn_sched_barrier(0);
    if (s + 1 < 8) {
      RAW_BAR();
      STAMP(0,3+s);
      rdfrags(2 * s + 2);
    }
    compute(2 * s + 1);
    __builtin_amdgcn_sched_barrier(0);
  }
  STAMP(0,11);
  float* st = (float*)(smem);
  float* csl = (float*)(smem + 2048);
  float* st2 = (float*)(smem + 4096);
  float* red = (float*)(smem + 8192);
  {
#pragma unroll
    for (int i = 0; i < 2; ++i) {
      const float q01 = (r & 1) ? sqm[i][1] : sqm[i][0], q23 = (r & 1) ? sqm[i][3] : sqm[i][2];
      const float q = (r & 2) ? q23 : q01;
      if (g == (r >> 2)) {
        f32x2 v = {rs[i][0], q};
        *(f32x2*)(st + (wr * 64 + wc * 32 + i * 16 + r) * 2) = v;
      }
    }
    float* c = csl + (wr * 2 + wc) * 48;
    if (r == 0) *(f32x4*)(c + 4 * g) = cq;
    if (g == 0) { c[16 + r] = ck[0]; c[32 + r] = cv[0]; }
  }
  __syncthreads();
  STAMP(0,12);
  {
    const f32x2 a0 = *(const f32x2*)(st + (wr * 64 + lane) * 2);
    const float mean = a0[0] * (1.f / 512.f);
    const float var = fmaxf(a0[1] * (1.f / 512.f) - mean * mean, 0.f);
    f32x2 v = {mean, __builtin_amdgcn_rsqf(var + 1e-5f)};
    *(f32x2*)(st2 + (wave * 64 + lane) * 2) = v;
  }
  const f32x4 csq4 = *(const f32x4*)(csl + wc * 48 + 4 * g) + *(const f32x4*)(csl + (2 + wc) * 48 + 4 * g);
  const float csk = csl[wc * 48 + 16 + r] + csl[(2 + wc) * 48 + 16 + r];
  const float csv = csl[wc * 48 + 32 + r] + csl[(2 + wc) * 48 + 32 + r];
  f32x4 kv = {}, zz = {};
#pragma unroll
  for (int ih = 0; ih < 2; ++ih) {
    u32x4 pk, vv;
#pragma unroll
    for (int i2 = 0; i2 < 2; ++i2) {
      const int i = ih * 2 + i2;
      const int mbl = ((ih ^ wc) << 1) + i2;
      {
        const f32x2 mi = *(const f32x2*)(st2 + (wave * 64 + mbl * 16 + r) * 2);
        float qv[4];
#pragma unroll
        for (int j = 0; j < 4; ++j) qv[j] = phi_f((accq[i][j] - mi[0] * csq4[j]) * mi[1]);
        u32x2 o = {pack_bf(qv[0], qv[1]), pack_bf(qv[2], qv[3])};
        const int mblk = rt * 8 + wr * 4 + mbl;
        *(u32x2*)(pq + ((size_t)(mblk * 16 + cg) * 64 + r + 16 * (2 * wc + (g >> 1))) * 8 + (g & 1) * 4) = o;
      }
      {
        const f32x4 m0 = *(const f32x4*)(st2 + (wave * 64 + mbl * 16 + 4 * g) * 2);
        const f32x4 m1 = *(const f32x4*)(st2 + (wave * 64 + mbl * 16 + 4 * g) * 2 + 4);
        const float mean[4] = {m0[0], m0[2], m1[0], m1[2]}, inv[4] = {m0[1], m0[3], m1[1], m1[3]};
        float kk[4], vj[4];
#pragma unroll
        for (int j = 0; j < 4; ++j) {
          kk[j] = phi_f((acck[i][j] - mean[j] * csk) * inv[j]);
          vj[j] = (accv[i][j] - mean[j] * csv) * inv[j];
        }
        pk[2 * i2] = pack_bf(kk[0], kk[1]);
        pk[2 * i2 + 1] = pack_bf(kk[2], kk[3]);
        vv[2 * i2] = pack_bf(vj[0], vj[1]);
        vv[2 * i2 + 1] = pack_bf(vj[2], vj[3]);
      }
    }
    kv = mfma16(pk, vv, kv);
    zz = mfma16(pk, ones, zz);
  }
  {
    float* rd = red + (wc * 2 + wr) * 272;
#pragma unroll
    for (int j = 0; j < 4; ++j) rd[(4 * g + j) * 16 + r] = kv[j];
    if (r == 0) *(f32x4*)(rd + 256 + 4 * g) = zz;
  }
  __syncthreads();
  for (int i = tid; i < 544; i += 256) {
    const int hh = i >= 272 ? 1 : 0, e = i - hh * 272;
    const float* rr = red + hh * 2 * 272 + e;
    part[(size_t)((cg * 2 + hh) * 16 + rt) * 272 + e] = rr[0] + rr[272];
  }
  asm volatile("" :: "v"(touch0));
  STAMP(0,13);
}

typedef __attribute__((address_space(1))) unsigned gu32;
DI void poll_ge(unsigned* c, unsigned target) {
  for (unsigned spins = 0; spins < (1u << 17); ++spins) {
    const unsigned v = __builtin_amdgcn_readfirstlane(
        __hip_atomic_load((gu32*)c, __ATOMIC_RELAXED, __HIP_MEMORY_SCOPE_AGENT));
    if (v >= target) break;
    __builtin_amdgcn_s_sleep(1);
  }
  asm volatile("" ::: "memory");
}
DI void glds16_sc1(const void* sbase, unsigned voff, unsigned dst) {
  asm volatile("s_mov_b32 m0, %2\n\ts_nop 0\n\tglobal_load_lds_dwordx4 %0, %1 sc1"
               :: "v"(voff), "s"(sbase), "s"(dst) : "memory");
}
DI void glds16v(const char* gsrc, unsigned dst) {
  asm volatile("s_mov_b32 m0, %1\n\ts_nop 0\n\tglobal_load_lds_dwordx4 %0, off" :: "v"(gsrc), "s"(dst) : "memory");
}
constexpr int K3_SLOT = 16384 + 24576, K3_NSLOT = 4;

__global__ __launch_bounds__(512) void kb_rest(const u16* __restrict__ x, const u16* __restrict__ pq,
                                               const float* __restrict__ part, const u16* __restrict__ wo,
                                               u16* wp, unsigned* cnt, u16* hf, float* hst,
                                               const float* __restrict__ csp,
                                               const u16* __restrict__ wg, const u16* __restrict__ wu,
                                               const u16* __restrict__ wd, u16* hhf, u16* __restrict__ out) {
  __shared__ __attribute__((aligned(16))) char smem[K3_SLOT * K3_NSLOT];
  const int tid = threadIdx.x, lane = tid & 63;
  const int wave = __builtin_amdgcn_readfirstlane(tid >> 6);
  const int xcd = blockIdx.x & 7, idx = blockIdx.x >> 3;
  const int r = lane & 15, g = lane >> 4;
  const int q4 = (lane & 15) >> 2, p = lane & 3;
  const __amdgpu_buffer_rsrc_t hrs = __builtin_amdgcn_make_buffer_rsrc(hf, 0, S_ * D_ * 2, 0x00020000);
  const __amdgpu_buffer_rsrc_t srs = __builtin_amdgcn_make_buffer_rsrc(hst, 0, S_ * 32 * 4 + 16 * 2752 * 4, 0x00020000);
  unsigned* cnt_w = cnt;
  unsigned* cnt_h = cnt + 8 * 64;
  unsigned* cnt_hh = cnt + 24 * 64;
  STAMP(2,0);
  {
    const int rt = xcd * 4 + (idx >> 3);
    const int ct = idx & 7;
    const int kg = wave >> 2, rb = wave & 3;
    const int mblk = rt * 4 + rb;
    const __amdgpu_buffer_rsrc_t wrs = __builtin_amdgcn_make_buffer_rsrc(wp, 0, 524288 + 8192, 0x00020000);
    const int ph = blockIdx.x >> 3, pc = blockIdx.x & 7;
    float* kv = (float*)(smem + 65536 + 16384);
    float pv[16];
    if (tid < 272) {
#pragma unroll
      for (int pp = 0; pp < 16; ++pp) pv[pp] = part[(size_t)(ph * 16 + pp) * 272 + tid];
    }
    float w[16];
    if (tid < 256) {
#pragma unroll
      for (int e = 0; e < 16; ++e) w[e] = bf_lo(wo[(size_t)(ph * 16 + e) * D_ + pc * 64 + (tid & 63)]);
    }
    float cpart[8];
    const int ccol = ((rt & 1) * 8 + ct) * 172 + (tid - 272);
    const bool do_cs = tid >= 272 && tid < 444;
    if (do_cs) {
#pragma unroll
      for (int kp = 0; kp < 8; ++kp) cpart[kp] = csp[kp * 2752 + ccol];
    }
    u32x4 a[8];
#pragma unroll
    for (int i = 0; i < 8; ++i) a[i] = *(const u32x4*)(pq + ((size_t)(mblk * 16 + kg * 8 + i) * 64 + lane) * 8);
    u32x2 xr[2];
#pragma unroll
    for (int j = 0; j < 2; ++j)
      xr[j] = *(const u32x2*)(x + (size_t)(mblk * 16 + r) * D_ + ct * 64 + (kg * 2 + j) * 16 + g * 4);
    if (tid < 272) {
      float s = 0.f;
#pragma unroll
      for (int pp = 0; pp < 16; ++pp) s += pv[pp];
      kv[tid] = s;
    }
    if (do_cs) {
      const float c = ((cpart[0] + cpart[1]) + (cpart[2] + cpart[3])) + ((cpart[4] + cpart[5]) + (cpart[6] + cpart[7]));
      __builtin_amdgcn_raw_buffer_store_b32(__float_as_uint(c), srs, (unsigned)(S_ * 32 * 4 + ((rt >> 1) * 2752 + ccol) * 4), 0, 16);
    }
    __syncthreads();
    STAMP(2,1);
    if (tid < 256) {
      const int c = tid & 63, dq = tid >> 6;
      float o[4];
#pragma unroll
      for (int j = 0; j < 4; ++j) {
        float s = 0.f;
#pragma unroll
        for (int e4 = 0; e4 < 4; ++e4) {
          const f32x4 k4 = *(const f32x4*)(kv + (dq * 4 + j) * 16 + e4 * 4);
          s = fmaf(k4[0], w[e4 * 4], s); s = fmaf(k4[1], w[e4 * 4 + 1], s);
          s = fmaf(k4[2], w[e4 * 4 + 2], s); s = fmaf(k4[3], w[e4 * 4 + 3], s);
        }
        o[j] = s;
      }
      const int n = pc * 64 + c, nb = n >> 4, cc = n & 15;
      const int ks = ph >> 1, gg = (ph & 1) * 2 + (dq >> 1);
      u32x2 v = {pack_bf(o[0], o[1]), pack_bf(o[2], o[3])};
      __builtin_amdgcn_raw_buffer_store_b64(v, wrs, (unsigned)((nb * 16 + ks) * 64 + gg * 16 + cc) * 16u + (dq & 1) * 8u, 0, 16);
    } else if (tid < 258) {
      const float* zs = kv + 256 + (tid - 256) * 8;
      u32x4 v = {pack_bf(zs[0], zs[1]), pack_bf(zs[2], zs[3]), pack_bf(zs[4], zs[5]), pack_bf(zs[6], zs[7])};
      __builtin_amdgcn_raw_buffer_store_b128(v, wrs, 524288u + (unsigned)(pc * 512 + ph * 16 + (tid - 256) * 8) * 2u, 0, 16);
    }
    WAIT_VM(0);
    __syncthreads();
    if (tid == 0) __hip_atomic_fetch_add((gu32*)(cnt_w + pc * 64), 1u, __ATOMIC_RELAXED, __HIP_MEMORY_SCOPE_AGENT);
    STAMP(2,2);
    unsigned touch = 0;
    if (wave >= 1) {
      const int t = (wave - 1) * 64 + lane;
      if (t < 344) touch += *(const unsigned*)((const char*)wd + (size_t)(idx * 344 + t) * 128);
      if (t < 384) {
        const int m = (t / 12) & 1, rr = idx * 16 + (t / 24), ln = t % 12;
        const size_t off = (size_t)rr * (F_ * 2) + (xcd & 1) * 1536 + (size_t)ln * 128;
        const size_t lim = (size_t)rr * (F_ * 2) + F_ * 2 - 4;
        touch += *(const unsigned*)((const char*)(m ? wu : wg) + (off < lim ? off : lim));
      }
    }
    if (wave == 0) poll_ge(cnt_w + ct * 64, 32u);
    __syncthreads();
    STAMP(2,3);
    u32x4 zf[8], wv[8];
#pragma unroll
    for (int i = 0; i < 8; ++i)
      zf[i] = __builtin_amdgcn_raw_buffer_load_b128(wrs, 524288u + (unsigned)(ct * 512 + (kg * 8 + i) * 32 + g * 8) * 2u, 0, 16);
#pragma unroll
    for (int i = 0; i < 8; ++i)
      wv[i] = __builtin_amdgcn_raw_buffer_load_b128(wrs, (unsigned)(ct * 65536 + (wave * 8 + i) * 1024 + lane * 16), 0, 16);
#pragma unroll
    for (int i = 0; i < 8; ++i) {
      float s = dot2(a[i][0], zf[i][0], 0.f);
      s = dot2(a[i][1], zf[i][1], s);
      s = dot2(a[i][2], zf[i][2], s);
      s = dot2(a[i][3], zf[i][3], s);
      const float inv = __builtin_amdgcn_rcpf(red16(s) + 1e-6f);
#pragma unroll
      for (int j = 0; j < 4; ++j) a[i][j] = pack_bf(bf_lo(a[i][j]) * inv, bf_hi(a[i][j]) * inv);
    }
#pragma unroll
    for (int i = 0; i < 8; ++i) *(u32x4*)(smem + (wave * 8 + i) * 1024 + lane * 16) = wv[i];
    __syncthreads();
    STAMP(2,4);
    f32x4 acc[4] = {};
#pragma unroll
    for (int i = 0; i < 8; ++i) {
#pragma unroll
      for (int nb = 0; nb < 4; ++nb) {
        const u32x4 b = *(const u32x4*)(smem + (nb * 16 + kg * 8 + i) * 1024 + lane * 16);
        acc[nb] = mfma16(b, a[i], acc[nb]);
      }
    }
    f32x4* xch = (f32x4*)(smem + 65536);
#pragma unroll
    for (int j = 0; j < 2; ++j) xch[(wave * 2 + j) * 64 + lane] = kg ? acc[j] : acc[2 + j];
    __syncthreads();
    float hs = 0.f, hq = 0.f;
    u32x2 oo[2];
#pragma unroll
    for (int j = 0; j < 2; ++j) {
      const f32x4 o4 = (kg ? acc[2 + j] : acc[j]) + xch[((wave ^ 4) * 2 + j) * 64 + lane];
      u32x2 o;
      o[0] = pack_bf(bf_lo(xr[j][0]) + o4[0], bf_hi(xr[j][0]) + o4[1]);
      o[1] = pack_bf(bf_lo(xr[j][1]) + o4[2], bf_hi(xr[j][1]) + o4[3]);
      oo[j] = o;
      hs = dot2(o[0], 0x3f803f80u, hs); hs = dot2(o[1], 0x3f803f80u, hs);
      hq = dot2(o[0], o[0], hq); hq = dot2(o[1], o[1], hq);
    }
    __builtin_amdgcn_raw_buffer_store_b128(
        merge16(oo[0], oo[1]), hrs, (unsigned)(((mblk * 16 + ct * 2 + kg) * 64 + r + 16 * (2 * (g & 1) + (g >> 1))) * 16), 0, 16);
    hs = red32(red16(hs)); hq = red32(red16(hq));
    if (g == 0) {
      u32x2 v = {__float_as_uint(hs), __float_as_uint(hq)};
      __builtin_amdgcn_raw_buffer_store_b64(v, srs, (unsigned)(((ct * 2 + kg) * S_ + mblk * 16 + r) * 8), 0, 16);
    }
    asm volatile("" :: "v"(touch));
    WAIT_VM(0);
    __syncthreads();
    if (tid == 0) __hip_atomic_fetch_add((gu32*)(cnt_h + (rt >> 1) * 64), 1u, __ATOMIC_RELAXED, __HIP_MEMORY_SCOPE_AGENT);
    STAMP(2,5);
  }
  const int rt3 = (xcd >> 1) * 4 + (idx >> 3);
  const int cg = (xcd & 1) * 8 + (idx & 7);
  const int f0 = cg < 11 ? cg * 96 : 1056 + (cg - 11) * 64;
  const int nvalid = cg < 11 ? 6 : 4;
  const int rt4 = xcd * 4 + (idx >> 3);
  const int ct4 = idx & 7;
  if (wave >= 4) {
    const int lw = wave - 4;
    const unsigned lbase = lds_addr(smem);
    {
      const char* abase = (const char*)(hf + (size_t)((rt3 * 8 + lw * 2) * 16) * 512);
      const long wdelta = (const char*)wu - (const char*)wg;
      const char* bsrc[6];
#pragma unroll
      for (int i = 0; i < 6; ++i) {
        const int line = (lw * 6 + i) * 4 + g;
        const int kq = line / 6, cgrp = line % 6;
        const int k = kq * 4 + ((lane >> 2) & 3);
        const int ch = (lane & 3) ^ (((k >> 3) & 1) << 1);
        int c3 = cgrp < 3 ? cgrp : cgrp - 3;
        c3 = (c3 * 2 < nvalid) ? c3 : 0;
        bsrc[i] = (const char*)(wg + f0) + (unsigned)(k * F_ + c3 * 32 + ch * 8) * 2u + (cgrp >= 3 ? wdelta : 0);
      }
      auto stA = [&](int s) {
        const unsigned slot = lbase + (s % K3_NSLOT) * K3_SLOT;
#pragma unroll
        for (int i = 0; i < 4; ++i)
          glds16_sc1(abase + (i >> 1) * 16384 + (s * 2 + (i & 1)) * 1024, lane * 16, slot + (lw * 4 + i) * 1024);
      };
      auto stB = [&](int s) {
        const unsigned slot = lbase + (s % K3_NSLOT) * K3_SLOT;
#pragma unroll
        for (int i = 0; i < 6; ++i) glds16v(bsrc[i] + (size_t)s * 64 * F_ * 2, slot + 16384 + (lw * 6 + i) * 1024);
      };
      stB(0); stB(1); stB(2); stB(3);
      poll_ge(cnt_h + rt3 * 64, 16u);
      stA(0); stA(1); stA(2); stA(3);
      wait_vm<12>(); BARE_BAR();
      wait_vm<8>();  BARE_BAR(); stA(4); stB(4);
      wait_vm<14>(); BARE_BAR(); stA(5); stB(5);
      wait_vm<20>(); BARE_BAR(); stA(6); stB(6);
      wait_vm<20>(); BARE_BAR(); stA(7); stB(7);
      wait_vm<20>(); BARE_BAR();
      wait_vm<10>(); BARE_BAR();
      wait_vm<0>();  BARE_BAR();
    }
    {
      const char* abase = (const char*)(hhf + (size_t)((rt4 * 4 + lw) * KS4) * 512);
      const char* bbase = (const char*)(wd + (size_t)lw * 32 * D_ + ct4 * 64);
      const unsigned boffs = (unsigned)(((g >> 1) * 4 + ((lane >> 2) & 3)) * D_ + (g & 1) * 32 + (lane & 3) * 8) * 2u;
      auto stA = [&](int s) {
        const unsigned slot = lbase + (s & 3) * 32768;
#pragma unroll
        for (int i = 0; i < 4; ++i)
          glds16_sc1(abase + (s * 4 + ((s == 10 && i == 3) ? 2 : i)) * 1024, lane * 16, slot + lw * 4096 + i * 1024);
      };
      auto stB = [&](int s) {
        const unsigned slot = lbase + (s & 3) * 32768;
        const int so = (s == 10 && lw == 3) ? s - 1 : s;
#pragma unroll
        for (int i = 0; i < 4; ++i)
          glds16(bbase + (size_t)(so * 128 + i * 8) * D_ * 2, (i & 1) ? (boffs ^ 32u) : boffs, slot + 16384 + lw * 4096 + i * 1024);
      };
      stB(0); stB(1); stB(2);
      BARE_BAR();
      stB(3);
      poll_ge(cnt_hh + (rt4 >> 1) * 64, 16u);
      stA(0); stA(1); stA(2); stA(3);
      wait_vm<12>(); BARE_BAR();
      wait_vm<8>();  BARE_BAR(); stA(4); stB(4);
      wait_vm<12>(); BARE_BAR(); stA(5); stB(5);
#pragma unroll
      for (int s = 2; s < 10; ++s) {
        if (s <= 7) wait_vm<16>(); else if (s == 8) wait_vm<8>(); else wait_vm<0>();
        BARE_BAR();
        if (s + 4 < 11) { stA(s + 4); stB(s + 4); }
      }
    }
    return;
  }
  const int wr = wave >> 1, wc = wave & 1;
  {
    const int aoff = wr * 8192 + lane * 16;
    const int bbase2 = 16384 + (2 * g) * 1536 + q4 * 64 + p * 8;
    int boff[3];
#pragma unroll
    for (int j = 0; j < 3; ++j) {
      const int fbl = 3 * wc + j;
      boff[j] = bbase2 + (fbl >> 1) * 256 + (((fbl & 1) ^ (g & 1)) << 5);
    }
    u32x2 hp[16];
    f32x4 cg4[3], cu4[3];
    float mean_l = 0.f, inv2_l = 0.f;
    f32x4 accg[4][3] = {}, accu[4][3] = {};
    u32x4 fa[2][4], fb[2][6];
    auto rdfrags = [&](int t) {
      const char* slot = smem + ((t >> 1) % K3_NSLOT) * K3_SLOT;
      const int kk = t & 1;
#pragma unroll
      for (int i = 0; i < 4; ++i) fa[kk][i] = *(const u32x4*)(slot + aoff + i * 2048 + kk * 1024);
#pragma unroll
      for (int j = 0; j < 3; ++j) {
        fb[kk][j] = tr_frag(slot + boff[j] + kk * 12288, slot + boff[j] + kk * 12288 + 1536);
        fb[kk][3 + j] = tr_frag(slot + boff[j] + 768 + kk * 12288, slot + boff[j] + 768 + kk * 12288 + 1536);
      }
    };
    auto compute = [&](int t) {
      const int kk = t & 1;
      const u32x4* a = fa[kk];
#pragma unroll
      for (int j = 0; j < 3; ++j) {
        const u32x4 bg = fb[kk][j], bu = fb[kk][3 + j];
#pragma unroll
        for (int i = 0; i < 4; ++i) {
          accg[i][j] = mfma16(bg, a[i], accg[i][j]);
          accu[i][j] = mfma16(bu, a[i], accu[i][j]);
        }
      }
    };
    BARE_BAR();
    STAMP(3,0);
    rdfrags(0);
#pragma unroll
    for (int s = 0; s < 8; ++s) {
      rdfrags(2 * s + 1);
      compute(2 * s);
      __builtin_amdgcn_sched_barrier(0);
      if (s + 1 < 8) {
        RAW_BAR();
        STAMP(3,1+s);
        rdfrags(2 * s + 2);
      }
      if (s == 4) {
#pragma unroll
        for (int i = 0; i < 16; ++i)
          hp[i] = __builtin_amdgcn_raw_buffer_load_b64(srs, (unsigned)((i * S_ + rt3 * 128 + wr * 64 + lane) * 8), 0, 16);
      }
      if (s == 6) {
        f32x2 t2 = {};
#pragma unroll
        for (int i = 0; i < 16; ++i) t2 += __builtin_bit_cast(f32x2, hp[i]);
        mean_l = t2[0] * (1.f / 512.f);
        const float var = fmaxf(t2[1] * (1.f / 512.f) - mean_l * mean_l, 0.f);
        inv2_l = __builtin_amdgcn_rcpf(var + 1e-5f);
        asm volatile("" : "+v"(mean_l), "+v"(inv2_l));
#pragma unroll
        for (int j = 0; j < 3; ++j) {
          const int fbl = (3 * wc + j) < nvalid ? (3 * wc + j) : 0;
          const unsigned co = (unsigned)(S_ * 32 * 4 + (rt3 * 2752 + f0 + fbl * 16 + 4 * g) * 4);
          cg4[j] = __builtin_bit_cast(f32x4, __builtin_amdgcn_raw_buffer_load_b128(srs, co, 0, 16));
          cu4[j] = __builtin_bit_cast(f32x4, __builtin_amdgcn_raw_buffer_load_b128(srs, co + F_ * 4, 0, 16));
        }
      }
      compute(2 * s + 1);
      __builtin_amdgcn_sched_barrier(0);
    }
    STAMP(3,9);
    const __amdgpu_buffer_rsrc_t hhrs = __builtin_amdgcn_make_buffer_rsrc(hhf, 0, 128 * KS4 * 1024, 0x00020000);
#pragma unroll
    for (int ip = 0; ip < 2; ++ip) {
      float mean[2], inv2[2];
#pragma unroll
      for (int i2 = 0; i2 < 2; ++i2) {
        mean[i2] = __shfl(mean_l, (ip * 2 + i2) * 16 + r);
        inv2[i2] = __shfl(inv2_l, (ip * 2 + i2) * 16 + r);
      }
      const int mblk = rt3 * 8 + wr * 4 + ip * 2 + (g & 1);
#pragma unroll
      for (int j = 0; j < 3; ++j) {
        const int fbl = 3 * wc + j;
        u32x2 o[2];
#pragma unroll
        for (int i2 = 0; i2 < 2; ++i2) {
          const int i = ip * 2 + i2;
          float hv[4];
#pragma unroll
          for (int e = 0; e < 4; ++e)
            hv[e] = ((accg[i][j][e] - mean[i2] * cg4[j][e]) * inv2[i2]) * (accu[i][j][e] - mean[i2] * cu4[j][e]);
          o[i2][0] = pack_bf(hv[0], hv[1]); o[i2][1] = pack_bf(hv[2], hv[3]);
        }
        const u32x4 v = merge16(o[0], o[1]);
        if (fbl < nvalid) {
          const int fb_ = (f0 >> 4) + fbl;
          __builtin_amdgcn_raw_buffer_store_b128(
              v, hhrs, (unsigned)(((mblk * KS4 + (fb_ >> 1)) * 64 + r + 16 * (2 * (fb_ & 1) + (g >> 1))) * 16), 0, 16);
        }
      }
    }
    WAIT_VM(0);
    RAW_BAR();
    if (tid == 0) __hip_atomic_fetch_add((gu32*)(cnt_hh + rt3 * 64), 1u, __ATOMIC_RELAXED, __HIP_MEMORY_SCOPE_AGENT);
    STAMP(3,10);
  }
  {
    const int boff = 16384 + (2 * g) * 512 + wc * 256 + q4 * 64 + p * 8;
    const int aoff = wr * 8192 + lane * 16;
    u32x2 hr[2][2];
    f32x4 acc[2][2] = {};
    u32x4 fa[2][2][2], fb[2][2][2];
    auto rdfrags = [&](int t) {
      const char* slot = smem + ((t >> 1) & 3) * 32768;
#pragma unroll
      for (int kk = 0; kk < 2; ++kk) {
        const int ksl = (t & 1) * 2 + kk;
#pragma unroll
        for (int mb = 0; mb < 2; ++mb) fa[t & 1][kk][mb] = *(const u32x4*)(slot + aoff + mb * 4096 + ksl * 1024);
#pragma unroll
        for (int nb = 0; nb < 2; ++nb) {
          const int ho = (nb ^ (g & 1)) << 5;
          fb[t & 1][kk][nb] = tr_frag(slot + boff + ksl * 4096 + ho, slot + boff + ksl * 4096 + 512 + ho);
        }
      }
    };
    auto compute = [&](int t) {
#pragma unroll
      for (int kk = 0; kk < 2; ++kk) {
        if (2 * t + kk < KS4) {
#pragma unroll
          for (int mb = 0; mb < 2; ++mb)
#pragma unroll
            for (int nb = 0; nb < 2; ++nb) acc[mb][nb] = mfma16(fb[t & 1][kk][nb], fa[t & 1][kk][mb], acc[mb][nb]);
        }
      }
    };
    BARE_BAR();
    STAMP(4,0);
    rdfrags(0);
#pragma unroll
    for (int mb = 0; mb < 2; ++mb)
#pragma unroll
      for (int nb = 0; nb < 2; ++nb)
        hr[mb][nb] = __builtin_amdgcn_raw_buffer_load_b64(
            hrs, (unsigned)((((rt4 * 4 + wr * 2 + mb) * 16 + ct4 * 2 + wc) * 64 + r + 16 * (2 * nb + (g >> 1))) * 16 + (g & 1) * 8), 0, 16);
#pragma unroll
    for (int s = 0; s < 11; ++s) {
      rdfrags(2 * s + 1);
      compute(2 * s);
      if (s + 1 < 11) {
        RAW_BAR();
        STAMP(4,1+s);
        rdfrags(2 * s + 2);
      }
      compute(2 * s + 1);
    }
#pragma unroll
    for (int mb = 0; mb < 2; ++mb)
#pragma unroll
      for (int nb = 0; nb < 2; ++nb) {
        const f32x4 o4 = acc[mb][nb];
        u32x2 o;
        o[0] = pack_bf(bf_lo(hr[mb][nb][0]) + o4[0], bf_hi(hr[mb][nb][0]) + o4[1]);
        o[1] = pack_bf(bf_lo(hr[mb][nb][1]) + o4[2], bf_hi(hr[mb][nb][1]) + o4[3]);
        *(u32x2*)(out + (size_t)(rt4 * 64 + wr * 32 + mb * 16 + r) * D_ + ct4 * 64 + wc * 32 + nb * 16 + g * 4) = o;
      }
    STAMP(4,14);
  }
}

extern "C" void kernel_launch(void* const* d_in, const int* in_sizes, int n_in, void* d_out, int out_size,
                              void* d_ws, size_t ws_size, hipStream_t stream) {
  const u16* x = (const u16*)d_in[0];
  const u16* wqkv = (const u16*)d_in[1];
  const u16* wo = (const u16*)d_in[2];
  const u16* wg = (const u16*)d_in[4];
  const u16* wu = (const u16*)d_in[5];
  const u16* wd = (const u16*)d_in[6];
  u16* out = (u16*)d_out;
  char* ws = (char*)d_ws;
  u16* pq = (u16*)(ws);
  u16* hf = (u16*)(ws + (2u << 20));
  u16* wp = (u16*)(ws + (4u << 20));
  float* part = (float*)(ws + (5u << 20));
  unsigned* cnt = (unsigned*)(ws + (6u << 20));
  float* csp = (float*)(ws + (6u << 20) + 65536);
  float* hst = (float*)(ws + (7u << 20));
  u16* hhf = (u16*)(ws + (8u << 20));
  k1_qkv<<<256, 512, 0, stream>>>(x, wqkv, pq, part, wg, wu, csp, cnt, wo, wd, out);
  kb_rest<<<256, 512, 0, stream>>>(x, pq, part, wo, wp, cnt, hf, hst, csp, wg, wu, wd, hhf, out);
}
```

```cpp
#include <hip/hip_runtime.h>
#include <hip/hip_bf16.h>
#include <stdint.h>

typedef unsigned short u16;
typedef __attribute__((ext_vector_type(8))) __bf16 bf16x8;
typedef __attribute__((ext_vector_type(2))) __bf16 bf16x2;
typedef __attribute__((ext_vector_type(4))) short s16x4;
typedef __attribute__((ext_vector_type(4))) float f32x4;
typedef __attribute__((ext_vector_type(2))) float f32x2;
typedef __attribute__((ext_vector_type(4))) unsigned u32x4;
typedef __attribute__((ext_vector_type(2))) unsigned u32x2;
typedef __attribute__((address_space(3))) s16x4* lds_s16x4_ptr;

#define DI __device__ __forceinline__
#define STAMP(kid,i)

constexpr int S_ = 2048, D_ = 512, F_ = 1376, H_ = 32;
constexpr int KS4 = F_ / 32;

DI float bf_lo(unsigned u) { return __uint_as_float(u << 16); }
DI float bf_hi(unsigned u) { return __uint_as_float(u & 0xffff0000u); }
DI unsigned pack_bf(float lo, float hi) {
  f32x2 f = {lo, hi};
  bf16x2 r = __builtin_convertvector(f, bf16x2);
  return __builtin_bit_cast(unsigned, r);
}
DI float bfr(float f) { return bf_lo(pack_bf(f, 0.f) & 0xffffu); }
DI f32x4 mfma16(u32x4 a, u32x4 b, f32x4 c) {
  return __builtin_amdgcn_mfma_f32_16x16x32_bf16(__builtin_bit_cast(bf16x8, a),
                                                 __builtin_bit_cast(bf16x8, b), c, 0, 0, 0);
}
DI float dot2(unsigned a, unsigned b, float c) {
  return __builtin_amdgcn_fdot2_f32_bf16(__builtin_bit_cast(bf16x2, a), __builtin_bit_cast(bf16x2, b), c, false);
}
DI u32x4 tr_frag(const char* lds0, const char* lds1) {
  s16x4 lo = __builtin_amdgcn_ds_read_tr16_b64_v4i16((lds_s16x4_ptr)lds0);
  s16x4 hi = __builtin_amdgcn_ds_read_tr16_b64_v4i16((lds_s16x4_ptr)lds1);
  u32x2 l2 = __builtin_bit_cast(u32x2, lo), h2 = __builtin_bit_cast(u32x2, hi);
  u32x4 r = {l2[0], l2[1], h2[0], h2[1]};
  return r;
}
DI unsigned lds_addr(char* l) { return (unsigned)(size_t)(__attribute__((address_space(3))) char*)l; }
DI void glds16(const void* sbase, unsigned voff, unsigned dst) {
  asm volatile("s_mov_b32 m0, %2\n\ts_nop 0\n\tglobal_load_lds_dwordx4 %0, %1"
               :: "v"(voff), "s"(sbase), "s"(dst) : "memory");
}
DI float red16(float s) {
  const unsigned u = __float_as_uint(s);
  auto r = __builtin_amdgcn_permlane16_swap(u, u, false, false);
  return __uint_as_float(r[0]) + __uint_as_float(r[1]);
}
DI float red32(float s) {
  const unsigned u = __float_as_uint(s);
  auto r = __builtin_amdgcn_permlane32_swap(u, u, false, false);
  return __uint_as_float(r[0]) + __uint_as_float(r[1]);
}
DI u32x4 merge16(u32x2 t0, u32x2 t1) {
  auto x = __builtin_amdgcn_permlane16_swap(t0[0], t1[0], false, false);
  auto y = __builtin_amdgcn_permlane16_swap(t0[1], t1[1], false, false);
  u32x4 v = {x[0], y[0], x[1], y[1]};
  return v;
}
#define WAIT_VM(n) asm volatile("s_waitcnt vmcnt(" #n ")" ::: "memory")
#define RAW_BAR() do { asm volatile("s_waitcnt lgkmcnt(0)" ::: "memory"); __builtin_amdgcn_s_barrier(); } while (0)
#define BARE_BAR() do { asm volatile("" ::: "memory"); __builtin_amdgcn_s_barrier(); } while (0)
template <int N> DI void wait_vm() { asm volatile("s_waitcnt vmcnt(%0)" ::"i"(N) : "memory"); }

DI float phi_f(float t) { return (t > 0.f ? t + 1.f : __expf(t)) + 1e-6f; }
DI void sq_frag(const u32x4& a, float& q) {
#pragma unroll
  for (int i = 0; i < 4; ++i) q = dot2(a[i], a[i], q);
}
#define ONES_FRAG ((u32x4){0x3f803f80u, 0x3f803f80u, 0x3f803f80u, 0x3f803f80u})

constexpr int K1_SLOT = 16384 + 12288, K1_NSLOT = 4;
__global__ __launch_bounds__(512) void k1_qkv(const u16* __restrict__ x, const u16* __restrict__ wqkv,
                                              u16* __restrict__ pq, float* __restrict__ part,
                                              const u16* __restrict__ wg, const u16* __restrict__ wu,
                                              float* __restrict__ csp, unsigned* __restrict__ cnt,
                                              const u16* __restrict__ wo, const u16* __restrict__ wd,
                                              const u16* outp) {
  __shared__ __attribute__((aligned(16))) char smem[K1_SLOT * K1_NSLOT];
  const int tid = threadIdx.x, lane = tid & 63;
  const int wave = __builtin_amdgcn_readfirstlane(tid >> 6);
  const int xcd = blockIdx.x & 7, idx = blockIdx.x >> 3;
  const int rt = (xcd >> 1) * 4 + (idx >> 3);
  const int cg = (xcd & 1) * 8 + (idx & 7);
  const int r = lane & 15, g = lane >> 4;
  STAMP(0,0);
  if (blockIdx.x == 0 && tid < 40) cnt[tid * 64] = 0u;
  if (wave >= 4) {
    const int lw = wave - 4;
    const unsigned lbase = lds_addr(smem);
    const unsigned aoffs = (unsigned)(lw * 32 + (lane >> 3)) * 1024u + (((lane & 7) ^ ((lane >> 4) & 3)) << 4);
    unsigned boffs[3];
#pragma unroll
    for (int i = 0; i < 3; ++i) {
      const int line = (lw * 3 + i) * 4 + g;
      const int kq = line / 3, cgrp = line % 3;
      const int k = kq * 4 + ((lane >> 2) & 3);
      const int ch = (lane & 3) ^ (((k >> 3) & 1) << 1);
      boffs[i] = (unsigned)(k * 1536 + cgrp * 512 + ch * 8) * 2u;
    }
    const char* abase = (const char*)(x + (size_t)rt * 128 * D_);
    const char* bbase = (const char*)(wqkv + cg * 32);
    auto stage = [&](int s) {
      const unsigned slot = lbase + (s % K1_NSLOT) * K1_SLOT;
#pragma unroll
      for (int i = 0; i < 4; ++i)
        glds16(abase + s * 128 + i * 8192, (i & 1) ? (aoffs ^ 64u) : aoffs, slot + lw * 4096 + i * 1024);
#pragma unroll
      for (int i = 0; i < 3; ++i)
        glds16(bbase + (size_t)s * 64 * 3072, boffs[i], slot + 16384 + (lw * 3 + i) * 1024);
    };
    stage(0); stage(1);
    wait_vm<7>();
    BARE_BAR();
    stage(2);
#pragma unroll
    for (int s = 0; s < 7; ++s) {
      if (s == 0 || s == 5) wait_vm<7>(); else if (s <= 4) wait_vm<14>(); else wait_vm<0>();
      BARE_BAR();
      if (s == 0) stage(3);
      if (s + 4 < 8) stage(s + 4);
    }
    const int W = blockIdx.x * 4 + lw;
    const int grp = W % 86, kp = (W / 86) & 7;
    u32x4 wv[4];
    {
      const u16* wsrc = (grp < 43 ? wg : wu) + (grp % 43) * 32 + (lane & 3) * 8 + (size_t)(kp * 64 + (lane >> 2)) * F_;
#pragma unroll
      for (int i = 0; i < 4; ++i) wv[i] = *(const u32x4*)(wsrc + (size_t)i * 16 * F_);
    }
    unsigned touch;
    {
      const int T = idx * 256 + lw * 64 + lane;
      const char* tp = (const char*)pq + (size_t)T * 4096;
      if (T >= 3584) tp = (const char*)outp + (size_t)(T - 3584) * 4096;
      if (T >= 4096) tp = (const char*)wo + (size_t)(T - 4096) * 4096;
      if (T >= 4224) tp = (const char*)wd + (size_t)(T - 4224) * 4096;
      if (T >= 4568) tp = (const char*)wg + (size_t)(T - 4568) * 4096;
      if (T >= 4912) tp = (const char*)wu + (size_t)(T - 4912) * 4096;
      if (T >= 5256) tp = (const char*)wu;
      touch = *(const volatile unsigned*)tp;
#pragma unroll
      for (int it = 0; it < 2; ++it) {
        const int t = it * 256 + lw * 64 + lane;
        if (t < 344) touch += *(const volatile unsigned*)((const char*)wd + (size_t)(idx * 344 + t) * 128);
        if (t < 384) {
          const int m = (t / 12) & 1, rr = idx * 16 + (t / 24), ln = t % 12;
          const size_t off = (size_t)rr * (F_ * 2) + (xcd & 1) * 1536 + (size_t)ln * 128;
          const size_t lim = (size_t)rr * (F_ * 2) + F_ * 2 - 4;
          touch += *(const volatile unsigned*)((const char*)(m ? wu : wg) + (off < lim ? off : lim));
        }
      }
    }
    BARE_BAR();
    if (W < 86 * 8) {
      float c8[8];
#pragma unroll
      for (int j = 0; j < 4; ++j) {
        c8[2 * j] = (bf_lo(wv[0][j]) + bf_lo(wv[1][j])) + (bf_lo(wv[2][j]) + bf_lo(wv[3][j]));
        c8[2 * j + 1] = (bf_hi(wv[0][j]) + bf_hi(wv[1][j])) + (bf_hi(wv[2][j]) + bf_hi(wv[3][j]));
      }
#pragma unroll
      for (int j = 0; j < 8; ++j) {
        float v = c8[j];
        v += __shfl_xor(v, 4); v += __shfl_xor(v, 8);
        c8[j] = red32(red16(v));
      }
      if (lane < 4) {
        float* dst = csp + kp * 2752 + grp * 32 + lane * 8;
        f32x4 v0 = {c8[0], c8[1], c8[2], c8[3]}, v1 = {c8[4], c8[5], c8[6], c8[7]};
        *(f32x4*)dst = v0; *(f32x4*)(dst + 4) = v1;
      }
    }
    asm volatile("" :: "v"(touch));
    BARE_BAR();
    return;
  }
  const int wr = wave >> 1, wc = wave & 1;
  const int q4 = (lane & 15) >> 2, p = lane & 3;
  unsigned touch0;
  {
    const int t = wave * 64 + lane;
    const int lx = idx * 128 + (t & 127);
    const char* px = (const char*)(x + (size_t)(xcd >> 1) * 512 * D_) + (size_t)lx * 128;
    const int lw_ = idx * 192 + (t < 192 ? t : 0);
    const char* pw = (const char*)wqkv + (size_t)(lw_ / 12) * 3072 + ((lw_ % 12) >> 2) * 1024 + (xcd & 1) * 512 + (lw_ & 3) * 128;
    touch0 = *(const volatile unsigned*)pw;
    if (t < 128) touch0 += *(const volatile unsigned*)px;
    if (t >= 240) touch0 += *(const volatile unsigned*)((const char*)wo + (size_t)(blockIdx.x * 16 + t - 240) * 128);
  }
  const int aoff = (wr * 64 + r) * 128 + ((g ^ ((r >> 1) & 7)) << 4);
  const int aoff0 = aoff + wc * 4096, aoff1 = aoff + (wc ^ 1) * 4096;
  const int boff = 16384 + (2 * g) * 768 + q4 * 64 + ((wc ^ (g & 1)) << 5) + p * 8;
  const u32x4 ones = ONES_FRAG;
  f32x4 accq[4] = {}, acck[4] = {}, accv[4] = {};
  f32x4 rs[2] = {}, sqm[2] = {}, cq = {}, ck = {}, cv = {};
  u32x4 fa[2][4], fb[2][3];
  auto rdfrags = [&](int t) {
    const char* slot = smem + ((t >> 1) % K1_NSLOT) * K1_SLOT;
    const int kk = t & 1;
#pragma unroll
    for (int i = 0; i < 4; ++i)
      fa[kk][i] = *(const u32x4*)(slot + (((i < 2 ? aoff0 : aoff1) + (i & 1) * 2048) ^ (kk << 6)));
#pragma unroll
    for (int j = 0; j < 3; ++j)
      fb[kk][j] = tr_frag(slot + boff + kk * 6144 + j * 256, slot + boff + kk * 6144 + 768 + j * 256);
  };
  auto compute = [&](int t) {
    const int kk = t & 1;
    const u32x4* a = fa[kk];
    const u32x4 bq = fb[kk][0], bk = fb[kk][1], bv = fb[kk][2];
#pragma unroll
    for (int i = 0; i < 4; ++i) {
      accq[i] = mfma16(bq, a[i], accq[i]);
      acck[i] = mfma16(a[i], bk, acck[i]);
      accv[i] = mfma16(a[i], bv, accv[i]);
    }
    rs[0] = mfma16(ones, a[0], rs[0]);
    rs[1] = mfma16(ones, a[1], rs[1]);
    sqm[0] = mfma16(a[0], a[0], sqm[0]);
    sqm[1] = mfma16(a[1], a[1], sqm[1]);
    if (kk == wr) {
      cq = mfma16(bq, ones, cq);
      ck = mfma16(ones, bk, ck);
      cv = mfma16(ones, bv, cv);
    }
  };
  BARE_BAR();
  STAMP(0,2);
  rdfrags(0);
#pragma unroll
  for (int s = 0; s < 8; ++s) {
    rdfrags(2 * s + 1);
    compute(2 * s);
    __builtin_amdgcn_sched_barrier(0);
    if (s + 1 < 8) {
      RAW_BAR();
      STAMP(0,3+s);
      rdfrags(2 * s + 2);
    }
    compute(2 * s + 1);
    __builtin_amdgcn_sched_barrier(0);
  }
  STAMP(0,11);
  float* st = (float*)(smem);
  float* csl = (float*)(smem + 2048);
  float* st2 = (float*)(smem + 4096);
  float* red = (float*)(smem + 8192);
  {
#pragma unroll
    for (int i = 0; i < 2; ++i) {
      const float q01 = (r & 1) ? sqm[i][1] : sqm[i][0], q23 = (r & 1) ? sqm[i][3] : sqm[i][2];
      const float q = (r & 2) ? q23 : q01;
      if (g == (r >> 2)) {
        f32x2 v = {rs[i][0], q};
        *(f32x2*)(st + (wr * 64 + wc * 32 + i * 16 + r) * 2) = v;
      }
    }
    float* c = csl + (wr * 2 + wc) * 48;
    if (r == 0) *(f32x4*)(c + 4 * g) = cq;
    if (g == 0) { c[16 + r] = ck[0]; c[32 + r] = cv[0]; }
  }
  __syncthreads();
  STAMP(0,12);
  {
    const f32x2 a0 = *(const f32x2*)(st + (wr * 64 + lane) * 2);
    const float mean = a0[0] * (1.f / 512.f);
    const float var = fmaxf(a0[1] * (1.f / 512.f) - mean * mean, 0.f);
    f32x2 v = {mean, __builtin_amdgcn_rsqf(var + 1e-5f)};
    *(f32x2*)(st2 + (wave * 64 + lane) * 2) = v;
  }
  const f32x4 csq4 = *(const f32x4*)(csl + wc * 48 + 4 * g) + *(const f32x4*)(csl + (2 + wc) * 48 + 4 * g);
  const float csk = csl[wc * 48 + 16 + r] + csl[(2 + wc) * 48 + 16 + r];
  const float csv = csl[wc * 48 + 32 + r] + csl[(2 + wc) * 48 + 32 + r];
  f32x4 kv = {}, zz = {};
#pragma unroll
  for (int ih = 0; ih < 2; ++ih) {
    u32x4 pk, vv;
#pragma unroll
    for (int i2 = 0; i2 < 2; ++i2) {
      const int i = ih * 2 + i2;
      const int mbl = ((ih ^ wc) << 1) + i2;
      {
        const f32x2 mi = *(const f32x2*)(st2 + (wave * 64 + mbl * 16 + r) * 2);
        float qv[4];
#pragma unroll
        for (int j = 0; j < 4; ++j) qv[j] = phi_f((accq[i][j] - mi[0] * csq4[j]) * mi[1]);
        u32x2 o = {pack_bf(qv[0], qv[1]), pack_bf(qv[2], qv[3])};
        const int mblk = rt * 8 + wr * 4 + mbl;
        *(u32x2*)(pq + ((size_t)(mblk * 16 + cg) * 64 + r + 16 * (2 * wc + (g >> 1))) * 8 + (g & 1) * 4) = o;
      }
      {
        const f32x4 m0 = *(const f32x4*)(st2 + (wave * 64 + mbl * 16 + 4 * g) * 2);
        const f32x4 m1 = *(const f32x4*)(st2 + (wave * 64 + mbl * 16 + 4 * g) * 2 + 4);
        const float mean[4] = {m0[0], m0[2], m1[0], m1[2]}, inv[4] = {m0[1], m0[3], m1[1], m1[3]};
        float kk[4], vj[4];
#pragma unroll
        for (int j = 0; j < 4; ++j) {
          kk[j] = phi_f((acck[i][j] - mean[j] * csk) * inv[j]);
          vj[j] = (accv[i][j] - mean[j] * csv) * inv[j];
        }
        pk[2 * i2] = pack_bf(kk[0], kk[1]);
        pk[2 * i2 + 1] = pack_bf(kk[2], kk[3]);
        vv[2 * i2] = pack_bf(vj[0], vj[1]);
        vv[2 * i2 + 1] = pack_bf(vj[2], vj[3]);
      }
    }
    kv = mfma16(pk, vv, kv);
    zz = mfma16(pk, ones, zz);
  }
  {
    float* rd = red + (wc * 2 + wr) * 272;
#pragma unroll
    for (int j = 0; j < 4; ++j) rd[(4 * g + j) * 16 + r] = kv[j];
    if (r == 0) *(f32x4*)(rd + 256 + 4 * g) = zz;
  }
  __syncthreads();
  for (int i = tid; i < 544; i += 256) {
    const int hh = i >= 272 ? 1 : 0, e = i - hh * 272;
    const float* rr = red + hh * 2 * 272 + e;
    part[(size_t)((cg * 2 + hh) * 16 + rt) * 272 + e] = rr[0] + rr[272];
  }
  asm volatile("" :: "v"(touch0));
  STAMP(0,13);
}

typedef __attribute__((address_space(1))) unsigned gu32;
DI void poll_ge(unsigned* c, unsigned target) {
  for (unsigned spins = 0; spins < (1u << 17); ++spins) {
    const unsigned v = __builtin_amdgcn_readfirstlane(
        __hip_atomic_load((gu32*)c, __ATOMIC_RELAXED, __HIP_MEMORY_SCOPE_AGENT));
    if (v >= target) break;
    __builtin_amdgcn_s_sleep(1);
  }
  asm volatile("" ::: "memory");
}
DI void glds16_sc1(const void* sbase, unsigned voff, unsigned dst) {
  asm volatile("s_mov_b32 m0, %2\n\ts_nop 0\n\tglobal_load_lds_dwordx4 %0, %1 sc1"
               :: "v"(voff), "s"(sbase), "s"(dst) : "memory");
}
DI void glds16v(const char* gsrc, unsigned dst) {
  asm volatile("s_mov_b32 m0, %1\n\ts_nop 0\n\tglobal_load_lds_dwordx4 %0, off" :: "v"(gsrc), "s"(dst) : "memory");
}
constexpr int K3_SLOT = 16384 + 24576, K3_NSLOT = 4;

__global__ __launch_bounds__(512) void kb_rest(const u16* __restrict__ x, const u16* __restrict__ pq,
                                               const float* __restrict__ part, const u16* __restrict__ wo,
                                               u16* wp, unsigned* cnt, u16* hf, float* hst,
                                               const float* __restrict__ csp,
                                               const u16* __restrict__ wg, const u16* __restrict__ wu,
                                               const u16* __restrict__ wd, u16* hhf, u16* __restrict__ out) {
  __shared__ __attribute__((aligned(16))) char smem[K3_SLOT * K3_NSLOT];
  const int tid = threadIdx.x, lane = tid & 63;
  const int wave = __builtin_amdgcn_readfirstlane(tid >> 6);
  const int xcd = blockIdx.x & 7, idx = blockIdx.x >> 3;
  const int r = lane & 15, g = lane >> 4;
  const int q4 = (lane & 15) >> 2, p = lane & 3;
  const __amdgpu_buffer_rsrc_t hrs = __builtin_amdgcn_make_buffer_rsrc(hf, 0, S_ * D_ * 2, 0x00020000);
  const __amdgpu_buffer_rsrc_t srs = __builtin_amdgcn_make_buffer_rsrc(hst, 0, S_ * 32 * 4 + 16 * 2752 * 4, 0x00020000);
  unsigned* cnt_w = cnt;
  unsigned* cnt_h = cnt + 8 * 64;
  unsigned* cnt_hh = cnt + 24 * 64;
  STAMP(2,0);
  {
    const int rt = xcd * 4 + (idx >> 3);
    const int ct = idx & 7;
    const int kg = wave >> 2, rb = wave & 3;
    const int mblk = rt * 4 + rb;
    const __amdgpu_buffer_rsrc_t wrs = __builtin_amdgcn_make_buffer_rsrc(wp, 0, 524288 + 8192, 0x00020000);
    const int ph = blockIdx.x >> 3, pc = blockIdx.x & 7;
    float* kv = (float*)(smem + 65536 + 16384);
    float pv[16];
    if (tid < 272) {
#pragma unroll
      for (int pp = 0; pp < 16; ++pp) pv[pp] = part[(size_t)(ph * 16 + pp) * 272 + tid];
    }
    float w[16];
    if (tid < 256) {
#pragma unroll
      for (int e = 0; e < 16; ++e) w[e] = bf_lo(wo[(size_t)(ph * 16 + e) * D_ + pc * 64 + (tid & 63)]);
    }
    if (tid < 272) {
      float s = 0.f;
#pragma unroll
      for (int pp = 0; pp < 16; ++pp) s += pv[pp];
      kv[tid] = s;
    }
    __syncthreads();
    STAMP(2,1);
    if (tid < 256) {
      const int c = tid & 63, dq = tid >> 6;
      float o[4];
#pragma unroll
      for (int j = 0; j < 4; ++j) {
        float s = 0.f;
#pragma unroll
        for (int e4 = 0; e4 < 4; ++e4) {
          const f32x4 k4 = *(const f32x4*)(kv + (dq * 4 + j) * 16 + e4 * 4);
          s = fmaf(k4[0], w[e4 * 4], s); s = fmaf(k4[1], w[e4 * 4 + 1], s);
          s = fmaf(k4[2], w[e4 * 4 + 2], s); s = fmaf(k4[3], w[e4 * 4 + 3], s);
        }
        o[j] = s;
      }
      const int n = pc * 64 + c, nb = n >> 4, cc = n & 15;
      const int ks = ph >> 1, gg = (ph & 1) * 2 + (dq >> 1);
      u32x2 v = {pack_bf(o[0], o[1]), pack_bf(o[2], o[3])};
      __builtin_amdgcn_raw_buffer_store_b64(v, wrs, (unsigned)((nb * 16 + ks) * 64 + gg * 16 + cc) * 16u + (dq & 1) * 8u, 0, 16);
    } else if (tid < 258) {
      const float* zs = kv + 256 + (tid - 256) * 8;
      u32x4 v = {pack_bf(zs[0], zs[1]), pack_bf(zs[2], zs[3]), pack_bf(zs[4], zs[5]), pack_bf(zs[6], zs[7])};
      __builtin_amdgcn_raw_buffer_store_b128(v, wrs, 524288u + (unsigned)(pc * 512 + ph * 16 + (tid - 256) * 8) * 2u, 0, 16);
    }
    WAIT_VM(0);
    __syncthreads();
    if (tid == 0) __hip_atomic_fetch_add((gu32*)(cnt_w + pc * 64), 1u, __ATOMIC_RELAXED, __HIP_MEMORY_SCOPE_AGENT);
    STAMP(2,2);
    float cpart[8];
    const int ccol = ((rt & 1) * 8 + ct) * 172 + (tid - 272);
    const bool do_cs = tid >= 272 && tid < 444;
    if (do_cs) {
#pragma unroll
      for (int kp = 0; kp < 8; ++kp) cpart[kp] = csp[kp * 2752 + ccol];
    }
    u32x4 a[8];
#pragma unroll
    for (int i = 0; i < 8; ++i) a[i] = *(const u32x4*)(pq + ((size_t)(mblk * 16 + kg * 8 + i) * 64 + lane) * 8);
    u32x2 xr[2];
#pragma unroll
    for (int j = 0; j < 2; ++j)
      xr[j] = *(const u32x2*)(x + (size_t)(mblk * 16 + r) * D_ + ct * 64 + (kg * 2 + j) * 16 + g * 4);
    if (do_cs) {
      const float c = ((cpart[0] + cpart[1]) + (cpart[2] + cpart[3])) + ((cpart[4] + cpart[5]) + (cpart[6] + cpart[7]));
      __builtin_amdgcn_raw_buffer_store_b32(__float_as_uint(c), srs, (unsigned)(S_ * 32 * 4 + ((rt >> 1) * 2752 + ccol) * 4), 0, 16);
    }
    if (wave == 0) poll_ge(cnt_w + ct * 64, 32u);
    __syncthreads();
    STAMP(2,3);
    u32x4 zf[8], wv[8];
#pragma unroll
    for (int i = 0; i < 8; ++i)
      zf[i] = __builtin_amdgcn_raw_buffer_load_b128(wrs, 524288u + (unsigned)(ct * 512 + (kg * 8 + i) * 32 + g * 8) * 2u, 0, 16);
#pragma unroll
    for (int i = 0; i < 8; ++i)
      wv[i] = __builtin_amdgcn_raw_buffer_load_b128(wrs, (unsigned)(ct * 65536 + (wave * 8 + i) * 1024 + lane * 16), 0, 16);
#pragma unroll
    for (int i = 0; i < 8; ++i) {
      float s = dot2(a[i][0], zf[i][0], 0.f);
      s = dot2(a[i][1], zf[i][1], s);
      s = dot2(a[i][2], zf[i][2], s);
      s = dot2(a[i][3], zf[i][3], s);
      const float inv = __builtin_amdgcn_rcpf(red16(s) + 1e-6f);
#pragma unroll
      for (int j = 0; j < 4; ++j) a[i][j] = pack_bf(bf_lo(a[i][j]) * inv, bf_hi(a[i][j]) * inv);
    }
#pragma unroll
    for (int i = 0; i < 8; ++i) *(u32x4*)(smem + (wave * 8 + i) * 1024 + lane * 16) = wv[i];
    __syncthreads();
    STAMP(2,4);
    f32x4 acc[4] = {};
#pragma unroll
    for (int i = 0; i < 8; ++i) {
#pragma unroll
      for (int nb = 0; nb < 4; ++nb) {
        const u32x4 b = *(const u32x4*)(smem + (nb * 16 + kg * 8 + i) * 1024 + lane * 16);
        acc[nb] = mfma16(b, a[i], acc[nb]);
      }
    }
    f32x4* xch = (f32x4*)(smem + 65536);
#pragma unroll
    for (int j = 0; j < 2; ++j) xch[(wave * 2 + j) * 64 + lane] = kg ? acc[j] : acc[2 + j];
    __syncthreads();
    float hs = 0.f, hq = 0.f;
    u32x2 oo[2];
#pragma unroll
    for (int j = 0; j < 2; ++j) {
      const f32x4 o4 = (kg ? acc[2 + j] : acc[j]) + xch[((wave ^ 4) * 2 + j) * 64 + lane];
      u32x2 o;
      o[0] = pack_bf(bf_lo(xr[j][0]) + o4[0], bf_hi(xr[j][0]) + o4[1]);
      o[1] = pack_bf(bf_lo(xr[j][1]) + o4[2], bf_hi(xr[j][1]) + o4[3]);
      oo[j] = o;
      hs = dot2(o[0], 0x3f803f80u, hs); hs = dot2(o[1], 0x3f803f80u, hs);
      hq = dot2(o[0], o[0], hq); hq = dot2(o[1], o[1], hq);
    }
    __builtin_amdgcn_raw_buffer_store_b128(
        merge16(oo[0], oo[1]), hrs, (unsigned)(((mblk * 16 + ct * 2 + kg) * 64 + r + 16 * (2 * (g & 1) + (g >> 1))) * 16), 0, 16);
    hs = red32(red16(hs)); hq = red32(red16(hq));
    if (g == 0) {
      u32x2 v = {__float_as_uint(hs), __float_as_uint(hq)};
      __builtin_amdgcn_raw_buffer_store_b64(v, srs, (unsigned)(((ct * 2 + kg) * S_ + mblk * 16 + r) * 8), 0, 16);
    }
    WAIT_VM(0);
    __syncthreads();
    if (tid == 0) __hip_atomic_fetch_add((gu32*)(cnt_h + (rt >> 1) * 64), 1u, __ATOMIC_RELAXED, __HIP_MEMORY_SCOPE_AGENT);
    STAMP(2,5);
  }
  const int rt3 = (xcd >> 1) * 4 + (idx >> 3);
  const int cg = (xcd & 1) * 8 + (idx & 7);
  const int f0 = cg < 11 ? cg * 96 : 1056 + (cg - 11) * 64;
  const int nvalid = cg < 11 ? 6 : 4;
  const int rt4 = xcd * 4 + (idx >> 3);
  const int ct4 = idx & 7;
  if (wave >= 4) {
    const int lw = wave - 4;
    const unsigned lbase = lds_addr(smem);
    {
      const char* abase = (const char*)(hf + (size_t)((rt3 * 8 + lw * 2) * 16) * 512);
      const long wdelta = (const char*)wu - (const char*)wg;
      const char* bsrc[6];
#pragma unroll
      for (int i = 0; i < 6; ++i) {
        const int line = (lw * 6 + i) * 4 + g;
        const int kq = line / 6, cgrp = line % 6;
        const int k = kq * 4 + ((lane >> 2) & 3);
        const int ch = (lane & 3) ^ (((k >> 3) & 1) << 1);
        int c3 = cgrp < 3 ? cgrp : cgrp - 3;
        c3 = (c3 * 2 < nvalid) ? c3 : 0;
        bsrc[i] = (const char*)(wg + f0) + (unsigned)(k * F_ + c3 * 32 + ch * 8) * 2u + (cgrp >= 3 ? wdelta : 0);
      }
      auto stA = [&](int s) {
        const unsigned slot = lbase + (s % K3_NSLOT) * K3_SLOT;
#pragma unroll
        for (int i = 0; i < 4; ++i)
          glds16_sc1(abase + (i >> 1) * 16384 + (s * 2 + (i & 1)) * 1024, lane * 16, slot + (lw * 4 + i) * 1024);
      };
      auto stB = [&](int s) {
        const unsigned slot = lbase + (s % K3_NSLOT) * K3_SLOT;
#pragma unroll
        for (int i = 0; i < 6; ++i) glds16v(bsrc[i] + (size_t)s * 64 * F_ * 2, slot + 16384 + (lw * 6 + i) * 1024);
      };
      stB(0); stB(1); stB(2); stB(3);
      poll_ge(cnt_h + rt3 * 64, 16u);
      stA(0); stA(1); stA(2); stA(3);
      wait_vm<12>(); BARE_BAR();
      wait_vm<8>();  BARE_BAR(); stA(4); stB(4);
      wait_vm<14>(); BARE_BAR(); stA(5); stB(5);
      wait_vm<20>(); BARE_BAR(); stA(6); stB(6);
      wait_vm<20>(); BARE_BAR(); stA(7); stB(7);
      wait_vm<20>(); BARE_BAR();
      wait_vm<10>(); BARE_BAR();
      wait_vm<0>();  BARE_BAR();
    }
    {
      const char* abase = (const char*)(hhf + (size_t)((rt4 * 4 + lw) * KS4) * 512);
      const char* bbase = (const char*)(wd + (size_t)lw * 32 * D_ + ct4 * 64);
      const unsigned boffs = (unsigned)(((g >> 1) * 4 + ((lane >> 2) & 3)) * D_ + (g & 1) * 32 + (lane & 3) * 8) * 2u;
      auto stA = [&](int s) {
        const unsigned slot = lbase + (s & 3) * 32768;
#pragma unroll
        for (int i = 0; i < 4; ++i)
          glds16_sc1(abase + (s * 4 + ((s == 10 && i == 3) ? 2 : i)) * 1024, lane * 16, slot + lw * 4096 + i * 1024);
      };
      auto stB = [&](int s) {
        const unsigned slot = lbase + (s & 3) * 32768;
        const int so = (s == 10 && lw == 3) ? s - 1 : s;
#pragma unroll
        for (int i = 0; i < 4; ++i)
          glds16(bbase + (size_t)(so * 128 + i * 8) * D_ * 2, (i & 1) ? (boffs ^ 32u) : boffs, slot + 16384 + lw * 4096 + i * 1024);
      };
      stB(0); stB(1); stB(2);
      BARE_BAR();
      stB(3);
      poll_ge(cnt_hh + (rt4 >> 1) * 64, 16u);
      stA(0); stA(1); stA(2); stA(3);
      wait_vm<12>(); BARE_BAR();
      wait_vm<8>();  BARE_BAR(); stA(4); stB(4);
      wait_vm<12>(); BARE_BAR(); stA(5); stB(5);
#pragma unroll
      for (int s = 2; s < 10; ++s) {
        if (s <= 7) wait_vm<16>(); else if (s == 8) wait_vm<8>(); else wait_vm<0>();
        BARE_BAR();
        if (s + 4 < 11) { stA(s + 4); stB(s + 4); }
      }
    }
    return;
  }
  const int wr = wave >> 1, wc = wave & 1;
  {
    const int aoff = wr * 8192 + lane * 16;
    const int bbase2 = 16384 + (2 * g) * 1536 + q4 * 64 + p * 8;
    int boff[3];
#pragma unroll
    for (int j = 0; j < 3; ++j) {
      const int fbl = 3 * wc + j;
      boff[j] = bbase2 + (fbl >> 1) * 256 + (((fbl & 1) ^ (g & 1)) << 5);
    }
    u32x2 hp[16];
    f32x4 cg4[3], cu4[3];
    float mean_l = 0.f, inv2_l = 0.f;
    f32x4 accg[4][3] = {}, accu[4][3] = {};
    u32x4 fa[2][4], fb[2][6];
    auto rdfrags = [&](int t) {
      const char* slot = smem + ((t >> 1) % K3_NSLOT) * K3_SLOT;
      const int kk = t & 1;
#pragma unroll
      for (int i = 0; i < 4; ++i) fa[kk][i] = *(const u32x4*)(slot + aoff + i * 2048 + kk * 1024);
#pragma unroll
      for (int j = 0; j < 3; ++j) {
        fb[kk][j] = tr_frag(slot + boff[j] + kk * 12288, slot + boff[j] + kk * 12288 + 1536);
        fb[kk][3 + j] = tr_frag(slot + boff[j] + 768 + kk * 12288, slot + boff[j] + 768 + kk * 12288 + 1536);
      }
    };
    auto compute = [&](int t) {
      const int kk = t & 1;
      const u32x4* a = fa[kk];
#pragma unroll
      for (int j = 0; j < 3; ++j) {
        const u32x4 bg = fb[kk][j], bu = fb[kk][3 + j];
#pragma unroll
        for (int i = 0; i < 4; ++i) {
          accg[i][j] = mfma16(bg, a[i], accg[i][j]);
          accu[i][j] = mfma16(bu, a[i], accu[i][j]);
        }
      }
    };
    BARE_BAR();
    STAMP(3,0);
    rdfrags(0);
#pragma unroll
    for (int s = 0; s < 8; ++s) {
      rdfrags(2 * s + 1);
      compute(2 * s);
      __builtin_amdgcn_sched_barrier(0);
      if (s + 1 < 8) {
        RAW_BAR();
        STAMP(3,1+s);
        rdfrags(2 * s + 2);
      }
      if (s == 4) {
#pragma unroll
        for (int i = 0; i < 16; ++i)
          hp[i] = __builtin_amdgcn_raw_buffer_load_b64(srs, (unsigned)((i * S_ + rt3 * 128 + wr * 64 + lane) * 8), 0, 16);
      }
      if (s == 6) {
        f32x2 t2 = {};
#pragma unroll
        for (int i = 0; i < 16; ++i) t2 += __builtin_bit_cast(f32x2, hp[i]);
        mean_l = t2[0] * (1.f / 512.f);
        const float var = fmaxf(t2[1] * (1.f / 512.f) - mean_l * mean_l, 0.f);
        inv2_l = __builtin_amdgcn_rcpf(var + 1e-5f);
        asm volatile("" : "+v"(mean_l), "+v"(inv2_l));
#pragma unroll
        for (int j = 0; j < 3; ++j) {
          const int fbl = (3 * wc + j) < nvalid ? (3 * wc + j) : 0;
          const unsigned co = (unsigned)(S_ * 32 * 4 + (rt3 * 2752 + f0 + fbl * 16 + 4 * g) * 4);
          cg4[j] = __builtin_bit_cast(f32x4, __builtin_amdgcn_raw_buffer_load_b128(srs, co, 0, 16));
          cu4[j] = __builtin_bit_cast(f32x4, __builtin_amdgcn_raw_buffer_load_b128(srs, co + F_ * 4, 0, 16));
        }
      }
      compute(2 * s + 1);
      __builtin_amdgcn_sched_barrier(0);
    }
    STAMP(3,9);
    const __amdgpu_buffer_rsrc_t hhrs = __builtin_amdgcn_make_buffer_rsrc(hhf, 0, 128 * KS4 * 1024, 0x00020000);
#pragma unroll
    for (int ip = 0; ip < 2; ++ip) {
      float mean[2], inv2[2];
#pragma unroll
      for (int i2 = 0; i2 < 2; ++i2) {
        mean[i2] = __shfl(mean_l, (ip * 2 + i2) * 16 + r);
        inv2[i2] = __shfl(inv2_l, (ip * 2 + i2) * 16 + r);
      }
      const int mblk = rt3 * 8 + wr * 4 + ip * 2 + (g & 1);
#pragma unroll
      for (int j = 0; j < 3; ++j) {
        const int fbl = 3 * wc + j;
        u32x2 o[2];
#pragma unroll
        for (int i2 = 0; i2 < 2; ++i2) {
          const int i = ip * 2 + i2;
          float hv[4];
#pragma unroll
          for (int e = 0; e < 4; ++e)
            hv[e] = ((accg[i][j][e] - mean[i2] * cg4[j][e]) * inv2[i2]) * (accu[i][j][e] - mean[i2] * cu4[j][e]);
          o[i2][0] = pack_bf(hv[0], hv[1]); o[i2][1] = pack_bf(hv[2], hv[3]);
        }
        const u32x4 v = merge16(o[0], o[1]);
        if (fbl < nvalid) {
          const int fb_ = (f0 >> 4) + fbl;
          __builtin_amdgcn_raw_buffer_store_b128(
              v, hhrs, (unsigned)(((mblk * KS4 + (fb_ >> 1)) * 64 + r + 16 * (2 * (fb_ & 1) + (g >> 1))) * 16), 0, 16);
        }
      }
    }
    WAIT_VM(0);
    RAW_BAR();
    if (tid == 0) __hip_atomic_fetch_add((gu32*)(cnt_hh + rt3 * 64), 1u, __ATOMIC_RELAXED, __HIP_MEMORY_SCOPE_AGENT);
    STAMP(3,10);
  }
  {
    const int boff = 16384 + (2 * g) * 512 + wc * 256 + q4 * 64 + p * 8;
    const int aoff = wr * 8192 + lane * 16;
    u32x2 hr[2][2];
    f32x4 acc[2][2] = {};
    u32x4 fa[2][2][2], fb[2][2][2];
    auto rdfrags = [&](int t) {
      const char* slot = smem + ((t >> 1) & 3) * 32768;
#pragma unroll
      for (int kk = 0; kk < 2; ++kk) {
        const int ksl = (t & 1) * 2 + kk;
#pragma unroll
        for (int mb = 0; mb < 2; ++mb) fa[t & 1][kk][mb] = *(const u32x4*)(slot + aoff + mb * 4096 + ksl * 1024);
#pragma unroll
        for (int nb = 0; nb < 2; ++nb) {
          const int ho = (nb ^ (g & 1)) << 5;
          fb[t & 1][kk][nb] = tr_frag(slot + boff + ksl * 4096 + ho, slot + boff + ksl * 4096 + 512 + ho);
        }
      }
    };
    auto compute = [&](int t) {
#pragma unroll
      for (int kk = 0; kk < 2; ++kk) {
        if (2 * t + kk < KS4) {
#pragma unroll
          for (int mb = 0; mb < 2; ++mb)
#pragma unroll
            for (int nb = 0; nb < 2; ++nb) acc[mb][nb] = mfma16(fb[t & 1][kk][nb], fa[t & 1][kk][mb], acc[mb][nb]);
        }
      }
    };
    BARE_BAR();
    STAMP(4,0);
    rdfrags(0);
#pragma unroll
    for (int mb = 0; mb < 2; ++mb)
#pragma unroll
      for (int nb = 0; nb < 2; ++nb)
        hr[mb][nb] = __builtin_amdgcn_raw_buffer_load_b64(
            hrs, (unsigned)((((rt4 * 4 + wr * 2 + mb) * 16 + ct4 * 2 + wc) * 64 + r + 16 * (2 * nb + (g >> 1))) * 16 + (g & 1) * 8), 0, 16);
#pragma unroll
    for (int s = 0; s < 11; ++s) {
      rdfrags(2 * s + 1);
      compute(2 * s);
      if (s + 1 < 11) {
        RAW_BAR();
        STAMP(4,1+s);
        rdfrags(2 * s + 2);
      }
      compute(2 * s + 1);
    }
#pragma unroll
    for (int mb = 0; mb < 2; ++mb)
#pragma unroll
      for (int nb = 0; nb < 2; ++nb) {
        const f32x4 o4 = acc[mb][nb];
        u32x2 o;
        o[0] = pack_bf(bf_lo(hr[mb][nb][0]) + o4[0], bf_hi(hr[mb][nb][0]) + o4[1]);
        o[1] = pack_bf(bf_lo(hr[mb][nb][1]) + o4[2], bf_hi(hr[mb][nb][1]) + o4[3]);
        *(u32x2*)(out + (size_t)(rt4 * 64 + wr * 32 + mb * 16 + r) * D_ + ct4 * 64 + wc * 32 + nb * 16 + g * 4) = o;
      }
    STAMP(4,14);
  }
}

extern "C" void kernel_launch(void* const* d_in, const int* in_sizes, int n_in, void* d_out, int out_size,
                              void* d_ws, size_t ws_size, hipStream_t stream) {
  const u16* x = (const u16*)d_in[0];
  const u16* wqkv = (const u16*)d_in[1];
  const u16* wo = (const u16*)d_in[2];
  const u16* wg = (const u16*)d_in[4];
  const u16* wu = (const u16*)d_in[5];
  const u16* wd = (const u16*)d_in[6];
  u16* out = (u16*)d_out;
  char* ws = (char*)d_ws;
  u16* pq = (u16*)(ws);
  u16* hf = (u16*)(ws + (2u << 20));
  u16* wp = (u16*)(ws + (4u << 20));
  float* part = (float*)(ws + (5u << 20));
  unsigned* cnt = (unsigned*)(ws + (6u << 20));
  float* csp = (float*)(ws + (6u << 20) + 65536);
  float* hst = (float*)(ws + (7u << 20));
  u16* hhf = (u16*)(ws + (8u << 20));
  k1_qkv<<<256, 512, 0, stream>>>(x, wqkv, pq, part, wg, wu, csp, cnt, wo, wd, out);
  kb_rest<<<256, 512, 0, stream>>>(x, pq, part, wo, wp, cnt, hf, hst, csp, wg, wu, wd, hhf, out);
}
```

```cpp
#include <hip/hip_runtime.h>
#include <hip/hip_bf16.h>
#include <stdint.h>

typedef unsigned short u16;
typedef __attribute__((ext_vector_type(8))) __bf16 bf16x8;
typedef __attribute__((ext_vector_type(2))) __bf16 bf16x2;
typedef __attribute__((ext_vector_type(4))) short s16x4;
typedef __attribute__((ext_vector_type(4))) float f32x4;
typedef __attribute__((ext_vector_type(2))) float f32x2;
typedef __attribute__((ext_vector_type(4))) unsigned u32x4;
typedef __attribute__((ext_vector_type(2))) unsigned u32x2;
typedef __attribute__((address_space(3))) s16x4* lds_s16x4_ptr;

#define DI __device__ __forceinline__
#define STAMP(kid,i)

constexpr int S_ = 2048, D_ = 512, F_ = 1376, H_ = 32;
constexpr int KS4 = F_ / 32;

DI float bf_lo(unsigned u) { return __uint_as_float(u << 16); }
DI float bf_hi(unsigned u) { return __uint_as_float(u & 0xffff0000u); }
DI unsigned pack_bf(float lo, float hi) {
  f32x2 f = {lo, hi};
  bf16x2 r = __builtin_convertvector(f, bf16x2);
  return __builtin_bit_cast(unsigned, r);
}
DI float bfr(float f) { return bf_lo(pack_bf(f, 0.f) & 0xffffu); }
DI f32x4 mfma16(u32x4 a, u32x4 b, f32x4 c) {
  return __builtin_amdgcn_mfma_f32_16x16x32_bf16(__builtin_bit_cast(bf16x8, a),
                                                 __builtin_bit_cast(bf16x8, b), c, 0, 0, 0);
}
DI float dot2(unsigned a, unsigned b, float c) {
  return __builtin_amdgcn_fdot2_f32_bf16(__builtin_bit_cast(bf16x2, a), __builtin_bit_cast(bf16x2, b), c, false);
}
DI u32x4 tr_frag(const char* lds0, const char* lds1) {
  s16x4 lo = __builtin_amdgcn_ds_read_tr16_b64_v4i16((lds_s16x4_ptr)lds0);
  s16x4 hi = __builtin_amdgcn_ds_read_tr16_b64_v4i16((lds_s16x4_ptr)lds1);
  u32x2 l2 = __builtin_bit_cast(u32x2, lo), h2 = __builtin_bit_cast(u32x2, hi);
  u32x4 r = {l2[0], l2[1], h2[0], h2[1]};
  return r;
}
DI unsigned lds_addr(char* l) { return (unsigned)(size_t)(__attribute__((address_space(3))) char*)l; }
DI void glds16(const void* sbase, unsigned voff, unsigned dst) {
  asm volatile("s_mov_b32 m0, %2\n\ts_nop 0\n\tglobal_load_lds_dwordx4 %0, %1"
               :: "v"(voff), "s"(sbase), "s"(dst) : "memory");
}
DI float red16(float s) {
  const unsigned u = __float_as_uint(s);
  auto r = __builtin_amdgcn_permlane16_swap(u, u, false, false);
  return __uint_as_float(r[0]) + __uint_as_float(r[1]);
}
DI float red32(float s) {
  const unsigned u = __float_as_uint(s);
  auto r = __builtin_amdgcn_permlane32_swap(u, u, false, false);
  return __uint_as_float(r[0]) + __uint_as_float(r[1]);
}
DI u32x4 merge16(u32x2 t0, u32x2 t1) {
  auto x = __builtin_amdgcn_permlane16_swap(t0[0], t1[0], false, false);
  auto y = __builtin_amdgcn_permlane16_swap(t0[1], t1[1], false, false);
  u32x4 v = {x[0], y[0], x[1], y[1]};
  return v;
}
#define WAIT_VM(n) asm volatile("s_waitcnt vmcnt(" #n ")" ::: "memory")
#define RAW_BAR() do { asm volatile("s_waitcnt lgkmcnt(0)" ::: "memory"); __builtin_amdgcn_s_barrier(); } while (0)
#define BARE_BAR() do { asm volatile("" ::: "memory"); __builtin_amdgcn_s_barrier(); } while (0)
template <int N> DI void wait_vm() { asm volatile("s_waitcnt vmcnt(%0)" ::"i"(N) : "memory"); }

DI float phi_f(float t) { return (t > 0.f ? t + 1.f : __expf(t)) + 1e-6f; }
DI void sq_frag(const u32x4& a, float& q) {
#pragma unroll
  for (int i = 0; i < 4; ++i) q = dot2(a[i], a[i], q);
}
#define ONES_FRAG ((u32x4){0x3f803f80u, 0x3f803f80u, 0x3f803f80u, 0x3f803f80u})

constexpr int K1_SLOT = 16384 + 12288, K1_NSLOT = 4;
__global__ __launch_bounds__(512) void k1_qkv(const u16* __restrict__ x, const u16* __restrict__ wqkv,
                                              u16* __restrict__ pq, float* __restrict__ part,
                                              const u16* __restrict__ wg, const u16* __restrict__ wu,
                                              float* __restrict__ csp, unsigned* __restrict__ cnt,
                                              const u16* __restrict__ wo, const u16* __restrict__ wd,
                                              const u16* outp) {
  __shared__ __attribute__((aligned(16))) char smem[K1_SLOT * K1_NSLOT];
  const int tid = threadIdx.x, lane = tid & 63;
  const int wave = __builtin_amdgcn_readfirstlane(tid >> 6);
  const int xcd = blockIdx.x & 7, idx = blockIdx.x >> 3;
  const int rt = (xcd >> 1) * 4 + (idx >> 3);
  const int cg = (xcd & 1) * 8 + (idx & 7);
  const int r = lane & 15, g = lane >> 4;
  STAMP(0,0);
  if (blockIdx.x == 0 && tid < 40) cnt[tid * 64] = 0u;
  if (wave >= 4) {
    const int lw = wave - 4;
    const unsigned lbase = lds_addr(smem);
    const unsigned aoffs = (unsigned)(lw * 32 + (lane >> 3)) * 1024u + (((lane & 7) ^ ((lane >> 4) & 3)) << 4);
    unsigned boffs[3];
#pragma unroll
    for (int i = 0; i < 3; ++i) {
      const int line = (lw * 3 + i) * 4 + g;
      const int kq = line / 3, cgrp = line % 3;
      const int k = kq * 4 + ((lane >> 2) & 3);
      const int ch = (lane & 3) ^ (((k >> 3) & 1) << 1);
      boffs[i] = (unsigned)(k * 1536 + cgrp * 512 + ch * 8) * 2u;
    }
    const char* abase = (const char*)(x + (size_t)rt * 128 * D_);
    const char* bbase = (const char*)(wqkv + cg * 32);
    auto stage = [&](int s) {
      const unsigned slot = lbase + (s % K1_NSLOT) * K1_SLOT;
#pragma unroll
      for (int i = 0; i < 4; ++i)
        glds16(abase + s * 128 + i * 8192, (i & 1) ? (aoffs ^ 64u) : aoffs, slot + lw * 4096 + i * 1024);
#pragma unroll
      for (int i = 0; i < 3; ++i)
        glds16(bbase + (size_t)s * 64 * 3072, boffs[i], slot + 16384 + (lw * 3 + i) * 1024);
    };
    stage(0); stage(1);
    wait_vm<7>();
    BARE_BAR();
    stage(2);
#pragma unroll
    for (int s = 0; s < 7; ++s) {
      if (s == 0 || s == 5) wait_vm<7>(); else if (s <= 4) wait_vm<14>(); else wait_vm<0>();
      BARE_BAR();
      if (s == 0) stage(3);
      if (s + 4 < 8) stage(s + 4);
    }
    const int W = blockIdx.x * 4 + lw;
    const int grp = W % 86, kp = (W / 86) & 7;
    u32x4 wv[4];
    {
      const u16* wsrc = (grp < 43 ? wg : wu) + (grp % 43) * 32 + (lane & 3) * 8 + (size_t)(kp * 64 + (lane >> 2)) * F_;
#pragma unroll
      for (int i = 0; i < 4; ++i) wv[i] = *(const u32x4*)(wsrc + (size_t)i * 16 * F_);
    }
    unsigned touch;
    {
      const int T = idx * 256 + lw * 64 + lane;
      const char* tp = (const char*)pq + (size_t)T * 4096;
      if (T >= 3584) tp = (const char*)outp + (size_t)(T - 3584) * 4096;
      if (T >= 4096) tp = (const char*)wo + (size_t)(T - 4096) * 4096;
      if (T >= 4224) tp = (const char*)wd + (size_t)(T - 4224) * 4096;
      if (T >= 4568) tp = (const char*)wg + (size_t)(T - 4568) * 4096;
      if (T >= 4912) tp = (const char*)wu + (size_t)(T - 4912) * 4096;
      if (T >= 5256) tp = (const char*)wu;
      touch = *(const volatile unsigned*)tp;
    }
    BARE_BAR();
    if (W < 86 * 8) {
      float c8[8];
#pragma unroll
      for (int j = 0; j < 4; ++j) {
        c8[2 * j] = (bf_lo(wv[0][j]) + bf_lo(wv[1][j])) + (bf_lo(wv[2][j]) + bf_lo(wv[3][j]));
        c8[2 * j + 1] = (bf_hi(wv[0][j]) + bf_hi(wv[1][j])) + (bf_hi(wv[2][j]) + bf_hi(wv[3][j]));
      }
#pragma unroll
      for (int j = 0; j < 8; ++j) {
        float v = c8[j];
        v += __shfl_xor(v, 4); v += __shfl_xor(v, 8);
        c8[j] = red32(red16(v));
      }
      if (lane < 4) {
        float* dst = csp + kp * 2752 + grp * 32 + lane * 8;
        f32x4 v0 = {c8[0], c8[1], c8[2], c8[3]}, v1 = {c8[4], c8[5], c8[6], c8[7]};
        *(f32x4*)dst = v0; *(f32x4*)(dst + 4) = v1;
      }
    }
    asm volatile("" :: "v"(touch));
    BARE_BAR();
    return;
  }
  const int wr = wave >> 1, wc = wave & 1;
  const int q4 = (lane & 15) >> 2, p = lane & 3;
  unsigned touch0;
  {
    const int t = wave * 64 + lane;
    const int lx = idx * 128 + (t & 127);
    const char* px = (const char*)(x + (size_t)(xcd >> 1) * 512 * D_) + (size_t)lx * 128;
    const int lw_ = idx * 192 + (t < 192 ? t : 0);
    const char* pw = (const char*)wqkv + (size_t)(lw_ / 12) * 3072 + ((lw_ % 12) >> 2) * 1024 + (xcd & 1) * 512 + (lw_ & 3) * 128;
    touch0 = *(const volatile unsigned*)pw;
    if (t < 128) touch0 += *(const volatile unsigned*)px;
    if (t >= 240) touch0 += *(const volatile unsigned*)((const char*)wo + (size_t)(blockIdx.x * 16 + t - 240) * 128);
  }
  const int aoff = (wr * 64 + r) * 128 + ((g ^ ((r >> 1) & 7)) << 4);
  const int aoff0 = aoff + wc * 4096, aoff1 = aoff + (wc ^ 1) * 4096;
  const int boff = 16384 + (2 * g) * 768 + q4 * 64 + ((wc ^ (g & 1)) << 5) + p * 8;
  const u32x4 ones = ONES_FRAG;
  f32x4 accq[4] = {}, acck[4] = {}, accv[4] = {};
  f32x4 rs[2] = {}, sqm[2] = {}, cq = {}, ck = {}, cv = {};
  u32x4 fa[2][4], fb[2][3];
  auto rdfrags = [&](int t) {
    const char* slot = smem + ((t >> 1) % K1_NSLOT) * K1_SLOT;
    const int kk = t & 1;
#pragma unroll
    for (int i = 0; i < 4; ++i)
      fa[kk][i] = *(const u32x4*)(slot + (((i < 2 ? aoff0 : aoff1) + (i & 1) * 2048) ^ (kk << 6)));
#pragma unroll
    for (int j = 0; j < 3; ++j)
      fb[kk][j] = tr_frag(slot + boff + kk * 6144 + j * 256, slot + boff + kk * 6144 + 768 + j * 256);
  };
  auto compute = [&](int t) {
    const int kk = t & 1;
    const u32x4* a = fa[kk];
    const u32x4 bq = fb[kk][0], bk = fb[kk][1], bv = fb[kk][2];
#pragma unroll
    for (int i = 0; i < 4; ++i) {
      accq[i] = mfma16(bq, a[i], accq[i]);
      acck[i] = mfma16(a[i], bk, acck[i]);
      accv[i] = mfma16(a[i], bv, accv[i]);
    }
    rs[0] = mfma16(ones, a[0], rs[0]);
    rs[1] = mfma16(ones, a[1], rs[1]);
    sqm[0] = mfma16(a[0], a[0], sqm[0]);
    sqm[1] = mfma16(a[1], a[1], sqm[1]);
    if (kk == wr) {
      cq = mfma16(bq, ones, cq);
      ck = mfma16(ones, bk, ck);
      cv = mfma16(ones, bv, cv);
    }
  };
  BARE_BAR();
  STAMP(0,2);
  rdfrags(0);
#pragma unroll
  for (int s = 0; s < 8; ++s) {
    rdfrags(2 * s + 1);
    compute(2 * s);
    __builtin_amdgcn_sched_barrier(0);
    if (s + 1 < 8) {
      RAW_BAR();
      STAMP(0,3+s);
      rdfrags(2 * s + 2);
    }
    compute(2 * s + 1);
    __builtin_amdgcn_sched_barrier(0);
  }
  STAMP(0,11);
  float* st = (float*)(smem);
  float* csl = (float*)(smem + 2048);
  float* st2 = (float*)(smem + 4096);
  float* red = (float*)(smem + 8192);
  {
#pragma unroll
    for (int i = 0; i < 2; ++i) {
      const float q01 = (r & 1) ? sqm[i][1] : sqm[i][0], q23 = (r & 1) ? sqm[i][3] : sqm[i][2];
      const float q = (r & 2) ? q23 : q01;
      if (g == (r >> 2)) {
        f32x2 v = {rs[i][0], q};
        *(f32x2*)(st + (wr * 64 + wc * 32 + i * 16 + r) * 2) = v;
      }
    }
    float* c = csl + (wr * 2 + wc) * 48;
    if (r == 0) *(f32x4*)(c + 4 * g) = cq;
    if (g == 0) { c[16 + r] = ck[0]; c[32 + r] = cv[0]; }
  }
  __syncthreads();
  STAMP(0,12);
  {
    const f32x2 a0 = *(const f32x2*)(st + (wr * 64 + lane) * 2);
    const float mean = a0[0] * (1.f / 512.f);
    const float var = fmaxf(a0[1] * (1.f / 512.f) - mean * mean, 0.f);
    f32x2 v = {mean, __builtin_amdgcn_rsqf(var + 1e-5f)};
    *(f32x2*)(st2 + (wave * 64 + lane) * 2) = v;
  }
  const f32x4 csq4 = *(const f32x4*)(csl + wc * 48 + 4 * g) + *(const f32x4*)(csl + (2 + wc) * 48 + 4 * g);
  const float csk = csl[wc * 48 + 16 + r] + csl[(2 + wc) * 48 + 16 + r];
  const float csv = csl[wc * 48 + 32 + r] + csl[(2 + wc) * 48 + 32 + r];
  f32x4 kv = {}, zz = {};
#pragma unroll
  for (int ih = 0; ih < 2; ++ih) {
    u32x4 pk, vv;
#pragma unroll
    for (int i2 = 0; i2 < 2; ++i2) {
      const int i = ih * 2 + i2;
      const int mbl = ((ih ^ wc) << 1) + i2;
      {
        const f32x2 mi = *(const f32x2*)(st2 + (wave * 64 + mbl * 16 + r) * 2);
        float qv[4];
#pragma unroll
        for (int j = 0; j < 4; ++j) qv[j] = phi_f((accq[i][j] - mi[0] * csq4[j]) * mi[1]);
        u32x2 o = {pack_bf(qv[0], qv[1]), pack_bf(qv[2], qv[3])};
        const int mblk = rt * 8 + wr * 4 + mbl;
        *(u32x2*)(pq + ((size_t)(mblk * 16 + cg) * 64 + r + 16 * (2 * wc + (g >> 1))) * 8 + (g & 1) * 4) = o;
      }
      {
        const f32x4 m0 = *(const f32x4*)(st2 + (wave * 64 + mbl * 16 + 4 * g) * 2);
        const f32x4 m1 = *(const f32x4*)(st2 + (wave * 64 + mbl * 16 + 4 * g) * 2 + 4);
        const float mean[4] = {m0[0], m0[2], m1[0], m1[2]}, inv[4] = {m0[1], m0[3], m1[1], m1[3]};
        float kk[4], vj[4];
#pragma unroll
        for (int j = 0; j < 4; ++j) {
          kk[j] = phi_f((acck[i][j] - mean[j] * csk) * inv[j]);
          vj[j] = (accv[i][j] - mean[j] * csv) * inv[j];
        }
        pk[2 * i2] = pack_bf(kk[0], kk[1]);
        pk[2 * i2 + 1] = pack_bf(kk[2], kk[3]);
        vv[2 * i2] = pack_bf(vj[0], vj[1]);
        vv[2 * i2 + 1] = pack_bf(vj[2], vj[3]);
      }
    }
    kv = mfma16(pk, vv, kv);
    zz = mfma16(pk, ones, zz);
  }
  {
    float* rd = red + (wc * 2 + wr) * 272;
#pragma unroll
    for (int j = 0; j < 4; ++j) rd[(4 * g + j) * 16 + r] = kv[j];
    if (r == 0) *(f32x4*)(rd + 256 + 4 * g) = zz;
  }
  __syncthreads();
  for (int i = tid; i < 544; i += 256) {
    const int hh = i >= 272 ? 1 : 0, e = i - hh * 272;
    const float* rr = red + hh * 2 * 272 + e;
    part[(size_t)((cg * 2 + hh) * 16 + rt) * 272 + e] = rr[0] + rr[272];
  }
  asm volatile("" :: "v"(touch0));
  STAMP(0,13);
}

typedef __attribute__((address_space(1))) unsigned gu32;
DI void poll_ge(unsigned* c, unsigned target) {
  for (unsigned spins = 0; spins < (1u << 17); ++spins) {
    const unsigned v = __builtin_amdgcn_readfirstlane(
        __hip_atomic_load((gu32*)c, __ATOMIC_RELAXED, __HIP_MEMORY_SCOPE_AGENT));
    if (v >= target) break;
    __builtin_amdgcn_s_sleep(1);
  }
  asm volatile("" ::: "memory");
}
DI void glds16_sc1(const void* sbase, unsigned voff, unsigned dst) {
  asm volatile("s_mov_b32 m0, %2\n\ts_nop 0\n\tglobal_load_lds_dwordx4 %0, %1 sc1"
               :: "v"(voff), "s"(sbase), "s"(dst) : "memory");
}
DI void glds16v(const char* gsrc, unsigned dst) {
  asm volatile("s_mov_b32 m0, %1\n\ts_nop 0\n\tglobal_load_lds_dwordx4 %0, off" :: "v"(gsrc), "s"(dst) : "memory");
}
constexpr int K3_SLOT = 16384 + 24576, K3_NSLOT = 4;

__global__ __launch_bounds__(512) void kb_rest(const u16* __restrict__ x, const u16* __restrict__ pq,
                                               const float* __restrict__ part, const u16* __restrict__ wo,
                                               u16* wp, unsigned* cnt, u16* hf, float* hst,
                                               const float* __restrict__ csp,
                                               const u16* __restrict__ wg, const u16* __restrict__ wu,
                                               const u16* __restrict__ wd, u16* hhf, u16* __restrict__ out) {
  __shared__ __attribute__((aligned(16))) char smem[K3_SLOT * K3_NSLOT];
  const int tid = threadIdx.x, lane = tid & 63;
  const int wave = __builtin_amdgcn_readfirstlane(tid >> 6);
  const int xcd = blockIdx.x & 7, idx = blockIdx.x >> 3;
  const int r = lane & 15, g = lane >> 4;
  const int q4 = (lane & 15) >> 2, p = lane & 3;
  const __amdgpu_buffer_rsrc_t hrs = __builtin_amdgcn_make_buffer_rsrc(hf, 0, S_ * D_ * 2, 0x00020000);
  const __amdgpu_buffer_rsrc_t srs = __builtin_amdgcn_make_buffer_rsrc(hst, 0, S_ * 32 * 4 + 16 * 2752 * 4, 0x00020000);
  unsigned* cnt_w = cnt;
  unsigned* cnt_h = cnt + 8 * 64;
  unsigned* cnt_hh = cnt + 24 * 64;
  STAMP(2,0);
  {
    const int rt = xcd * 4 + (idx >> 3);
    const int ct = idx & 7;
    const int kg = wave >> 2, rb = wave & 3;
    const int mblk = rt * 4 + rb;
    const __amdgpu_buffer_rsrc_t wrs = __builtin_amdgcn_make_buffer_rsrc(wp, 0, 524288 + 8192, 0x00020000);
    const int ph = blockIdx.x >> 3, pc = blockIdx.x & 7;
    float* kv = (float*)(smem + 65536 + 16384);
    float pv[16];
    if (tid < 272) {
#pragma unroll
      for (int pp = 0; pp < 16; ++pp) pv[pp] = part[(size_t)(ph * 16 + pp) * 272 + tid];
    }
    float w[16];
    if (tid < 256) {
#pragma unroll
      for (int e = 0; e < 16; ++e) w[e] = bf_lo(wo[(size_t)(ph * 16 + e) * D_ + pc * 64 + (tid & 63)]);
    }
    if (tid < 272) {
      float s = 0.f;
#pragma unroll
      for (int pp = 0; pp < 16; ++pp) s += pv[pp];
      kv[tid] = s;
    }
    __syncthreads();
    STAMP(2,1);
    if (tid < 256) {
      const int c = tid & 63, dq = tid >> 6;
      float o[4];
#pragma unroll
      for (int j = 0; j < 4; ++j) {
        float s = 0.f;
#pragma unroll
        for (int e4 = 0; e4 < 4; ++e4) {
          const f32x4 k4 = *(const f32x4*)(kv + (dq * 4 + j) * 16 + e4 * 4);
          s = fmaf(k4[0], w[e4 * 4], s); s = fmaf(k4[1], w[e4 * 4 + 1], s);
          s = fmaf(k4[2], w[e4 * 4 + 2], s); s = fmaf(k4[3], w[e4 * 4 + 3], s);
        }
        o[j] = s;
      }
      const int n = pc * 64 + c, nb = n >> 4, cc = n & 15;
      const int ks = ph >> 1, gg = (ph & 1) * 2 + (dq >> 1);
      u32x2 v = {pack_bf(o[0], o[1]), pack_bf(o[2], o[3])};
      __builtin_amdgcn_raw_buffer_store_b64(v, wrs, (unsigned)((nb * 16 + ks) * 64 + gg * 16 + cc) * 16u + (dq & 1) * 8u, 0, 16);
    } else if (tid < 258) {
      const float* zs = kv + 256 + (tid - 256) * 8;
      u32x4 v = {pack_bf(zs[0], zs[1]), pack_bf(zs[2], zs[3]), pack_bf(zs[4], zs[5]), pack_bf(zs[6], zs[7])};
      __builtin_amdgcn_raw_buffer_store_b128(v, wrs, 524288u + (unsigned)(pc * 512 + ph * 16 + (tid - 256) * 8) * 2u, 0, 16);
    }
    WAIT_VM(0);
    __syncthreads();
    if (tid == 0) __hip_atomic_fetch_add((gu32*)(cnt_w + pc * 64), 1u, __ATOMIC_RELAXED, __HIP_MEMORY_SCOPE_AGENT);
    STAMP(2,2);
    float cpart[8];
    const int ccol = ((rt & 1) * 8 + ct) * 172 + (tid - 272);
    const bool do_cs = tid >= 272 && tid < 444;
    if (do_cs) {
#pragma unroll
      for (int kp = 0; kp < 8; ++kp) cpart[kp] = csp[kp * 2752 + ccol];
    }
    u32x4 a[8];
#pragma unroll
    for (int i = 0; i < 8; ++i) a[i] = *(const u32x4*)(pq + ((size_t)(mblk * 16 + kg * 8 + i) * 64 + lane) * 8);
    u32x2 xr[2];
#pragma unroll
    for (int j = 0; j < 2; ++j)
      xr[j] = *(const u32x2*)(x + (size_t)(mblk * 16 + r) * D_ + ct * 64 + (kg * 2 + j) * 16 + g * 4);
    if (do_cs) {
      const float c = ((cpart[0] + cpart[1]) + (cpart[2] + cpart[3])) + ((cpart[4] + cpart[5]) + (cpart[6] + cpart[7]));
      __builtin_amdgcn_raw_buffer_store_b32(__float_as_uint(c), srs, (unsigned)(S_ * 32 * 4 + ((rt >> 1) * 2752 + ccol) * 4), 0, 16);
    }
    if (wave == 0) poll_ge(cnt_w + ct * 64, 32u);
    __syncthreads();
    STAMP(2,3);
    u32x4 zf[8], wv[8];
#pragma unroll
    for (int i = 0; i < 8; ++i)
      zf[i] = __builtin_amdgcn_raw_buffer_load_b128(wrs, 524288u + (unsigned)(ct * 512 + (kg * 8 + i) * 32 + g * 8) * 2u, 0, 16);
#pragma unroll
    for (int i = 0; i < 8; ++i)
      wv[i] = __builtin_amdgcn_raw_buffer_load_b128(wrs, (unsigned)(ct * 65536 + (wave * 8 + i) * 1024 + lane * 16), 0, 16);
    unsigned touch = 0;
    {
      asm volatile("" ::: "memory");
      if (tid < 344) touch += *(const volatile unsigned*)((const char*)wd + (size_t)(idx * 344 + tid) * 128);
      if (tid < 384) {
        const int m = (tid / 12) & 1, rr = idx * 16 + (tid / 24), ln = tid % 12;
        const size_t off = (size_t)rr * (F_ * 2) + (xcd & 1) * 1536 + (size_t)ln * 128;
        const size_t lim = (size_t)rr * (F_ * 2) + F_ * 2 - 4;
        touch += *(const volatile unsigned*)((const char*)(m ? wu : wg) + (off < lim ? off : lim));
      }
    }
#pragma unroll
    for (int i = 0; i < 8; ++i) {
      float s = dot2(a[i][0], zf[i][0], 0.f);
      s = dot2(a[i][1], zf[i][1], s);
      s = dot2(a[i][2], zf[i][2], s);
      s = dot2(a[i][3], zf[i][3], s);
      const float inv = __builtin_amdgcn_rcpf(red16(s) + 1e-6f);
#pragma unroll
      for (int j = 0; j < 4; ++j) a[i][j] = pack_bf(bf_lo(a[i][j]) * inv, bf_hi(a[i][j]) * inv);
    }
#pragma unroll
    for (int i = 0; i < 8; ++i) *(u32x4*)(smem + (wave * 8 + i) * 1024 + lane * 16) = wv[i];
    __syncthreads();
    STAMP(2,4);
    f32x4 acc[4] = {};
#pragma unroll
    for (int i = 0; i < 8; ++i) {
#pragma unroll
      for (int nb = 0; nb < 4; ++nb) {
        const u32x4 b = *(const u32x4*)(smem + (nb * 16 + kg * 8 + i) * 1024 + lane * 16);
        acc[nb] = mfma16(b, a[i], acc[nb]);
      }
    }
    f32x4* xch = (f32x4*)(smem + 65536);
#pragma unroll
    for (int j = 0; j < 2; ++j) xch[(wave * 2 + j) * 64 + lane] = kg ? acc[j] : acc[2 + j];
    __syncthreads();
    float hs = 0.f, hq = 0.f;
    u32x2 oo[2];
#pragma unroll
    for (int j = 0; j < 2; ++j) {
      const f32x4 o4 = (kg ? acc[2 + j] : acc[j]) + xch[((wave ^ 4) * 2 + j) * 64 + lane];
      u32x2 o;
      o[0] = pack_bf(bf_lo(xr[j][0]) + o4[0], bf_hi(xr[j][0]) + o4[1]);
      o[1] = pack_bf(bf_lo(xr[j][1]) + o4[2], bf_hi(xr[j][1]) + o4[3]);
      oo[j] = o;
      hs = dot2(o[0], 0x3f803f80u, hs); hs = dot2(o[1], 0x3f803f80u, hs);
      hq = dot2(o[0], o[0], hq); hq = dot2(o[1], o[1], hq);
    }
    __builtin_amdgcn_raw_buffer_store_b128(
        merge16(oo[0], oo[1]), hrs, (unsigned)(((mblk * 16 + ct * 2 + kg) * 64 + r + 16 * (2 * (g & 1) + (g >> 1))) * 16), 0, 16);
    hs = red32(red16(hs)); hq = red32(red16(hq));
    if (g == 0) {
      u32x2 v = {__float_as_uint(hs), __float_as_uint(hq)};
      __builtin_amdgcn_raw_buffer_store_b64(v, srs, (unsigned)(((ct * 2 + kg) * S_ + mblk * 16 + r) * 8), 0, 16);
    }
    asm volatile("" :: "v"(touch));
    WAIT_VM(0);
    __syncthreads();
    if (tid == 0) __hip_atomic_fetch_add((gu32*)(cnt_h + (rt >> 1) * 64), 1u, __ATOMIC_RELAXED, __HIP_MEMORY_SCOPE_AGENT);
    STAMP(2,5);
  }
  const int rt3 = (xcd >> 1) * 4 + (idx >> 3);
  const int cg = (xcd & 1) * 8 + (idx & 7);
  const int f0 = cg < 11 ? cg * 96 : 1056 + (cg - 11) * 64;
  const int nvalid = cg < 11 ? 6 : 4;
  const int rt4 = xcd * 4 + (idx >> 3);
  const int ct4 = idx & 7;
  if (wave >= 4) {
    const int lw = wave - 4;
    const unsigned lbase = lds_addr(smem);
    {
      const char* abase = (const char*)(hf + (size_t)((rt3 * 8 + lw * 2) * 16) * 512);
      const long wdelta = (const char*)wu - (const char*)wg;
      const char* bsrc[6];
#pragma unroll
      for (int i = 0; i < 6; ++i) {
        const int line = (lw * 6 + i) * 4 + g;
        const int kq = line / 6, cgrp = line % 6;
        const int k = kq * 4 + ((lane >> 2) & 3);
        const int ch = (lane & 3) ^ (((k >> 3) & 1) << 1);
        int c3 = cgrp < 3 ? cgrp : cgrp - 3;
        c3 = (c3 * 2 < nvalid) ? c3 : 0;
        bsrc[i] = (const char*)(wg + f0) + (unsigned)(k * F_ + c3 * 32 + ch * 8) * 2u + (cgrp >= 3 ? wdelta : 0);
      }
      auto stA = [&](int s) {
        const unsigned slot = lbase + (s % K3_NSLOT) * K3_SLOT;
#pragma unroll
        for (int i = 0; i < 4; ++i)
          glds16_sc1(abase + (i >> 1) * 16384 + (s * 2 + (i & 1)) * 1024, lane * 16, slot + (lw * 4 + i) * 1024);
      };
      auto stB = [&](int s) {
        const unsigned slot = lbase + (s % K3_NSLOT) * K3_SLOT;
#pragma unroll
        for (int i = 0; i < 6; ++i) glds16v(bsrc[i] + (size_t)s * 64 * F_ * 2, slot + 16384 + (lw * 6 + i) * 1024);
      };
      stB(0); stB(1); stB(2); stB(3);
      poll_ge(cnt_h + rt3 * 64, 16u);
      stA(0); stA(1); stA(2); stA(3);
      wait_vm<12>(); BARE_BAR();
      wait_vm<8>();  BARE_BAR(); stA(4); stB(4);
      wait_vm<14>(); BARE_BAR(); stA(5); stB(5);
      wait_vm<20>(); BARE_BAR(); stA(6); stB(6);
      wait_vm<20>(); BARE_BAR(); stA(7); stB(7);
      wait_vm<20>(); BARE_BAR();
      wait_vm<10>(); BARE_BAR();
      wait_vm<0>();  BARE_BAR();
    }
    {
      const char* abase = (const char*)(hhf + (size_t)((rt4 * 4 + lw) * KS4) * 512);
      const char* bbase = (const char*)(wd + (size_t)lw * 32 * D_ + ct4 * 64);
      const unsigned boffs = (unsigned)(((g >> 1) * 4 + ((lane >> 2) & 3)) * D_ + (g & 1) * 32 + (lane & 3) * 8) * 2u;
      auto stA = [&](int s) {
        const unsigned slot = lbase + (s & 3) * 32768;
#pragma unroll
        for (int i = 0; i < 4; ++i)
          glds16_sc1(abase + (s * 4 + ((s == 10 && i == 3) ? 2 : i)) * 1024, lane * 16, slot + lw * 4096 + i * 1024);
      };
      auto stB = [&](int s) {
        const unsigned slot = lbase + (s & 3) * 32768;
        const int so = (s == 10 && lw == 3) ? s - 1 : s;
#pragma unroll
        for (int i = 0; i < 4; ++i)
          glds16(bbase + (size_t)(so * 128 + i * 8) * D_ * 2, (i & 1) ? (boffs ^ 32u) : boffs, slot + 16384 + lw * 4096 + i * 1024);
      };
      stB(0); stB(1); stB(2);
      BARE_BAR();
      stB(3);
      poll_ge(cnt_hh + (rt4 >> 1) * 64, 16u);
      stA(0); stA(1); stA(2); stA(3);
      wait_vm<12>(); BARE_BAR();
      wait_vm<8>();  BARE_BAR(); stA(4); stB(4);
      wait_vm<12>(); BARE_BAR(); stA(5); stB(5);
#pragma unroll
      for (int s = 2; s < 10; ++s) {
        if (s <= 7) wait_vm<16>(); else if (s == 8) wait_vm<8>(); else wait_vm<0>();
        BARE_BAR();
        if (s + 4 < 11) { stA(s + 4); stB(s + 4); }
      }
    }
    return;
  }
  const int wr = wave >> 1, wc = wave & 1;
  {
    const int aoff = wr * 8192 + lane * 16;
    const int bbase2 = 16384 + (2 * g) * 1536 + q4 * 64 + p * 8;
    int boff[3];
#pragma unroll
    for (int j = 0; j < 3; ++j) {
      const int fbl = 3 * wc + j;
      boff[j] = bbase2 + (fbl >> 1) * 256 + (((fbl & 1) ^ (g & 1)) << 5);
    }
    u32x2 hp[16];
    f32x4 cg4[3], cu4[3];
    float mean_l = 0.f, inv2_l = 0.f;
    f32x4 accg[4][3] = {}, accu[4][3] = {};
    u32x4 fa[2][4], fb[2][6];
    auto rdfrags = [&](int t) {
      const char* slot = smem + ((t >> 1) % K3_NSLOT) * K3_SLOT;
      const int kk = t & 1;
#pragma unroll
      for (int i = 0; i < 4; ++i) fa[kk][i] = *(const u32x4*)(slot + aoff + i * 2048 + kk * 1024);
#pragma unroll
      for (int j = 0; j < 3; ++j) {
        fb[kk][j] = tr_frag(slot + boff[j] + kk * 12288, slot + boff[j] + kk * 12288 + 1536);
        fb[kk][3 + j] = tr_frag(slot + boff[j] + 768 + kk * 12288, slot + boff[j] + 768 + kk * 12288 + 1536);
      }
    };
    auto compute = [&](int t) {
      const int kk = t & 1;
      const u32x4* a = fa[kk];
#pragma unroll
      for (int j = 0; j < 3; ++j) {
        const u32x4 bg = fb[kk][j], bu = fb[kk][3 + j];
#pragma unroll
        for (int i = 0; i < 4; ++i) {
          accg[i][j] = mfma16(bg, a[i], accg[i][j]);
          accu[i][j] = mfma16(bu, a[i], accu[i][j]);
        }
      }
    };
    BARE_BAR();
    STAMP(3,0);
    rdfrags(0);
#pragma unroll
    for (int s = 0; s < 8; ++s) {
      rdfrags(2 * s + 1);
      compute(2 * s);
      __builtin_amdgcn_sched_barrier(0);
      if (s + 1 < 8) {
        RAW_BAR();
        STAMP(3,1+s);
        rdfrags(2 * s + 2);
      }
      if (s == 4) {
#pragma unroll
        for (int i = 0; i < 16; ++i)
          hp[i] = __builtin_amdgcn_raw_buffer_load_b64(srs, (unsigned)((i * S_ + rt3 * 128 + wr * 64 + lane) * 8), 0, 16);
      }
      if (s == 6) {
        f32x2 t2 = {};
#pragma unroll
        for (int i = 0; i < 16; ++i) t2 += __builtin_bit_cast(f32x2, hp[i]);
        mean_l = t2[0] * (1.f / 512.f);
        const float var = fmaxf(t2[1] * (1.f / 512.f) - mean_l * mean_l, 0.f);
        inv2_l = __builtin_amdgcn_rcpf(var + 1e-5f);
        asm volatile("" : "+v"(mean_l), "+v"(inv2_l));
#pragma unroll
        for (int j = 0; j < 3; ++j) {
          const int fbl = (3 * wc + j) < nvalid ? (3 * wc + j) : 0;
          const unsigned co = (unsigned)(S_ * 32 * 4 + (rt3 * 2752 + f0 + fbl * 16 + 4 * g) * 4);
          cg4[j] = __builtin_bit_cast(f32x4, __builtin_amdgcn_raw_buffer_load_b128(srs, co, 0, 16));
          cu4[j] = __builtin_bit_cast(f32x4, __builtin_amdgcn_raw_buffer_load_b128(srs, co + F_ * 4, 0, 16));
        }
      }
      compute(2 * s + 1);
      __builtin_amdgcn_sched_barrier(0);
    }
    STAMP(3,9);
    const __amdgpu_buffer_rsrc_t hhrs = __builtin_amdgcn_make_buffer_rsrc(hhf, 0, 128 * KS4 * 1024, 0x00020000);
#pragma unroll
    for (int ip = 0; ip < 2; ++ip) {
      float mean[2], inv2[2];
#pragma unroll
      for (int i2 = 0; i2 < 2; ++i2) {
        mean[i2] = __shfl(mean_l, (ip * 2 + i2) * 16 + r);
        inv2[i2] = __shfl(inv2_l, (ip * 2 + i2) * 16 + r);
      }
      const int mblk = rt3 * 8 + wr * 4 + ip * 2 + (g & 1);
#pragma unroll
      for (int j = 0; j < 3; ++j) {
        const int fbl = 3 * wc + j;
        u32x2 o[2];
#pragma unroll
        for (int i2 = 0; i2 < 2; ++i2) {
          const int i = ip * 2 + i2;
          float hv[4];
#pragma unroll
          for (int e = 0; e < 4; ++e)
            hv[e] = ((accg[i][j][e] - mean[i2] * cg4[j][e]) * inv2[i2]) * (accu[i][j][e] - mean[i2] * cu4[j][e]);
          o[i2][0] = pack_bf(hv[0], hv[1]); o[i2][1] = pack_bf(hv[2], hv[3]);
        }
        const u32x4 v = merge16(o[0], o[1]);
        if (fbl < nvalid) {
          const int fb_ = (f0 >> 4) + fbl;
          __builtin_amdgcn_raw_buffer_store_b128(
              v, hhrs, (unsigned)(((mblk * KS4 + (fb_ >> 1)) * 64 + r + 16 * (2 * (fb_ & 1) + (g >> 1))) * 16), 0, 16);
        }
      }
    }
    WAIT_VM(0);
    RAW_BAR();
    if (tid == 0) __hip_atomic_fetch_add((gu32*)(cnt_hh + rt3 * 64), 1u, __ATOMIC_RELAXED, __HIP_MEMORY_SCOPE_AGENT);
    STAMP(3,10);
  }
  {
    const int boff = 16384 + (2 * g) * 512 + wc * 256 + q4 * 64 + p * 8;
    const int aoff = wr * 8192 + lane * 16;
    u32x2 hr[2][2];
    f32x4 acc[2][2] = {};
    u32x4 fa[2][2][2], fb[2][2][2];
    auto rdfrags = [&](int t) {
      const char* slot = smem + ((t >> 1) & 3) * 32768;
#pragma unroll
      for (int kk = 0; kk < 2; ++kk) {
        const int ksl = (t & 1) * 2 + kk;
#pragma unroll
        for (int mb = 0; mb < 2; ++mb) fa[t & 1][kk][mb] = *(const u32x4*)(slot + aoff + mb * 4096 + ksl * 1024);
#pragma unroll
        for (int nb = 0; nb < 2; ++nb) {
          const int ho = (nb ^ (g & 1)) << 5;
          fb[t & 1][kk][nb] = tr_frag(slot + boff + ksl * 4096 + ho, slot + boff + ksl * 4096 + 512 + ho);
        }
      }
    };
    auto compute = [&](int t) {
#pragma unroll
      for (int kk = 0; kk < 2; ++kk) {
        if (2 * t + kk < KS4) {
#pragma unroll
          for (int mb = 0; mb < 2; ++mb)
#pragma unroll
            for (int nb = 0; nb < 2; ++nb) acc[mb][nb] = mfma16(fb[t & 1][kk][nb], fa[t & 1][kk][mb], acc[mb][nb]);
        }
      }
    };
    BARE_BAR();
    STAMP(4,0);
    rdfrags(0);
#pragma unroll
    for (int mb = 0; mb < 2; ++mb)
#pragma unroll
      for (int nb = 0; nb < 2; ++nb)
        hr[mb][nb] = __builtin_amdgcn_raw_buffer_load_b64(
            hrs, (unsigned)((((rt4 * 4 + wr * 2 + mb) * 16 + ct4 * 2 + wc) * 64 + r + 16 * (2 * nb + (g >> 1))) * 16 + (g & 1) * 8), 0, 16);
#pragma unroll
    for (int s = 0; s < 11; ++s) {
      rdfrags(2 * s + 1);
      compute(2 * s);
      if (s + 1 < 11) {
        RAW_BAR();
        STAMP(4,1+s);
        rdfrags(2 * s + 2);
      }
      compute(2 * s + 1);
    }
#pragma unroll
    for (int mb = 0; mb < 2; ++mb)
#pragma unroll
      for (int nb = 0; nb < 2; ++nb) {
        const f32x4 o4 = acc[mb][nb];
        u32x2 o;
        o[0] = pack_bf(bf_lo(hr[mb][nb][0]) + o4[0], bf_hi(hr[mb][nb][0]) + o4[1]);
        o[1] = pack_bf(bf_lo(hr[mb][nb][1]) + o4[2], bf_hi(hr[mb][nb][1]) + o4[3]);
        *(u32x2*)(out + (size_t)(rt4 * 64 + wr * 32 + mb * 16 + r) * D_ + ct4 * 64 + wc * 32 + nb * 16 + g * 4) = o;
      }
    STAMP(4,14);
  }
}

extern "C" void kernel_launch(void* const* d_in, const int* in_sizes, int n_in, void* d_out, int out_size,
                              void* d_ws, size_t ws_size, hipStream_t stream) {
  const u16* x = (const u16*)d_in[0];
  const u16* wqkv = (const u16*)d_in[1];
  const u16* wo = (const u16*)d_in[2];
  const u16* wg = (const u16*)d_in[4];
  const u16* wu = (const u16*)d_in[5];
  const u16* wd = (const u16*)d_in[6];
  u16* out = (u16*)d_out;
  char* ws = (char*)d_ws;
  u16* pq = (u16*)(ws);
  u16* hf = (u16*)(ws + (2u << 20));
  u16* wp = (u16*)(ws + (4u << 20));
  float* part = (float*)(ws + (5u << 20));
  unsigned* cnt = (unsigned*)(ws + (6u << 20));
  float* csp = (float*)(ws + (6u << 20) + 65536);
  float* hst = (float*)(ws + (7u << 20));
  u16* hhf = (u16*)(ws + (8u << 20));
  k1_qkv<<<256, 512, 0, stream>>>(x, wqkv, pq, part, wg, wu, csp, cnt, wo, wd, out);
  kb_rest<<<256, 512, 0, stream>>>(x, pq, part, wo, wp, cnt, hf, hst, csp, wg, wu, wd, hhf, out);
}
```

```cpp
#include <hip/hip_runtime.h>
#include <hip/hip_bf16.h>
#include <stdint.h>

typedef unsigned short u16;
typedef __attribute__((ext_vector_type(8))) __bf16 bf16x8;
typedef __attribute__((ext_vector_type(2))) __bf16 bf16x2;
typedef __attribute__((ext_vector_type(4))) short s16x4;
typedef __attribute__((ext_vector_type(4))) float f32x4;
typedef __attribute__((ext_vector_type(2))) float f32x2;
typedef __attribute__((ext_vector_type(4))) unsigned u32x4;
typedef __attribute__((ext_vector_type(2))) unsigned u32x2;
typedef __attribute__((address_space(3))) s16x4* lds_s16x4_ptr;

#define DI __device__ __forceinline__
#define STAMP(kid,i)

constexpr int S_ = 2048, D_ = 512, F_ = 1376, H_ = 32;
constexpr int KS4 = F_ / 32;

DI float bf_lo(unsigned u) { return __uint_as_float(u << 16); }
DI float bf_hi(unsigned u) { return __uint_as_float(u & 0xffff0000u); }
DI unsigned pack_bf(float lo, float hi) {
  f32x2 f = {lo, hi};
  bf16x2 r = __builtin_convertvector(f, bf16x2);
  return __builtin_bit_cast(unsigned, r);
}
DI float bfr(float f) { return bf_lo(pack_bf(f, 0.f) & 0xffffu); }
DI f32x4 mfma16(u32x4 a, u32x4 b, f32x4 c) {
  return __builtin_amdgcn_mfma_f32_16x16x32_bf16(__builtin_bit_cast(bf16x8, a),
                                                 __builtin_bit_cast(bf16x8, b), c, 0, 0, 0);
}
DI float dot2(unsigned a, unsigned b, float c) {
  return __builtin_amdgcn_fdot2_f32_bf16(__builtin_bit_cast(bf16x2, a), __builtin_bit_cast(bf16x2, b), c, false);
}
DI u32x4 tr_frag(const char* lds0, const char* lds1) {
  s16x4 lo = __builtin_amdgcn_ds_read_tr16_b64_v4i16((lds_s16x4_ptr)lds0);
  s16x4 hi = __builtin_amdgcn_ds_read_tr16_b64_v4i16((lds_s16x4_ptr)lds1);
  u32x2 l2 = __builtin_bit_cast(u32x2, lo), h2 = __builtin_bit_cast(u32x2, hi);
  u32x4 r = {l2[0], l2[1], h2[0], h2[1]};
  return r;
}
DI unsigned lds_addr(char* l) { return (unsigned)(size_t)(__attribute__((address_space(3))) char*)l; }
DI void glds16(const void* sbase, unsigned voff, unsigned dst) {
  asm volatile("s_mov_b32 m0, %2\n\ts_nop 0\n\tglobal_load_lds_dwordx4 %0, %1"
               :: "v"(voff), "s"(sbase), "s"(dst) : "memory");
}
DI float red16(float s) {
  const unsigned u = __float_as_uint(s);
  auto r = __builtin_amdgcn_permlane16_swap(u, u, false, false);
  return __uint_as_float(r[0]) + __uint_as_float(r[1]);
}
DI float red32(float s) {
  const unsigned u = __float_as_uint(s);
  auto r = __builtin_amdgcn_permlane32_swap(u, u, false, false);
  return __uint_as_float(r[0]) + __uint_as_float(r[1]);
}
DI u32x4 merge16(u32x2 t0, u32x2 t1) {
  auto x = __builtin_amdgcn_permlane16_swap(t0[0], t1[0], false, false);
  auto y = __builtin_amdgcn_permlane16_swap(t0[1], t1[1], false, false);
  u32x4 v = {x[0], y[0], x[1], y[1]};
  return v;
}
#define WAIT_VM(n) asm volatile("s_waitcnt vmcnt(" #n ")" ::: "memory")
#define RAW_BAR() do { asm volatile("s_waitcnt lgkmcnt(0)" ::: "memory"); __builtin_amdgcn_s_barrier(); } while (0)
#define BARE_BAR() do { asm volatile("" ::: "memory"); __builtin_amdgcn_s_barrier(); } while (0)
template <int N> DI void wait_vm() { asm volatile("s_waitcnt vmcnt(%0)" ::"i"(N) : "memory"); }

DI float phi_f(float t) { return (t > 0.f ? t + 1.f : __expf(t)) + 1e-6f; }
DI void sq_frag(const u32x4& a, float& q) {
#pragma unroll
  for (int i = 0; i < 4; ++i) q = dot2(a[i], a[i], q);
}
#define ONES_FRAG ((u32x4){0x3f803f80u, 0x3f803f80u, 0x3f803f80u, 0x3f803f80u})

constexpr int K1_SLOT = 16384 + 12288, K1_NSLOT = 4;
__global__ __launch_bounds__(512) void k1_qkv(const u16* __restrict__ x, const u16* __restrict__ wqkv,
                                              u16* __restrict__ pq, float* __restrict__ part,
                                              const u16* __restrict__ wg, const u16* __restrict__ wu,
                                              float* __restrict__ csp, unsigned* __restrict__ cnt,
                                              const u16* __restrict__ wo, const u16* __restrict__ wd,
                                              const u16* outp) {
  __shared__ __attribute__((aligned(16))) char smem[K1_SLOT * K1_NSLOT];
  const int tid = threadIdx.x, lane = tid & 63;
  const int wave = __builtin_amdgcn_readfirstlane(tid >> 6);
  const int xcd = blockIdx.x & 7, idx = blockIdx.x >> 3;
  const int rt = (xcd >> 1) * 4 + (idx >> 3);
  const int cg = (xcd & 1) * 8 + (idx & 7);
  const int r = lane & 15, g = lane >> 4;
  STAMP(0,0);
  if (blockIdx.x == 0 && tid < 40) cnt[tid * 64] = 0u;
  if (wave >= 4) {
    const int lw = wave - 4;
    const unsigned lbase = lds_addr(smem);
    const unsigned aoffs = (unsigned)(lw * 32 + (lane >> 3)) * 1024u + (((lane & 7) ^ ((lane >> 4) & 3)) << 4);
    unsigned boffs[3];
#pragma unroll
    for (int i = 0; i < 3; ++i) {
      const int line = (lw * 3 + i) * 4 + g;
      const int kq = line / 3, cgrp = line % 3;
      const int k = kq * 4 + ((lane >> 2) & 3);
      const int ch = (lane & 3) ^ (((k >> 3) & 1) << 1);
      boffs[i] = (unsigned)(k * 1536 + cgrp * 512 + ch * 8) * 2u;
    }
    const char* abase = (const char*)(x + (size_t)rt * 128 * D_);
    const char* bbase = (const char*)(wqkv + cg * 32);
    auto stage = [&](int s) {
      const unsigned slot = lbase + (s % K1_NSLOT) * K1_SLOT;
#pragma unroll
      for (int i = 0; i < 4; ++i)
        glds16(abase + s * 128 + i * 8192, (i & 1) ? (aoffs ^ 64u) : aoffs, slot + lw * 4096 + i * 1024);
#pragma unroll
      for (int i = 0; i < 3; ++i)
        glds16(bbase + (size_t)s * 64 * 3072, boffs[i], slot + 16384 + (lw * 3 + i) * 1024);
    };
    stage(0); stage(1);
    wait_vm<7>();
    BARE_BAR();
    stage(2);
#pragma unroll
    for (int s = 0; s < 7; ++s) {
      if (s == 0 || s == 5) wait_vm<7>(); else if (s <= 4) wait_vm<14>(); else wait_vm<0>();
      BARE_BAR();
      if (s == 0) stage(3);
      if (s + 4 < 8) stage(s + 4);
    }
    const int W = blockIdx.x * 4 + lw;
    const int grp = W % 86, kp = (W / 86) & 7;
    u32x4 wv[4];
    {
      const u16* wsrc = (grp < 43 ? wg : wu) + (grp % 43) * 32 + (lane & 3) * 8 + (size_t)(kp * 64 + (lane >> 2)) * F_;
#pragma unroll
      for (int i = 0; i < 4; ++i) wv[i] = *(const u32x4*)(wsrc + (size_t)i * 16 * F_);
    }
    unsigned touch;
    {
      const int T = idx * 256 + lw * 64 + lane;
      const char* tp = (const char*)pq + (size_t)T * 4096;
      if (T >= 3584) tp = (const char*)outp + (size_t)(T - 3584) * 4096;
      if (T >= 4096) tp = (const char*)wo + (size_t)(T - 4096) * 4096;
      if (T >= 4224) tp = (const char*)wd + (size_t)(T - 4224) * 4096;
      if (T >= 4568) tp = (const char*)wg + (size_t)(T - 4568) * 4096;
      if (T >= 4912) tp = (const char*)wu + (size_t)(T - 4912) * 4096;
      if (T >= 5256) tp = (const char*)wu;
      touch = *(const volatile unsigned*)tp;
    }
    BARE_BAR();
    if (W < 86 * 8) {
      float c8[8];
#pragma unroll
      for (int j = 0; j < 4; ++j) {
        c8[2 * j] = (bf_lo(wv[0][j]) + bf_lo(wv[1][j])) + (bf_lo(wv[2][j]) + bf_lo(wv[3][j]));
        c8[2 * j + 1] = (bf_hi(wv[0][j]) + bf_hi(wv[1][j])) + (bf_hi(wv[2][j]) + bf_hi(wv[3][j]));
      }
#pragma unroll
      for (int j = 0; j < 8; ++j) {
        float v = c8[j];
        v += __shfl_xor(v, 4); v += __shfl_xor(v, 8);
        c8[j] = red32(red16(v));
      }
      if (lane < 4) {
        float* dst = csp + kp * 2752 + grp * 32 + lane * 8;
        f32x4 v0 = {c8[0], c8[1], c8[2], c8[3]}, v1 = {c8[4], c8[5], c8[6], c8[7]};
        *(f32x4*)dst = v0; *(f32x4*)(dst + 4) = v1;
      }
    }
    asm volatile("" :: "v"(touch));
    BARE_BAR();
    return;
  }
  const int wr = wave >> 1, wc = wave & 1;
  const int q4 = (lane & 15) >> 2, p = lane & 3;
  unsigned touch0;
  {
    const int t = wave * 64 + lane;
    const int lx = idx * 128 + (t & 127);
    const char* px = (const char*)(x + (size_t)(xcd >> 1) * 512 * D_) + (size_t)lx * 128;
    const int lw_ = idx * 192 + (t < 192 ? t : 0);
    const char* pw = (const char*)wqkv + (size_t)(lw_ / 12) * 3072 + ((lw_ % 12) >> 2) * 1024 + (xcd & 1) * 512 + (lw_ & 3) * 128;
    touch0 = *(const volatile unsigned*)pw;
    if (t < 128) touch0 += *(const volatile unsigned*)px;
    if (t >= 240) touch0 += *(const volatile unsigned*)((const char*)wo + (size_t)(blockIdx.x * 16 + t - 240) * 128);
    if (t >= 128 && t < 128 + 43) {
      const size_t o = (size_t)(blockIdx.x * 43 + t - 128) * 128;
      touch0 += *(const volatile unsigned*)((const char*)wg + o);
      touch0 += *(const volatile unsigned*)((const char*)wu + o);
      touch0 += *(const volatile unsigned*)((const char*)wd + o);
    }
  }
  const int aoff = (wr * 64 + r) * 128 + ((g ^ ((r >> 1) & 7)) << 4);
  const int aoff0 = aoff + wc * 4096, aoff1 = aoff + (wc ^ 1) * 4096;
  const int boff = 16384 + (2 * g) * 768 + q4 * 64 + ((wc ^ (g & 1)) << 5) + p * 8;
  const u32x4 ones = ONES_FRAG;
  f32x4 accq[4] = {}, acck[4] = {}, accv[4] = {};
  f32x4 rs[2] = {}, sqm[2] = {}, cq = {}, ck = {}, cv = {};
  u32x4 fa[2][4], fb[2][3];
  auto rdfrags = [&](int t) {
    const char* slot = smem + ((t >> 1) % K1_NSLOT) * K1_SLOT;
    const int kk = t & 1;
#pragma unroll
    for (int i = 0; i < 4; ++i)
      fa[kk][i] = *(const u32x4*)(slot + (((i < 2 ? aoff0 : aoff1) + (i & 1) * 2048) ^ (kk << 6)));
#pragma unroll
    for (int j = 0; j < 3; ++j)
      fb[kk][j] = tr_frag(slot + boff + kk * 6144 + j * 256, slot + boff + kk * 6144 + 768 + j * 256);
  };
  auto compute = [&](int t) {
    const int kk = t & 1;
    const u32x4* a = fa[kk];
    const u32x4 bq = fb[kk][0], bk = fb[kk][1], bv = fb[kk][2];
#pragma unroll
    for (int i = 0; i < 4; ++i) {
      accq[i] = mfma16(bq, a[i], accq[i]);
      acck[i] = mfma16(a[i], bk, acck[i]);
      accv[i] = mfma16(a[i], bv, accv[i]);
    }
    rs[0] = mfma16(ones, a[0], rs[0]);
    rs[1] = mfma16(ones, a[1], rs[1]);
    sqm[0] = mfma16(a[0], a[0], sqm[0]);
    sqm[1] = mfma16(a[1], a[1], sqm[1]);
    if (kk == wr) {
      cq = mfma16(bq, ones, cq);
      ck = mfma16(ones, bk, ck);
      cv = mfma16(ones, bv, cv);
    }
  };
  BARE_BAR();
  STAMP(0,2);
  rdfrags(0);
#pragma unroll
  for (int s = 0; s < 8; ++s) {
    rdfrags(2 * s + 1);
    compute(2 * s);
    __builtin_amdgcn_sched_barrier(0);
    if (s + 1 < 8) {
      RAW_BAR();
      STAMP(0,3+s);
      rdfrags(2 * s + 2);
    }
    compute(2 * s + 1);
    __builtin_amdgcn_sched_barrier(0);
  }
  STAMP(0,11);
  float* st = (float*)(smem);
  float* csl = (float*)(smem + 2048);
  float* st2 = (float*)(smem + 4096);
  float* red = (float*)(smem + 8192);
  {
#pragma unroll
    for (int i = 0; i < 2; ++i) {
      const float q01 = (r & 1) ? sqm[i][1] : sqm[i][0], q23 = (r & 1) ? sqm[i][3] : sqm[i][2];
      const float q = (r & 2) ? q23 : q01;
      if (g == (r >> 2)) {
        f32x2 v = {rs[i][0], q};
        *(f32x2*)(st + (wr * 64 + wc * 32 + i * 16 + r) * 2) = v;
      }
    }
    float* c = csl + (wr * 2 + wc) * 48;
    if (r == 0) *(f32x4*)(c + 4 * g) = cq;
    if (g == 0) { c[16 + r] = ck[0]; c[32 + r] = cv[0]; }
  }
  __syncthreads();
  STAMP(0,12);
  {
    const f32x2 a0 = *(const f32x2*)(st + (wr * 64 + lane) * 2);
    const float mean = a0[0] * (1.f / 512.f);
    const float var = fmaxf(a0[1] * (1.f / 512.f) - mean * mean, 0.f);
    f32x2 v = {mean, __builtin_amdgcn_rsqf(var + 1e-5f)};
    *(f32x2*)(st2 + (wave * 64 + lane) * 2) = v;
  }
  const f32x4 csq4 = *(const f32x4*)(csl + wc * 48 + 4 * g) + *(const f32x4*)(csl + (2 + wc) * 48 + 4 * g);
  const float csk = csl[wc * 48 + 16 + r] + csl[(2 + wc) * 48 + 16 + r];
  const float csv = csl[wc * 48 + 32 + r] + csl[(2 + wc) * 48 + 32 + r];
  f32x4 kv = {}, zz = {};
#pragma unroll
  for (int ih = 0; ih < 2; ++ih) {
    u32x4 pk, vv;
#pragma unroll
    for (int i2 = 0; i2 < 2; ++i2) {
      const int i = ih * 2 + i2;
      const int mbl = ((ih ^ wc) << 1) + i2;
      {
        const f32x2 mi = *(const f32x2*)(st2 + (wave * 64 + mbl * 16 + r) * 2);
        float qv[4];
#pragma unroll
        for (int j = 0; j < 4; ++j) qv[j] = phi_f((accq[i][j] - mi[0] * csq4[j]) * mi[1]);
        u32x2 o = {pack_bf(qv[0], qv[1]), pack_bf(qv[2], qv[3])};
        const int mblk = rt * 8 + wr * 4 + mbl;
        *(u32x2*)(pq + ((size_t)(mblk * 16 + cg) * 64 + r + 16 * (2 * wc + (g >> 1))) * 8 + (g & 1) * 4) = o;
      }
      {
        const f32x4 m0 = *(const f32x4*)(st2 + (wave * 64 + mbl * 16 + 4 * g) * 2);
        const f32x4 m1 = *(const f32x4*)(st2 + (wave * 64 + mbl * 16 + 4 * g) * 2 + 4);
        const float mean[4] = {m0[0], m0[2], m1[0], m1[2]}, inv[4] = {m0[1], m0[3], m1[1], m1[3]};
        float kk[4], vj[4];
#pragma unroll
        for (int j = 0; j < 4; ++j) {
          kk[j] = phi_f((acck[i][j] - mean[j] * csk) * inv[j]);
          vj[j] = (accv[i][j] - mean[j] * csv) * inv[j];
        }
        pk[2 * i2] = pack_bf(kk[0], kk[1]);
        pk[2 * i2 + 1] = pack_bf(kk[2], kk[3]);
        vv[2 * i2] = pack_bf(vj[0], vj[1]);
        vv[2 * i2 + 1] = pack_bf(vj[2], vj[3]);
      }
    }
    kv = mfma16(pk, vv, kv);
    zz = mfma16(pk, ones, zz);
  }
  {
    float* rd = red + (wc * 2 + wr) * 272;
#pragma unroll
    for (int j = 0; j < 4; ++j) rd[(4 * g + j) * 16 + r] = kv[j];
    if (r == 0) *(f32x4*)(rd + 256 + 4 * g) = zz;
  }
  __syncthreads();
  for (int i = tid; i < 544; i += 256) {
    const int hh = i >= 272 ? 1 : 0, e = i - hh * 272;
    const float* rr = red + hh * 2 * 272 + e;
    part[(size_t)((cg * 2 + hh) * 16 + rt) * 272 + e] = rr[0] + rr[272];
  }
  asm volatile("" :: "v"(touch0));
  STAMP(0,13);
}

typedef __attribute__((address_space(1))) unsigned gu32;
DI void poll_ge(unsigned* c, unsigned target) {
  for (unsigned spins = 0; spins < (1u << 17); ++spins) {
    const unsigned v = __builtin_amdgcn_readfirstlane(
        __hip_atomic_load((gu32*)c, __ATOMIC_RELAXED, __HIP_MEMORY_SCOPE_AGENT));
    if (v >= target) break;
    __builtin_amdgcn_s_sleep(1);
  }
  asm volatile("" ::: "memory");
}
DI void glds16_sc1(const void* sbase, unsigned voff, unsigned dst) {
  asm volatile("s_mov_b32 m0, %2\n\ts_nop 0\n\tglobal_load_lds_dwordx4 %0, %1 sc1"
               :: "v"(voff), "s"(sbase), "s"(dst) : "memory");
}
DI void glds16v(const char* gsrc, unsigned dst) {
  asm volatile("s_mov_b32 m0, %1\n\ts_nop 0\n\tglobal_load_lds_dwordx4 %0, off" :: "v"(gsrc), "s"(dst) : "memory");
}
constexpr int K3_SLOT = 16384 + 24576, K3_NSLOT = 4;

__global__ __launch_bounds__(512) void kb_rest(const u16* __restrict__ x, const u16* __restrict__ pq,
                                               const float* __restrict__ part, const u16* __restrict__ wo,
                                               u16* wp, unsigned* cnt, u16* hf, float* hst,
                                               const float* __restrict__ csp,
                                               const u16* __restrict__ wg, const u16* __restrict__ wu,
                                               const u16* __restrict__ wd, u16* hhf, u16* __restrict__ out) {
  __shared__ __attribute__((aligned(16))) char smem[K3_SLOT * K3_NSLOT];
  const int tid = threadIdx.x, lane = tid & 63;
  const int wave = __builtin_amdgcn_readfirstlane(tid >> 6);
  const int xcd = blockIdx.x & 7, idx = blockIdx.x >> 3;
  const int r = lane & 15, g = lane >> 4;
  const int q4 = (lane & 15) >> 2, p = lane & 3;
  const __amdgpu_buffer_rsrc_t hrs = __builtin_amdgcn_make_buffer_rsrc(hf, 0, S_ * D_ * 2, 0x00020000);
  const __amdgpu_buffer_rsrc_t srs = __builtin_amdgcn_make_buffer_rsrc(hst, 0, S_ * 32 * 4 + 16 * 2752 * 4, 0x00020000);
  unsigned* cnt_w = cnt;
  unsigned* cnt_h = cnt + 8 * 64;
  unsigned* cnt_hh = cnt + 24 * 64;
  STAMP(2,0);
  {
    const int rt = xcd * 4 + (idx >> 3);
    const int ct = idx & 7;
    const int kg = wave >> 2, rb = wave & 3;
    const int mblk = rt * 4 + rb;
    const __amdgpu_buffer_rsrc_t wrs = __builtin_amdgcn_make_buffer_rsrc(wp, 0, 524288 + 8192, 0x00020000);
    const int ph = blockIdx.x >> 3, pc = blockIdx.x & 7;
    float* kv = (float*)(smem + 65536 + 16384);
    float pv[16];
    if (tid < 272) {
#pragma unroll
      for (int pp = 0; pp < 16; ++pp) pv[pp] = part[(size_t)(ph * 16 + pp) * 272 + tid];
    }
    float w[16];
    if (tid < 256) {
#pragma unroll
      for (int e = 0; e < 16; ++e) w[e] = bf_lo(wo[(size_t)(ph * 16 + e) * D_ + pc * 64 + (tid & 63)]);
    }
    if (tid < 272) {
      float s = 0.f;
#pragma unroll
      for (int pp = 0; pp < 16; ++pp) s += pv[pp];
      kv[tid] = s;
    }
    __syncthreads();
    STAMP(2,1);
    if (tid < 256) {
      const int c = tid & 63, dq = tid >> 6;
      float o[4];
#pragma unroll
      for (int j = 0; j < 4; ++j) {
        float s = 0.f;
#pragma unroll
        for (int e4 = 0; e4 < 4; ++e4) {
          const f32x4 k4 = *(const f32x4*)(kv + (dq * 4 + j) * 16 + e4 * 4);
          s = fmaf(k4[0], w[e4 * 4], s); s = fmaf(k4[1], w[e4 * 4 + 1], s);
          s = fmaf(k4[2], w[e4 * 4 + 2], s); s = fmaf(k4[3], w[e4 * 4 + 3], s);
        }
        o[j] = s;
      }
      const int n = pc * 64 + c, nb = n >> 4, cc = n & 15;
      const int ks = ph >> 1, gg = (ph & 1) * 2 + (dq >> 1);
      u32x2 v = {pack_bf(o[0], o[1]), pack_bf(o[2], o[3])};
      __builtin_amdgcn_raw_buffer_store_b64(v, wrs, (unsigned)((nb * 16 + ks) * 64 + gg * 16 + cc) * 16u + (dq & 1) * 8u, 0, 16);
    } else if (tid < 258) {
      const float* zs = kv + 256 + (tid - 256) * 8;
      u32x4 v = {pack_bf(zs[0], zs[1]), pack_bf(zs[2], zs[3]), pack_bf(zs[4], zs[5]), pack_bf(zs[6], zs[7])};
      __builtin_amdgcn_raw_buffer_store_b128(v, wrs, 524288u + (unsigned)(pc * 512 + ph * 16 + (tid - 256) * 8) * 2u, 0, 16);
    }
    WAIT_VM(0);
    __syncthreads();
    if (tid == 0) __hip_atomic_fetch_add((gu32*)(cnt_w + pc * 64), 1u, __ATOMIC_RELAXED, __HIP_MEMORY_SCOPE_AGENT);
    STAMP(2,2);
    float cpart[8];
    const int ccol = ((rt & 1) * 8 + ct) * 172 + (tid - 272);
    const bool do_cs = tid >= 272 && tid < 444;
    if (do_cs) {
#pragma unroll
      for (int kp = 0; kp < 8; ++kp) cpart[kp] = csp[kp * 2752 + ccol];
    }
    u32x4 a[8];
#pragma unroll
    for (int i = 0; i < 8; ++i) a[i] = *(const u32x4*)(pq + ((size_t)(mblk * 16 + kg * 8 + i) * 64 + lane) * 8);
    u32x2 xr[2];
#pragma unroll
    for (int j = 0; j < 2; ++j)
      xr[j] = *(const u32x2*)(x + (size_t)(mblk * 16 + r) * D_ + ct * 64 + (kg * 2 + j) * 16 + g * 4);
    if (do_cs) {
      const float c = ((cpart[0] + cpart[1]) + (cpart[2] + cpart[3])) + ((cpart[4] + cpart[5]) + (cpart[6] + cpart[7]));
      __builtin_amdgcn_raw_buffer_store_b32(__float_as_uint(c), srs, (unsigned)(S_ * 32 * 4 + ((rt >> 1) * 2752 + ccol) * 4), 0, 16);
    }
    if (wave == 0) poll_ge(cnt_w + ct * 64, 32u);
    __syncthreads();
    STAMP(2,3);
    u32x4 zf[8], wv[8];
#pragma unroll
    for (int i = 0; i < 8; ++i)
      zf[i] = __builtin_amdgcn_raw_buffer_load_b128(wrs, 524288u + (unsigned)(ct * 512 + (kg * 8 + i) * 32 + g * 8) * 2u, 0, 16);
#pragma unroll
    for (int i = 0; i < 8; ++i)
      wv[i] = __builtin_amdgcn_raw_buffer_load_b128(wrs, (unsigned)(ct * 65536 + (wave * 8 + i) * 1024 + lane * 16), 0, 16);
    unsigned touch = 0;
    {
      asm volatile("" ::: "memory");
      if (tid < 344) touch += *(const volatile unsigned*)((const char*)wd + (size_t)(idx * 344 + tid) * 128);
      if (tid < 384) {
        const int m = (tid / 12) & 1, rr = idx * 16 + (tid / 24), ln = tid % 12;
        const size_t off = (size_t)rr * (F_ * 2) + (xcd & 1) * 1536 + (size_t)ln * 128;
        const size_t lim = (size_t)rr * (F_ * 2) + F_ * 2 - 4;
        touch += *(const volatile unsigned*)((const char*)(m ? wu : wg) + (off < lim ? off : lim));
      }
    }
#pragma unroll
    for (int i = 0; i < 8; ++i) {
      float s = dot2(a[i][0], zf[i][0], 0.f);
      s = dot2(a[i][1], zf[i][1], s);
      s = dot2(a[i][2], zf[i][2], s);
      s = dot2(a[i][3], zf[i][3], s);
      const float inv = __builtin_amdgcn_rcpf(red16(s) + 1e-6f);
#pragma unroll
      for (int j = 0; j < 4; ++j) a[i][j] = pack_bf(bf_lo(a[i][j]) * inv, bf_hi(a[i][j]) * inv);
    }
#pragma unroll
    for (int i = 0; i < 8; ++i) *(u32x4*)(smem + (wave * 8 + i) * 1024 + lane * 16) = wv[i];
    __syncthreads();
    STAMP(2,4);
    f32x4 acc[4] = {};
#pragma unroll
    for (int i = 0; i < 8; ++i) {
#pragma unroll
      for (int nb = 0; nb < 4; ++nb) {
        const u32x4 b = *(const u32x4*)(smem + (nb * 16 + kg * 8 + i) * 1024 + lane * 16);
        acc[nb] = mfma16(b, a[i], acc[nb]);
      }
    }
    f32x4* xch = (f32x4*)(smem + 65536);
#pragma unroll
    for (int j = 0; j < 2; ++j) xch[(wave * 2 + j) * 64 + lane] = kg ? acc[j] : acc[2 + j];
    __syncthreads();
    float hs = 0.f, hq = 0.f;
    u32x2 oo[2];
#pragma unroll
    for (int j = 0; j < 2; ++j) {
      const f32x4 o4 = (kg ? acc[2 + j] : acc[j]) + xch[((wave ^ 4) * 2 + j) * 64 + lane];
      u32x2 o;
      o[0] = pack_bf(bf_lo(xr[j][0]) + o4[0], bf_hi(xr[j][0]) + o4[1]);
      o[1] = pack_bf(bf_lo(xr[j][1]) + o4[2], bf_hi(xr[j][1]) + o4[3]);
      oo[j] = o;
      hs = dot2(o[0], 0x3f803f80u, hs); hs = dot2(o[1], 0x3f803f80u, hs);
      hq = dot2(o[0], o[0], hq); hq = dot2(o[1], o[1], hq);
    }
    __builtin_amdgcn_raw_buffer_store_b128(
        merge16(oo[0], oo[1]), hrs, (unsigned)(((mblk * 16 + ct * 2 + kg) * 64 + r + 16 * (2 * (g & 1) + (g >> 1))) * 16), 0, 16);
    hs = red32(red16(hs)); hq = red32(red16(hq));
    if (g == 0) {
      u32x2 v = {__float_as_uint(hs), __float_as_uint(hq)};
      __builtin_amdgcn_raw_buffer_store_b64(v, srs, (unsigned)(((ct * 2 + kg) * S_ + mblk * 16 + r) * 8), 0, 16);
    }
    asm volatile("" :: "v"(touch));
    WAIT_VM(0);
    __syncthreads();
    if (tid == 0) __hip_atomic_fetch_add((gu32*)(cnt_h + (rt >> 1) * 64), 1u, __ATOMIC_RELAXED, __HIP_MEMORY_SCOPE_AGENT);
    STAMP(2,5);
  }
  const int rt3 = (xcd >> 1) * 4 + (idx >> 3);
  const int cg = (xcd & 1) * 8 + (idx & 7);
  const int f0 = cg < 11 ? cg * 96 : 1056 + (cg - 11) * 64;
  const int nvalid = cg < 11 ? 6 : 4;
  const int rt4 = xcd * 4 + (idx >> 3);
  const int ct4 = idx & 7;
  if (wave >= 4) {
    const int lw = wave - 4;
    const unsigned lbase = lds_addr(smem);
    {
      const char* abase = (const char*)(hf + (size_t)((rt3 * 8 + lw * 2) * 16) * 512);
      const long wdelta = (const char*)wu - (const char*)wg;
      const char* bsrc[6];
#pragma unroll
      for (int i = 0; i < 6; ++i) {
        const int line = (lw * 6 + i) * 4 + g;
        const int kq = line / 6, cgrp = line % 6;
        const int k = kq * 4 + ((lane >> 2) & 3);
        const int ch = (lane & 3) ^ (((k >> 3) & 1) << 1);
        int c3 = cgrp < 3 ? cgrp : cgrp - 3;
        c3 = (c3 * 2 < nvalid) ? c3 : 0;
        bsrc[i] = (const char*)(wg + f0) + (unsigned)(k * F_ + c3 * 32 + ch * 8) * 2u + (cgrp >= 3 ? wdelta : 0);
      }
      auto stA = [&](int s) {
        const unsigned slot = lbase + (s % K3_NSLOT) * K3_SLOT;
#pragma unroll
        for (int i = 0; i < 4; ++i)
          glds16_sc1(abase + (i >> 1) * 16384 + (s * 2 + (i & 1)) * 1024, lane * 16, slot + (lw * 4 + i) * 1024);
      };
      auto stB = [&](int s) {
        const unsigned slot = lbase + (s % K3_NSLOT) * K3_SLOT;
#pragma unroll
        for (int i = 0; i < 6; ++i) glds16v(bsrc[i] + (size_t)s * 64 * F_ * 2, slot + 16384 + (lw * 6 + i) * 1024);
      };
      stB(0); stB(1); stB(2); stB(3);
      poll_ge(cnt_h + rt3 * 64, 16u);
      stA(0); stA(1); stA(2); stA(3);
      wait_vm<12>(); BARE_BAR();
      wait_vm<8>();  BARE_BAR(); stA(4); stB(4);
      wait_vm<14>(); BARE_BAR(); stA(5); stB(5);
      wait_vm<20>(); BARE_BAR(); stA(6); stB(6);
      wait_vm<20>(); BARE_BAR(); stA(7); stB(7);
      wait_vm<20>(); BARE_BAR();
      wait_vm<10>(); BARE_BAR();
      wait_vm<0>();  BARE_BAR();
    }
    {
      const char* abase = (const char*)(hhf + (size_t)((rt4 * 4 + lw) * KS4) * 512);
      const char* bbase = (const char*)(wd + (size_t)lw * 32 * D_ + ct4 * 64);
      const unsigned boffs = (unsigned)(((g >> 1) * 4 + ((lane >> 2) & 3)) * D_ + (g & 1) * 32 + (lane & 3) * 8) * 2u;
      auto stA = [&](int s) {
        const unsigned slot = lbase + (s & 3) * 32768;
#pragma unroll
        for (int i = 0; i < 4; ++i)
          glds16_sc1(abase + (s * 4 + ((s == 10 && i == 3) ? 2 : i)) * 1024, lane * 16, slot + lw * 4096 + i * 1024);
      };
      auto stB = [&](int s) {
        const unsigned slot = lbase + (s & 3) * 32768;
        const int so = (s == 10 && lw == 3) ? s - 1 : s;
#pragma unroll
        for (int i = 0; i < 4; ++i)
          glds16(bbase + (size_t)(so * 128 + i * 8) * D_ * 2, (i & 1) ? (boffs ^ 32u) : boffs, slot + 16384 + lw * 4096 + i * 1024);
      };
      stB(0); stB(1); stB(2);
      BARE_BAR();
      stB(3);
      poll_ge(cnt_hh + (rt4 >> 1) * 64, 16u);
      stA(0); stA(1); stA(2); stA(3);
      wait_vm<12>(); BARE_BAR();
      wait_vm<8>();  BARE_BAR(); stA(4); stB(4);
      wait_vm<12>(); BARE_BAR(); stA(5); stB(5);
#pragma unroll
      for (int s = 2; s < 10; ++s) {
        if (s <= 7) wait_vm<16>(); else if (s == 8) wait_vm<8>(); else wait_vm<0>();
        BARE_BAR();
        if (s + 4 < 11) { stA(s + 4); stB(s + 4); }
      }
    }
    return;
  }
  const int wr = wave >> 1, wc = wave & 1;
  {
    const int aoff = wr * 8192 + lane * 16;
    const int bbase2 = 16384 + (2 * g) * 1536 + q4 * 64 + p * 8;
    int boff[3];
#pragma unroll
    for (int j = 0; j < 3; ++j) {
      const int fbl = 3 * wc + j;
      boff[j] = bbase2 + (fbl >> 1) * 256 + (((fbl & 1) ^ (g & 1)) << 5);
    }
    u32x2 hp[16];
    f32x4 cg4[3], cu4[3];
    float mean_l = 0.f, inv2_l = 0.f;
    f32x4 accg[4][3] = {}, accu[4][3] = {};
    u32x4 fa[2][4], fb[2][6];
    auto rdfrags = [&](int t) {
      const char* slot = smem + ((t >> 1) % K3_NSLOT) * K3_SLOT;
      const int kk = t & 1;
#pragma unroll
      for (int i = 0; i < 4; ++i) fa[kk][i] = *(const u32x4*)(slot + aoff + i * 2048 + kk * 1024);
#pragma unroll
      for (int j = 0; j < 3; ++j) {
        fb[kk][j] = tr_frag(slot + boff[j] + kk * 12288, slot + boff[j] + kk * 12288 + 1536);
        fb[kk][3 + j] = tr_frag(slot + boff[j] + 768 + kk * 12288, slot + boff[j] + 768 + kk * 12288 + 1536);
      }
    };
    auto compute = [&](int t) {
      const int kk = t & 1;
      const u32x4* a = fa[kk];
#pragma unroll
      for (int j = 0; j < 3; ++j) {
        const u32x4 bg = fb[kk][j], bu = fb[kk][3 + j];
#pragma unroll
        for (int i = 0; i < 4; ++i) {
          accg[i][j] = mfma16(bg, a[i], accg[i][j]);
          accu[i][j] = mfma16(bu, a[i], accu[i][j]);
        }
      }
    };
    BARE_BAR();
    STAMP(3,0);
    rdfrags(0);
#pragma unroll
    for (int s = 0; s < 8; ++s) {
      rdfrags(2 * s + 1);
      compute(2 * s);
      __builtin_amdgcn_sched_barrier(0);
      if (s + 1 < 8) {
        RAW_BAR();
        STAMP(3,1+s);
        rdfrags(2 * s + 2);
      }
      if (s == 4) {
#pragma unroll
        for (int i = 0; i < 16; ++i)
          hp[i] = __builtin_amdgcn_raw_buffer_load_b64(srs, (unsigned)((i * S_ + rt3 * 128 + wr * 64 + lane) * 8), 0, 16);
      }
      if (s == 6) {
        f32x2 t2 = {};
#pragma unroll
        for (int i = 0; i < 16; ++i) t2 += __builtin_bit_cast(f32x2, hp[i]);
        mean_l = t2[0] * (1.f / 512.f);
        const float var = fmaxf(t2[1] * (1.f / 512.f) - mean_l * mean_l, 0.f);
        inv2_l = __builtin_amdgcn_rcpf(var + 1e-5f);
        asm volatile("" : "+v"(mean_l), "+v"(inv2_l));
#pragma unroll
        for (int j = 0; j < 3; ++j) {
          const int fbl = (3 * wc + j) < nvalid ? (3 * wc + j) : 0;
          const unsigned co = (unsigned)(S_ * 32 * 4 + (rt3 * 2752 + f0 + fbl * 16 + 4 * g) * 4);
          cg4[j] = __builtin_bit_cast(f32x4, __builtin_amdgcn_raw_buffer_load_b128(srs, co, 0, 16));
          cu4[j] = __builtin_bit_cast(f32x4, __builtin_amdgcn_raw_buffer_load_b128(srs, co + F_ * 4, 0, 16));
        }
      }
      compute(2 * s + 1);
      __builtin_amdgcn_sched_barrier(0);
    }
    STAMP(3,9);
    const __amdgpu_buffer_rsrc_t hhrs = __builtin_amdgcn_make_buffer_rsrc(hhf, 0, 128 * KS4 * 1024, 0x00020000);
#pragma unroll
    for (int ip = 0; ip < 2; ++ip) {
      float mean[2], inv2[2];
#pragma unroll
      for (int i2 = 0; i2 < 2; ++i2) {
        mean[i2] = __shfl(mean_l, (ip * 2 + i2) * 16 + r);
        inv2[i2] = __shfl(inv2_l, (ip * 2 + i2) * 16 + r);
      }
      const int mblk = rt3 * 8 + wr * 4 + ip * 2 + (g & 1);
#pragma unroll
      for (int j = 0; j < 3; ++j) {
        const int fbl = 3 * wc + j;
        u32x2 o[2];
#pragma unroll
        for (int i2 = 0; i2 < 2; ++i2) {
          const int i = ip * 2 + i2;
          float hv[4];
#pragma unroll
          for (int e = 0; e < 4; ++e)
            hv[e] = ((accg[i][j][e] - mean[i2] * cg4[j][e]) * inv2[i2]) * (accu[i][j][e] - mean[i2] * cu4[j][e]);
          o[i2][0] = pack_bf(hv[0], hv[1]); o[i2][1] = pack_bf(hv[2], hv[3]);
        }
        const u32x4 v = merge16(o[0], o[1]);
        if (fbl < nvalid) {
          const int fb_ = (f0 >> 4) + fbl;
          __builtin_amdgcn_raw_buffer_store_b128(
              v, hhrs, (unsigned)(((mblk * KS4 + (fb_ >> 1)) * 64 + r + 16 * (2 * (fb_ & 1) + (g >> 1))) * 16), 0, 16);
        }
      }
    }
    WAIT_VM(0);
    RAW_BAR();
    if (tid == 0) __hip_atomic_fetch_add((gu32*)(cnt_hh + rt3 * 64), 1u, __ATOMIC_RELAXED, __HIP_MEMORY_SCOPE_AGENT);
    STAMP(3,10);
  }
  {
    const int boff = 16384 + (2 * g) * 512 + wc * 256 + q4 * 64 + p * 8;
    const int aoff = wr * 8192 + lane * 16;
    u32x2 hr[2][2];
    f32x4 acc[2][2] = {};
    u32x4 fa[2][2][2], fb[2][2][2];
    auto rdfrags = [&](int t) {
      const char* slot = smem + ((t >> 1) & 3) * 32768;
#pragma unroll
      for (int kk = 0; kk < 2; ++kk) {
        const int ksl = (t & 1) * 2 + kk;
#pragma unroll
        for (int mb = 0; mb < 2; ++mb) fa[t & 1][kk][mb] = *(const u32x4*)(slot + aoff + mb * 4096 + ksl * 1024);
#pragma unroll
        for (int nb = 0; nb < 2; ++nb) {
          const int ho = (nb ^ (g & 1)) << 5;
          fb[t & 1][kk][nb] = tr_frag(slot + boff + ksl * 4096 + ho, slot + boff + ksl * 4096 + 512 + ho);
        }
      }
    };
    auto compute = [&](int t) {
#pragma unroll
      for (int kk = 0; kk < 2; ++kk) {
        if (2 * t + kk < KS4) {
#pragma unroll
          for (int mb = 0; mb < 2; ++mb)
#pragma unroll
            for (int nb = 0; nb < 2; ++nb) acc[mb][nb] = mfma16(fb[t & 1][kk][nb], fa[t & 1][kk][mb], acc[mb][nb]);
        }
      }
    };
    BARE_BAR();
    STAMP(4,0);
    rdfrags(0);
#pragma unroll
    for (int mb = 0; mb < 2; ++mb)
#pragma unroll
      for (int nb = 0; nb < 2; ++nb)
        hr[mb][nb] = __builtin_amdgcn_raw_buffer_load_b64(
            hrs, (unsigned)((((rt4 * 4 + wr * 2 + mb) * 16 + ct4 * 2 + wc) * 64 + r + 16 * (2 * nb + (g >> 1))) * 16 + (g & 1) * 8), 0, 16);
#pragma unroll
    for (int s = 0; s < 11; ++s) {
      rdfrags(2 * s + 1);
      compute(2 * s);
      if (s + 1 < 11) {
        RAW_BAR();
        STAMP(4,1+s);
        rdfrags(2 * s + 2);
      }
      compute(2 * s + 1);
    }
#pragma unroll
    for (int mb = 0; mb < 2; ++mb)
#pragma unroll
      for (int nb = 0; nb < 2; ++nb) {
        const f32x4 o4 = acc[mb][nb];
        u32x2 o;
        o[0] = pack_bf(bf_lo(hr[mb][nb][0]) + o4[0], bf_hi(hr[mb][nb][0]) + o4[1]);
        o[1] = pack_bf(bf_lo(hr[mb][nb][1]) + o4[2], bf_hi(hr[mb][nb][1]) + o4[3]);
        *(u32x2*)(out + (size_t)(rt4 * 64 + wr * 32 + mb * 16 + r) * D_ + ct4 * 64 + wc * 32 + nb * 16 + g * 4) = o;
      }
    STAMP(4,14);
  }
}

extern "C" void kernel_launch(void* const* d_in, const int* in_sizes, int n_in, void* d_out, int out_size,
                              void* d_ws, size_t ws_size, hipStream_t stream) {
  const u16* x = (const u16*)d_in[0];
  const u16* wqkv = (const u16*)d_in[1];
  const u16* wo = (const u16*)d_in[2];
  const u16* wg = (const u16*)d_in[4];
  const u16* wu = (const u16*)d_in[5];
  const u16* wd = (const u16*)d_in[6];
  u16* out = (u16*)d_out;
  char* ws = (char*)d_ws;
  u16* pq = (u16*)(ws);
  u16* hf = (u16*)(ws + (2u << 20));
  u16* wp = (u16*)(ws + (4u << 20));
  float* part = (float*)(ws + (5u << 20));
  unsigned* cnt = (unsigned*)(ws + (6u << 20));
  float* csp = (float*)(ws + (6u << 20) + 65536);
  float* hst = (float*)(ws + (7u << 20));
  u16* hhf = (u16*)(ws + (8u << 20));
  k1_qkv<<<256, 512, 0, stream>>>(x, wqkv, pq, part, wg, wu, csp, cnt, wo, wd, out);
  kb_rest<<<256, 512, 0, stream>>>(x, pq, part, wo, wp, cnt, hf, hst, csp, wg, wu, wd, hhf, out);
}
```

```cpp
#include <hip/hip_runtime.h>
#include <hip/hip_bf16.h>
#include <stdint.h>

typedef unsigned short u16;
typedef __attribute__((ext_vector_type(8))) __bf16 bf16x8;
typedef __attribute__((ext_vector_type(2))) __bf16 bf16x2;
typedef __attribute__((ext_vector_type(4))) short s16x4;
typedef __attribute__((ext_vector_type(4))) float f32x4;
typedef __attribute__((ext_vector_type(2))) float f32x2;
typedef __attribute__((ext_vector_type(4))) unsigned u32x4;
typedef __attribute__((ext_vector_type(2))) unsigned u32x2;
typedef __attribute__((address_space(3))) s16x4* lds_s16x4_ptr;

#define DI __device__ __forceinline__
#define STAMP(kid,i)

constexpr int S_ = 2048, D_ = 512, F_ = 1376, H_ = 32;
constexpr int KS4 = F_ / 32;

DI float bf_lo(unsigned u) { return __uint_as_float(u << 16); }
DI float bf_hi(unsigned u) { return __uint_as_float(u & 0xffff0000u); }
DI unsigned pack_bf(float lo, float hi) {
  f32x2 f = {lo, hi};
  bf16x2 r = __builtin_convertvector(f, bf16x2);
  return __builtin_bit_cast(unsigned, r);
}
DI float bfr(float f) { return bf_lo(pack_bf(f, 0.f) & 0xffffu); }
DI f32x4 mfma16(u32x4 a, u32x4 b, f32x4 c) {
  return __builtin_amdgcn_mfma_f32_16x16x32_bf16(__builtin_bit_cast(bf16x8, a),
                                                 __builtin_bit_cast(bf16x8, b), c, 0, 0, 0);
}
DI float dot2(unsigned a, unsigned b, float c) {
  return __builtin_amdgcn_fdot2_f32_bf16(__builtin_bit_cast(bf16x2, a), __builtin_bit_cast(bf16x2, b), c, false);
}
DI u32x4 tr_frag(const char* lds0, const char* lds1) {
  s16x4 lo = __builtin_amdgcn_ds_read_tr16_b64_v4i16((lds_s16x4_ptr)lds0);
  s16x4 hi = __builtin_amdgcn_ds_read_tr16_b64_v4i16((lds_s16x4_ptr)lds1);
  u32x2 l2 = __builtin_bit_cast(u32x2, lo), h2 = __builtin_bit_cast(u32x2, hi);
  u32x4 r = {l2[0], l2[1], h2[0], h2[1]};
  return r;
}
DI unsigned lds_addr(char* l) { return (unsigned)(size_t)(__attribute__((address_space(3))) char*)l; }
DI void glds16(const void* sbase, unsigned voff, unsigned dst) {
  asm volatile("s_mov_b32 m0, %2\n\ts_nop 0\n\tglobal_load_lds_dwordx4 %0, %1"
               :: "v"(voff), "s"(sbase), "s"(dst) : "memory");
}
DI float red16(float s) {
  const unsigned u = __float_as_uint(s);
  auto r = __builtin_amdgcn_permlane16_swap(u, u, false, false);
  return __uint_as_float(r[0]) + __uint_as_float(r[1]);
}
DI float red32(float s) {
  const unsigned u = __float_as_uint(s);
  auto r = __builtin_amdgcn_permlane32_swap(u, u, false, false);
  return __uint_as_float(r[0]) + __uint_as_float(r[1]);
}
DI u32x4 merge16(u32x2 t0, u32x2 t1) {
  auto x = __builtin_amdgcn_permlane16_swap(t0[0], t1[0], false, false);
  auto y = __builtin_amdgcn_permlane16_swap(t0[1], t1[1], false, false);
  u32x4 v = {x[0], y[0], x[1], y[1]};
  return v;
}
#define WAIT_VM(n) asm volatile("s_waitcnt vmcnt(" #n ")" ::: "memory")
#define RAW_BAR() do { asm volatile("s_waitcnt lgkmcnt(0)" ::: "memory"); __builtin_amdgcn_s_barrier(); } while (0)
#define BARE_BAR() do { asm volatile("" ::: "memory"); __builtin_amdgcn_s_barrier(); } while (0)
template <int N> DI void wait_vm() { asm volatile("s_waitcnt vmcnt(%0)" ::"i"(N) : "memory"); }

DI float phi_f(float t) { return (t > 0.f ? t + 1.f : __expf(t)) + 1e-6f; }
DI void sq_frag(const u32x4& a, float& q) {
#pragma unroll
  for (int i = 0; i < 4; ++i) q = dot2(a[i], a[i], q);
}
#define ONES_FRAG ((u32x4){0x3f803f80u, 0x3f803f80u, 0x3f803f80u, 0x3f803f80u})

constexpr int K1_SLOT = 16384 + 12288, K1_NSLOT = 4;
__global__ __launch_bounds__(512) void k1_qkv(const u16* __restrict__ x, const u16* __restrict__ wqkv,
                                              u16* __restrict__ pq, float* __restrict__ part,
                                              const u16* __restrict__ wg, const u16* __restrict__ wu,
                                              float* __restrict__ csp, unsigned* __restrict__ cnt,
                                              const u16* __restrict__ wo, const u16* __restrict__ wd,
                                              const u16* outp) {
  __shared__ __attribute__((aligned(16))) char smem[K1_SLOT * K1_NSLOT];
  const int tid = threadIdx.x, lane = tid & 63;
  const int wave = __builtin_amdgcn_readfirstlane(tid >> 6);
  const int xcd = blockIdx.x & 7, idx = blockIdx.x >> 3;
  const int rt = (xcd >> 1) * 4 + (idx >> 3);
  const int cg = (xcd & 1) * 8 + (idx & 7);
  const int r = lane & 15, g = lane >> 4;
  STAMP(0,0);
  if (blockIdx.x == 0 && tid < 40) cnt[tid * 64] = 0u;
  if (wave >= 4) {
    const int lw = wave - 4;
    const unsigned lbase = lds_addr(smem);
    const unsigned aoffs = (unsigned)(lw * 32 + (lane >> 3)) * 1024u + (((lane & 7) ^ ((lane >> 4) & 3)) << 4);
    unsigned boffs[3];
#pragma unroll
    for (int i = 0; i < 3; ++i) {
      const int line = (lw * 3 + i) * 4 + g;
      const int kq = line / 3, cgrp = line % 3;
      const int k = kq * 4 + ((lane >> 2) & 3);
      const int ch = (lane & 3) ^ (((k >> 3) & 1) << 1);
      boffs[i] = (unsigned)(k * 1536 + cgrp * 512 + ch * 8) * 2u;
    }
    const char* abase = (const char*)(x + (size_t)rt * 128 * D_);
    const char* bbase = (const char*)(wqkv + cg * 32);
    auto stage = [&](int s) {
      const unsigned slot = lbase + (s % K1_NSLOT) * K1_SLOT;
#pragma unroll
      for (int i = 0; i < 4; ++i)
        glds16(abase + s * 128 + i * 8192, (i & 1) ? (aoffs ^ 64u) : aoffs, slot + lw * 4096 + i * 1024);
#pragma unroll
      for (int i = 0; i < 3; ++i)
        glds16(bbase + (size_t)s * 64 * 3072, boffs[i], slot + 16384 + (lw * 3 + i) * 1024);
    };
    stage(0); stage(1);
    wait_vm<7>();
    BARE_BAR();
    stage(2);
#pragma unroll
    for (int s = 0; s < 7; ++s) {
      if (s == 0 || s == 5) wait_vm<7>(); else if (s <= 4) wait_vm<14>(); else wait_vm<0>();
      BARE_BAR();
      if (s == 0) stage(3);
      if (s + 4 < 8) stage(s + 4);
    }
    const int W = blockIdx.x * 4 + lw;
    const int grp = W % 86, kp = (W / 86) & 7;
    u32x4 wv[4];
    {
      const u16* wsrc = (grp < 43 ? wg : wu) + (grp % 43) * 32 + (lane & 3) * 8 + (size_t)(kp * 64 + (lane >> 2)) * F_;
#pragma unroll
      for (int i = 0; i < 4; ++i) wv[i] = *(const u32x4*)(wsrc + (size_t)i * 16 * F_);
    }
    unsigned touch;
    {
      const int T = idx * 256 + lw * 64 + lane;
      const char* tp = (const char*)pq + (size_t)T * 4096;
      if (T >= 3584) tp = (const char*)outp + (size_t)(T - 3584) * 4096;
      if (T >= 4096) tp = (const char*)wo + (size_t)(T - 4096) * 4096;
      if (T >= 4224) tp = (const char*)wd + (size_t)(T - 4224) * 4096;
      if (T >= 4568) tp = (const char*)wg + (size_t)(T - 4568) * 4096;
      if (T >= 4912) tp = (const char*)wu + (size_t)(T - 4912) * 4096;
      if (T >= 5256) tp = (const char*)wu;
      touch = *(const volatile unsigned*)tp;
    }
    BARE_BAR();
    if (W < 86 * 8) {
      float c8[8];
#pragma unroll
      for (int j = 0; j < 4; ++j) {
        c8[2 * j] = (bf_lo(wv[0][j]) + bf_lo(wv[1][j])) + (bf_lo(wv[2][j]) + bf_lo(wv[3][j]));
        c8[2 * j + 1] = (bf_hi(wv[0][j]) + bf_hi(wv[1][j])) + (bf_hi(wv[2][j]) + bf_hi(wv[3][j]));
      }
#pragma unroll
      for (int j = 0; j < 8; ++j) {
        float v = c8[j];
        v += __shfl_xor(v, 4); v += __shfl_xor(v, 8);
        c8[j] = red32(red16(v));
      }
      if (lane < 4) {
        float* dst = csp + kp * 2752 + grp * 32 + lane * 8;
        f32x4 v0 = {c8[0], c8[1], c8[2], c8[3]}, v1 = {c8[4], c8[5], c8[6], c8[7]};
        *(f32x4*)dst = v0; *(f32x4*)(dst + 4) = v1;
      }
    }
    asm volatile("" :: "v"(touch));
    BARE_BAR();
    return;
  }
  const int wr = wave >> 1, wc = wave & 1;
  const int q4 = (lane & 15) >> 2, p = lane & 3;
  unsigned tv[5];
  {
    const int t = wave * 64 + lane;
    const int lx = idx * 128 + (t & 127);
    const char* px = (const char*)(x + (size_t)(xcd >> 1) * 512 * D_) + (size_t)lx * 128;
    const int lw_ = idx * 192 + (t < 192 ? t : 0);
    const char* pw = (const char*)wqkv + (size_t)(lw_ / 12) * 3072 + ((lw_ % 12) >> 2) * 1024 + (xcd & 1) * 512 + (lw_ & 3) * 128;
    if (t >= 240) px = (const char*)wo + (size_t)(blockIdx.x * 16 + t - 240) * 128;
    const size_t o = (size_t)(blockIdx.x * 43 + (t < 43 ? t : 0)) * 128;
    asm volatile("global_load_dword %0, %1, off" : "=v"(tv[0]) : "v"(pw) : "memory");
    if (t < 128 || t >= 240) asm volatile("global_load_dword %0, %1, off" : "=v"(tv[1]) : "v"(px) : "memory");
    if (wave == 2) {
      asm volatile("global_load_dword %0, %1, off" : "=v"(tv[2]) : "v"((const char*)wg + (size_t)(blockIdx.x * 43 + (lane < 43 ? lane : 0)) * 128) : "memory");
      asm volatile("global_load_dword %0, %1, off" : "=v"(tv[3]) : "v"((const char*)wu + (size_t)(blockIdx.x * 43 + (lane < 43 ? lane : 0)) * 128) : "memory");
      asm volatile("global_load_dword %0, %1, off" : "=v"(tv[4]) : "v"((const char*)wd + (size_t)(blockIdx.x * 43 + (lane < 43 ? lane : 0)) * 128) : "memory");
    }
    (void)o;
  }
  const int aoff = (wr * 64 + r) * 128 + ((g ^ ((r >> 1) & 7)) << 4);
  const int aoff0 = aoff + wc * 4096, aoff1 = aoff + (wc ^ 1) * 4096;
  const int boff = 16384 + (2 * g) * 768 + q4 * 64 + ((wc ^ (g & 1)) << 5) + p * 8;
  const u32x4 ones = ONES_FRAG;
  f32x4 accq[4] = {}, acck[4] = {}, accv[4] = {};
  f32x4 rs[2] = {}, sqm[2] = {}, cq = {}, ck = {}, cv = {};
  u32x4 fa[2][4], fb[2][3];
  auto rdfrags = [&](int t) {
    const char* slot = smem + ((t >> 1) % K1_NSLOT) * K1_SLOT;
    const int kk = t & 1;
#pragma unroll
    for (int i = 0; i < 4; ++i)
      fa[kk][i] = *(const u32x4*)(slot + (((i < 2 ? aoff0 : aoff1) + (i & 1) * 2048) ^ (kk << 6)));
#pragma unroll
    for (int j = 0; j < 3; ++j)
      fb[kk][j] = tr_frag(slot + boff + kk * 6144 + j * 256, slot + boff + kk * 6144 + 768 + j * 256);
  };
  auto compute = [&](int t) {
    const int kk = t & 1;
    const u32x4* a = fa[kk];
    const u32x4 bq = fb[kk][0], bk = fb[kk][1], bv = fb[kk][2];
#pragma unroll
    for (int i = 0; i < 4; ++i) {
      accq[i] = mfma16(bq, a[i], accq[i]);
      acck[i] = mfma16(a[i], bk, acck[i]);
      accv[i] = mfma16(a[i], bv, accv[i]);
    }
    rs[0] = mfma16(ones, a[0], rs[0]);
    rs[1] = mfma16(ones, a[1], rs[1]);
    sqm[0] = mfma16(a[0], a[0], sqm[0]);
    sqm[1] = mfma16(a[1], a[1], sqm[1]);
    if (kk == wr) {
      cq = mfma16(bq, ones, cq);
      ck = mfma16(ones, bk, ck);
      cv = mfma16(ones, bv, cv);
    }
  };
  BARE_BAR();
  STAMP(0,2);
  rdfrags(0);
#pragma unroll
  for (int s = 0; s < 8; ++s) {
    rdfrags(2 * s + 1);
    compute(2 * s);
    __builtin_amdgcn_sched_barrier(0);
    if (s + 1 < 8) {
      RAW_BAR();
      STAMP(0,3+s);
      rdfrags(2 * s + 2);
    }
    compute(2 * s + 1);
    __builtin_amdgcn_sched_barrier(0);
  }
  STAMP(0,11);
  float* st = (float*)(smem);
  float* csl = (float*)(smem + 2048);
  float* st2 = (float*)(smem + 4096);
  float* red = (float*)(smem + 8192);
  {
#pragma unroll
    for (int i = 0; i < 2; ++i) {
      const float q01 = (r & 1) ? sqm[i][1] : sqm[i][0], q23 = (r & 1) ? sqm[i][3] : sqm[i][2];
      const float q = (r & 2) ? q23 : q01;
      if (g == (r >> 2)) {
        f32x2 v = {rs[i][0], q};
        *(f32x2*)(st + (wr * 64 + wc * 32 + i * 16 + r) * 2) = v;
      }
    }
    float* c = csl + (wr * 2 + wc) * 48;
    if (r == 0) *(f32x4*)(c + 4 * g) = cq;
    if (g == 0) { c[16 + r] = ck[0]; c[32 + r] = cv[0]; }
  }
  __syncthreads();
  STAMP(0,12);
  {
    const f32x2 a0 = *(const f32x2*)(st + (wr * 64 + lane) * 2);
    const float mean = a0[0] * (1.f / 512.f);
    const float var = fmaxf(a0[1] * (1.f / 512.f) - mean * mean, 0.f);
    f32x2 v = {mean, __builtin_amdgcn_rsqf(var + 1e-5f)};
    *(f32x2*)(st2 + (wave * 64 + lane) * 2) = v;
  }
  const f32x4 csq4 = *(const f32x4*)(csl + wc * 48 + 4 * g) + *(const f32x4*)(csl + (2 + wc) * 48 + 4 * g);
  const float csk = csl[wc * 48 + 16 + r] + csl[(2 + wc) * 48 + 16 + r];
  const float csv = csl[wc * 48 + 32 + r] + csl[(2 + wc) * 48 + 32 + r];
  f32x4 kv = {}, zz = {};
#pragma unroll
  for (int ih = 0; ih < 2; ++ih) {
    u32x4 pk, vv;
#pragma unroll
    for (int i2 = 0; i2 < 2; ++i2) {
      const int i = ih * 2 + i2;
      const int mbl = ((ih ^ wc) << 1) + i2;
      {
        const f32x2 mi = *(const f32x2*)(st2 + (wave * 64 + mbl * 16 + r) * 2);
        float qv[4];
#pragma unroll
        for (int j = 0; j < 4; ++j) qv[j] = phi_f((accq[i][j] - mi[0] * csq4[j]) * mi[1]);
        u32x2 o = {pack_bf(qv[0], qv[1]), pack_bf(qv[2], qv[3])};
        const int mblk = rt * 8 + wr * 4 + mbl;
        *(u32x2*)(pq + ((size_t)(mblk * 16 + cg) * 64 + r + 16 * (2 * wc + (g >> 1))) * 8 + (g & 1) * 4) = o;
      }
      {
        const f32x4 m0 = *(const f32x4*)(st2 + (wave * 64 + mbl * 16 + 4 * g) * 2);
        const f32x4 m1 = *(const f32x4*)(st2 + (wave * 64 + mbl * 16 + 4 * g) * 2 + 4);
        const float mean[4] = {m0[0], m0[2], m1[0], m1[2]}, inv[4] = {m0[1], m0[3], m1[1], m1[3]};
        float kk[4], vj[4];
#pragma unroll
        for (int j = 0; j < 4; ++j) {
          kk[j] = phi_f((acck[i][j] - mean[j] * csk) * inv[j]);
          vj[j] = (accv[i][j] - mean[j] * csv) * inv[j];
        }
        pk[2 * i2] = pack_bf(kk[0], kk[1]);
        pk[2 * i2 + 1] = pack_bf(kk[2], kk[3]);
        vv[2 * i2] = pack_bf(vj[0], vj[1]);
        vv[2 * i2 + 1] = pack_bf(vj[2], vj[3]);
      }
    }
    kv = mfma16(pk, vv, kv);
    zz = mfma16(pk, ones, zz);
  }
  {
    float* rd = red + (wc * 2 + wr) * 272;
#pragma unroll
    for (int j = 0; j < 4; ++j) rd[(4 * g + j) * 16 + r] = kv[j];
    if (r == 0) *(f32x4*)(rd + 256 + 4 * g) = zz;
  }
  __syncthreads();
  for (int i = tid; i < 544; i += 256) {
    const int hh = i >= 272 ? 1 : 0, e = i - hh * 272;
    const float* rr = red + hh * 2 * 272 + e;
    part[(size_t)((cg * 2 + hh) * 16 + rt) * 272 + e] = rr[0] + rr[272];
  }
  asm volatile("s_waitcnt vmcnt(0)" :: "v"(tv[0]), "v"(tv[1]), "v"(tv[2]), "v"(tv[3]), "v"(tv[4]) : "memory");
  STAMP(0,13);
}

typedef __attribute__((address_space(1))) unsigned gu32;
DI void poll_ge(unsigned* c, unsigned target) {
  for (unsigned spins = 0; spins < (1u << 17); ++spins) {
    const unsigned v = __builtin_amdgcn_readfirstlane(
        __hip_atomic_load((gu32*)c, __ATOMIC_RELAXED, __HIP_MEMORY_SCOPE_AGENT));
    if (v >= target) break;
    __builtin_amdgcn_s_sleep(1);
  }
  asm volatile("" ::: "memory");
}
DI void glds16_sc1(const void* sbase, unsigned voff, unsigned dst) {
  asm volatile("s_mov_b32 m0, %2\n\ts_nop 0\n\tglobal_load_lds_dwordx4 %0, %1 sc1"
               :: "v"(voff), "s"(sbase), "s"(dst) : "memory");
}
DI void glds16v(const char* gsrc, unsigned dst) {
  asm volatile("s_mov_b32 m0, %1\n\ts_nop 0\n\tglobal_load_lds_dwordx4 %0, off" :: "v"(gsrc), "s"(dst) : "memory");
}
constexpr int K3_SLOT = 16384 + 24576, K3_NSLOT = 4;

__global__ __launch_bounds__(512) void kb_rest(const u16* __restrict__ x, const u16* __restrict__ pq,
                                               const float* __restrict__ part, const u16* __restrict__ wo,
                                               u16* wp, unsigned* cnt, u16* hf, float* hst,
                                               const float* __restrict__ csp,
                                               const u16* __restrict__ wg, const u16* __restrict__ wu,
                                               const u16* __restrict__ wd, u16* hhf, u16* __restrict__ out) {
  __shared__ __attribute__((aligned(16))) char smem[K3_SLOT * K3_NSLOT];
  const int tid = threadIdx.x, lane = tid & 63;
  const int wave = __builtin_amdgcn_readfirstlane(tid >> 6);
  const int xcd = blockIdx.x & 7, idx = blockIdx.x >> 3;
  const int r = lane & 15, g = lane >> 4;
  const int q4 = (lane & 15) >> 2, p = lane & 3;
  const __amdgpu_buffer_rsrc_t hrs = __builtin_amdgcn_make_buffer_rsrc(hf, 0, S_ * D_ * 2, 0x00020000);
  const __amdgpu_buffer_rsrc_t srs = __builtin_amdgcn_make_buffer_rsrc(hst, 0, S_ * 32 * 4 + 16 * 2752 * 4, 0x00020000);
  unsigned* cnt_w = cnt;
  unsigned* cnt_h = cnt + 8 * 64;
  unsigned* cnt_hh = cnt + 24 * 64;
  STAMP(2,0);
  {
    const int rt = xcd * 4 + (idx >> 3);
    const int ct = idx & 7;
    const int kg = wave >> 2, rb = wave & 3;
    const int mblk = rt * 4 + rb;
    const __amdgpu_buffer_rsrc_t wrs = __builtin_amdgcn_make_buffer_rsrc(wp, 0, 524288 + 8192, 0x00020000);
    const int ph = blockIdx.x >> 3, pc = blockIdx.x & 7;
    float* kv = (float*)(smem + 65536 + 16384);
    float pv[16];
    if (tid < 272) {
#pragma unroll
      for (int pp = 0; pp < 16; ++pp) pv[pp] = part[(size_t)(ph * 16 + pp) * 272 + tid];
    }
    float w[16];
    if (tid < 256) {
#pragma unroll
      for (int e = 0; e < 16; ++e) w[e] = bf_lo(wo[(size_t)(ph * 16 + e) * D_ + pc * 64 + (tid & 63)]);
    }
    if (tid < 272) {
      float s = 0.f;
#pragma unroll
      for (int pp = 0; pp < 16; ++pp) s += pv[pp];
      kv[tid] = s;
    }
    __syncthreads();
    STAMP(2,1);
    if (tid < 256) {
      const int c = tid & 63, dq = tid >> 6;
      float o[4];
#pragma unroll
      for (int j = 0; j < 4; ++j) {
        float s = 0.f;
#pragma unroll
        for (int e4 = 0; e4 < 4; ++e4) {
          const f32x4 k4 = *(const f32x4*)(kv + (dq * 4 + j) * 16 + e4 * 4);
          s = fmaf(k4[0], w[e4 * 4], s); s = fmaf(k4[1], w[e4 * 4 + 1], s);
          s = fmaf(k4[2], w[e4 * 4 + 2], s); s = fmaf(k4[3], w[e4 * 4 + 3], s);
        }
        o[j] = s;
      }
      const int n = pc * 64 + c, nb = n >> 4, cc = n & 15;
      const int ks = ph >> 1, gg = (ph & 1) * 2 + (dq >> 1);
      u32x2 v = {pack_bf(o[0], o[1]), pack_bf(o[2], o[3])};
      __builtin_amdgcn_raw_buffer_store_b64(v, wrs, (unsigned)((nb * 16 + ks) * 64 + gg * 16 + cc) * 16u + (dq & 1) * 8u, 0, 16);
    } else if (tid < 258) {
      const float* zs = kv + 256 + (tid - 256) * 8;
      u32x4 v = {pack_bf(zs[0], zs[1]), pack_bf(zs[2], zs[3]), pack_bf(zs[4], zs[5]), pack_bf(zs[6], zs[7])};
      __builtin_amdgcn_raw_buffer_store_b128(v, wrs, 524288u + (unsigned)(pc * 512 + ph * 16 + (tid - 256) * 8) * 2u, 0, 16);
    }
    WAIT_VM(0);
    __syncthreads();
    if (tid == 0) __hip_atomic_fetch_add((gu32*)(cnt_w + pc * 64), 1u, __ATOMIC_RELAXED, __HIP_MEMORY_SCOPE_AGENT);
    STAMP(2,2);
    float cpart[8];
    const int ccol = ((rt & 1) * 8 + ct) * 172 + (tid - 272);
    const bool do_cs = tid >= 272 && tid < 444;
    if (do_cs) {
#pragma unroll
      for (int kp = 0; kp < 8; ++kp) cpart[kp] = csp[kp * 2752 + ccol];
    }
    u32x4 a[8];
#pragma unroll
    for (int i = 0; i < 8; ++i) a[i] = *(const u32x4*)(pq + ((size_t)(mblk * 16 + kg * 8 + i) * 64 + lane) * 8);
    u32x2 xr[2];
#pragma unroll
    for (int j = 0; j < 2; ++j)
      xr[j] = *(const u32x2*)(x + (size_t)(mblk * 16 + r) * D_ + ct * 64 + (kg * 2 + j) * 16 + g * 4);
    if (do_cs) {
      const float c = ((cpart[0] + cpart[1]) + (cpart[2] + cpart[3])) + ((cpart[4] + cpart[5]) + (cpart[6] + cpart[7]));
      __builtin_amdgcn_raw_buffer_store_b32(__float_as_uint(c), srs, (unsigned)(S_ * 32 * 4 + ((rt >> 1) * 2752 + ccol) * 4), 0, 16);
    }
    if (wave == 0) poll_ge(cnt_w + ct * 64, 32u);
    __syncthreads();
    STAMP(2,3);
    u32x4 zf[8], wv[8];
#pragma unroll
    for (int i = 0; i < 8; ++i)
      zf[i] = __builtin_amdgcn_raw_buffer_load_b128(wrs, 524288u + (unsigned)(ct * 512 + (kg * 8 + i) * 32 + g * 8) * 2u, 0, 16);
#pragma unroll
    for (int i = 0; i < 8; ++i)
      wv[i] = __builtin_amdgcn_raw_buffer_load_b128(wrs, (unsigned)(ct * 65536 + (wave * 8 + i) * 1024 + lane * 16), 0, 16);
    unsigned touch = 0;
    {
      asm volatile("" ::: "memory");
      if (tid < 344) touch += *(const volatile unsigned*)((const char*)wd + (size_t)(idx * 344 + tid) * 128);
      if (tid < 384) {
        const int m = (tid / 12) & 1, rr = idx * 16 + (tid / 24), ln = tid % 12;
        const size_t off = (size_t)rr * (F_ * 2) + (xcd & 1) * 1536 + (size_t)ln * 128;
        const size_t lim = (size_t)rr * (F_ * 2) + F_ * 2 - 4;
        touch += *(const volatile unsigned*)((const char*)(m ? wu : wg) + (off < lim ? off : lim));
      }
    }
#pragma unroll
    for (int i = 0; i < 8; ++i) {
      float s = dot2(a[i][0], zf[i][0], 0.f);
      s = dot2(a[i][1], zf[i][1], s);
      s = dot2(a[i][2], zf[i][2], s);
      s = dot2(a[i][3], zf[i][3], s);
      const float inv = __builtin_amdgcn_rcpf(red16(s) + 1e-6f);
#pragma unroll
      for (int j = 0; j < 4; ++j) a[i][j] = pack_bf(bf_lo(a[i][j]) * inv, bf_hi(a[i][j]) * inv);
    }
#pragma unroll
    for (int i = 0; i < 8; ++i) *(u32x4*)(smem + (wave * 8 + i) * 1024 + lane * 16) = wv[i];
    __syncthreads();
    STAMP(2,4);
    f32x4 acc[4] = {};
#pragma unroll
    for (int i = 0; i < 8; ++i) {
#pragma unroll
      for (int nb = 0; nb < 4; ++nb) {
        const u32x4 b = *(const u32x4*)(smem + (nb * 16 + kg * 8 + i) * 1024 + lane * 16);
        acc[nb] = mfma16(b, a[i], acc[nb]);
      }
    }
    f32x4* xch = (f32x4*)(smem + 65536);
#pragma unroll
    for (int j = 0; j < 2; ++j) xch[(wave * 2 + j) * 64 + lane] = kg ? acc[j] : acc[2 + j];
    __syncthreads();
    float hs = 0.f, hq = 0.f;
    u32x2 oo[2];
#pragma unroll
    for (int j = 0; j < 2; ++j) {
      const f32x4 o4 = (kg ? acc[2 + j] : acc[j]) + xch[((wave ^ 4) * 2 + j) * 64 + lane];
      u32x2 o;
      o[0] = pack_bf(bf_lo(xr[j][0]) + o4[0], bf_hi(xr[j][0]) + o4[1]);
      o[1] = pack_bf(bf_lo(xr[j][1]) + o4[2], bf_hi(xr[j][1]) + o4[3]);
      oo[j] = o;
      hs = dot2(o[0], 0x3f803f80u, hs); hs = dot2(o[1], 0x3f803f80u, hs);
      hq = dot2(o[0], o[0], hq); hq = dot2(o[1], o[1], hq);
    }
    __builtin_amdgcn_raw_buffer_store_b128(
        merge16(oo[0], oo[1]), hrs, (unsigned)(((mblk * 16 + ct * 2 + kg) * 64 + r + 16 * (2 * (g & 1) + (g >> 1))) * 16), 0, 16);
    hs = red32(red16(hs)); hq = red32(red16(hq));
    if (g == 0) {
      u32x2 v = {__float_as_uint(hs), __float_as_uint(hq)};
      __builtin_amdgcn_raw_buffer_store_b64(v, srs, (unsigned)(((ct * 2 + kg) * S_ + mblk * 16 + r) * 8), 0, 16);
    }
    asm volatile("" :: "v"(touch));
    WAIT_VM(0);
    __syncthreads();
    if (tid == 0) __hip_atomic_fetch_add((gu32*)(cnt_h + (rt >> 1) * 64), 1u, __ATOMIC_RELAXED, __HIP_MEMORY_SCOPE_AGENT);
    STAMP(2,5);
  }
  const int rt3 = (xcd >> 1) * 4 + (idx >> 3);
  const int cg = (xcd & 1) * 8 + (idx & 7);
  const int f0 = cg < 11 ? cg * 96 : 1056 + (cg - 11) * 64;
  const int nvalid = cg < 11 ? 6 : 4;
  const int rt4 = xcd * 4 + (idx >> 3);
  const int ct4 = idx & 7;
  if (wave >= 4) {
    const int lw = wave - 4;
    const unsigned lbase = lds_addr(smem);
    {
      const char* abase = (const char*)(hf + (size_t)((rt3 * 8 + lw * 2) * 16) * 512);
      const long wdelta = (const char*)wu - (const char*)wg;
      const char* bsrc[6];
#pragma unroll
      for (int i = 0; i < 6; ++i) {
        const int line = (lw * 6 + i) * 4 + g;
        const int kq = line / 6, cgrp = line % 6;
        const int k = kq * 4 + ((lane >> 2) & 3);
        const int ch = (lane & 3) ^ (((k >> 3) & 1) << 1);
        int c3 = cgrp < 3 ? cgrp : cgrp - 3;
        c3 = (c3 * 2 < nvalid) ? c3 : 0;
        bsrc[i] = (const char*)(wg + f0) + (unsigned)(k * F_ + c3 * 32 + ch * 8) * 2u + (cgrp >= 3 ? wdelta : 0);
      }
      auto stA = [&](int s) {
        const unsigned slot = lbase + (s % K3_NSLOT) * K3_SLOT;
#pragma unroll
        for (int i = 0; i < 4; ++i)
          glds16_sc1(abase + (i >> 1) * 16384 + (s * 2 + (i & 1)) * 1024, lane * 16, slot + (lw * 4 + i) * 1024);
      };
      auto stB = [&](int s) {
        const unsigned slot = lbase + (s % K3_NSLOT) * K3_SLOT;
#pragma unroll
        for (int i = 0; i < 6; ++i) glds16v(bsrc[i] + (size_t)s * 64 * F_ * 2, slot + 16384 + (lw * 6 + i) * 1024);
      };
      stB(0); stB(1); stB(2); stB(3);
      poll_ge(cnt_h + rt3 * 64, 16u);
      stA(0); stA(1); stA(2); stA(3);
      wait_vm<12>(); BARE_BAR();
      wait_vm<8>();  BARE_BAR(); stA(4); stB(4);
      wait_vm<14>(); BARE_BAR(); stA(5); stB(5);
      wait_vm<20>(); BARE_BAR(); stA(6); stB(6);
      wait_vm<20>(); BARE_BAR(); stA(7); stB(7);
      wait_vm<20>(); BARE_BAR();
      wait_vm<10>(); BARE_BAR();
      wait_vm<0>();  BARE_BAR();
    }
    {
      const char* abase = (const char*)(hhf + (size_t)((rt4 * 4 + lw) * KS4) * 512);
      const char* bbase = (const char*)(wd + (size_t)lw * 32 * D_ + ct4 * 64);
      const unsigned boffs = (unsigned)(((g >> 1) * 4 + ((lane >> 2) & 3)) * D_ + (g & 1) * 32 + (lane & 3) * 8) * 2u;
      auto stA = [&](int s) {
        const unsigned slot = lbase + (s & 3) * 32768;
#pragma unroll
        for (int i = 0; i < 4; ++i)
          glds16_sc1(abase + (s * 4 + ((s == 10 && i == 3) ? 2 : i)) * 1024, lane * 16, slot + lw * 4096 + i * 1024);
      };
      auto stB = [&](int s) {
        const unsigned slot = lbase + (s & 3) * 32768;
        const int so = (s == 10 && lw == 3) ? s - 1 : s;
#pragma unroll
        for (int i = 0; i < 4; ++i)
          glds16(bbase + (size_t)(so * 128 + i * 8) * D_ * 2, (i & 1) ? (boffs ^ 32u) : boffs, slot + 16384 + lw * 4096 + i * 1024);
      };
      stB(0); stB(1); stB(2);
      BARE_BAR();
      stB(3);
      poll_ge(cnt_hh + (rt4 >> 1) * 64, 16u);
      stA(0); stA(1); stA(2); stA(3);
      wait_vm<12>(); BARE_BAR();
      wait_vm<8>();  BARE_BAR(); stA(4); stB(4);
      wait_vm<12>(); BARE_BAR(); stA(5); stB(5);
#pragma unroll
      for (int s = 2; s < 10; ++s) {
        if (s <= 7) wait_vm<16>(); else if (s == 8) wait_vm<8>(); else wait_vm<0>();
        BARE_BAR();
        if (s + 4 < 11) { stA(s + 4); stB(s + 4); }
      }
    }
    return;
  }
  const int wr = wave >> 1, wc = wave & 1;
  {
    const int aoff = wr * 8192 + lane * 16;
    const int bbase2 = 16384 + (2 * g) * 1536 + q4 * 64 + p * 8;
    int boff[3];
#pragma unroll
    for (int j = 0; j < 3; ++j) {
      const int fbl = 3 * wc + j;
      boff[j] = bbase2 + (fbl >> 1) * 256 + (((fbl & 1) ^ (g & 1)) << 5);
    }
    u32x2 hp[16];
    f32x4 cg4[3], cu4[3];
    float mean_l = 0.f, inv2_l = 0.f;
    f32x4 accg[4][3] = {}, accu[4][3] = {};
    u32x4 fa[2][4], fb[2][6];
    auto rdfrags = [&](int t) {
      const char* slot = smem + ((t >> 1) % K3_NSLOT) * K3_SLOT;
      const int kk = t & 1;
#pragma unroll
      for (int i = 0; i < 4; ++i) fa[kk][i] = *(const u32x4*)(slot + aoff + i * 2048 + kk * 1024);
#pragma unroll
      for (int j = 0; j < 3; ++j) {
        fb[kk][j] = tr_frag(slot + boff[j] + kk * 12288, slot + boff[j] + kk * 12288 + 1536);
        fb[kk][3 + j] = tr_frag(slot + boff[j] + 768 + kk * 12288, slot + boff[j] + 768 + kk * 12288 + 1536);
      }
    };
    auto compute = [&](int t) {
      const int kk = t & 1;
      const u32x4* a = fa[kk];
#pragma unroll
      for (int j = 0; j < 3; ++j) {
        const u32x4 bg = fb[kk][j], bu = fb[kk][3 + j];
#pragma unroll
        for (int i = 0; i < 4; ++i) {
          accg[i][j] = mfma16(bg, a[i], accg[i][j]);
          accu[i][j] = mfma16(bu, a[i], accu[i][j]);
        }
      }
    };
    BARE_BAR();
    STAMP(3,0);
    rdfrags(0);
#pragma unroll
    for (int s = 0; s < 8; ++s) {
      rdfrags(2 * s + 1);
      compute(2 * s);
      __builtin_amdgcn_sched_barrier(0);
      if (s + 1 < 8) {
        RAW_BAR();
        STAMP(3,1+s);
        rdfrags(2 * s + 2);
      }
      if (s == 4) {
#pragma unroll
        for (int i = 0; i < 16; ++i)
          hp[i] = __builtin_amdgcn_raw_buffer_load_b64(srs, (unsigned)((i * S_ + rt3 * 128 + wr * 64 + lane) * 8), 0, 16);
      }
      if (s == 6) {
        f32x2 t2 = {};
#pragma unroll
        for (int i = 0; i < 16; ++i) t2 += __builtin_bit_cast(f32x2, hp[i]);
        mean_l = t2[0] * (1.f / 512.f);
        const float var = fmaxf(t2[1] * (1.f / 512.f) - mean_l * mean_l, 0.f);
        inv2_l = __builtin_amdgcn_rcpf(var + 1e-5f);
        asm volatile("" : "+v"(mean_l), "+v"(inv2_l));
#pragma unroll
        for (int j = 0; j < 3; ++j) {
          const int fbl = (3 * wc + j) < nvalid ? (3 * wc + j) : 0;
          const unsigned co = (unsigned)(S_ * 32 * 4 + (rt3 * 2752 + f0 + fbl * 16 + 4 * g) * 4);
          cg4[j] = __builtin_bit_cast(f32x4, __builtin_amdgcn_raw_buffer_load_b128(srs, co, 0, 16));
          cu4[j] = __builtin_bit_cast(f32x4, __builtin_amdgcn_raw_buffer_load_b128(srs, co + F_ * 4, 0, 16));
        }
      }
      compute(2 * s + 1);
      __builtin_amdgcn_sched_barrier(0);
    }
    STAMP(3,9);
    const __amdgpu_buffer_rsrc_t hhrs = __builtin_amdgcn_make_buffer_rsrc(hhf, 0, 128 * KS4 * 1024, 0x00020000);
#pragma unroll
    for (int ip = 0; ip < 2; ++ip) {
      float mean[2], inv2[2];
#pragma unroll
      for (int i2 = 0; i2 < 2; ++i2) {
        mean[i2] = __shfl(mean_l, (ip * 2 + i2) * 16 + r);
        inv2[i2] = __shfl(inv2_l, (ip * 2 + i2) * 16 + r);
      }
      const int mblk = rt3 * 8 + wr * 4 + ip * 2 + (g & 1);
#pragma unroll
      for (int j = 0; j < 3; ++j) {
        const int fbl = 3 * wc + j;
        u32x2 o[2];
#pragma unroll
        for (int i2 = 0; i2 < 2; ++i2) {
          const int i = ip * 2 + i2;
          float hv[4];
#pragma unroll
          for (int e = 0; e < 4; ++e)
            hv[e] = ((accg[i][j][e] - mean[i2] * cg4[j][e]) * inv2[i2]) * (accu[i][j][e] - mean[i2] * cu4[j][e]);
          o[i2][0] = pack_bf(hv[0], hv[1]); o[i2][1] = pack_bf(hv[2], hv[3]);
        }
        const u32x4 v = merge16(o[0], o[1]);
        if (fbl < nvalid) {
          const int fb_ = (f0 >> 4) + fbl;
          __builtin_amdgcn_raw_buffer_store_b128(
              v, hhrs, (unsigned)(((mblk * KS4 + (fb_ >> 1)) * 64 + r + 16 * (2 * (fb_ & 1) + (g >> 1))) * 16), 0, 16);
        }
      }
    }
    WAIT_VM(0);
    RAW_BAR();
    if (tid == 0) __hip_atomic_fetch_add((gu32*)(cnt_hh + rt3 * 64), 1u, __ATOMIC_RELAXED, __HIP_MEMORY_SCOPE_AGENT);
    STAMP(3,10);
  }
  {
    const int boff = 16384 + (2 * g) * 512 + wc * 256 + q4 * 64 + p * 8;
    const int aoff = wr * 8192 + lane * 16;
    u32x2 hr[2][2];
    f32x4 acc[2][2] = {};
    u32x4 fa[2][2][2], fb[2][2][2];
    auto rdfrags = [&](int t) {
      const char* slot = smem + ((t >> 1) & 3) * 32768;
#pragma unroll
      for (int kk = 0; kk < 2; ++kk) {
        const int ksl = (t & 1) * 2 + kk;
#pragma unroll
        for (int mb = 0; mb < 2; ++mb) fa[t & 1][kk][mb] = *(const u32x4*)(slot + aoff + mb * 4096 + ksl * 1024);
#pragma unroll
        for (int nb = 0; nb < 2; ++nb) {
          const int ho = (nb ^ (g & 1)) << 5;
          fb[t & 1][kk][nb] = tr_frag(slot + boff + ksl * 4096 + ho, slot + boff + ksl * 4096 + 512 + ho);
        }
      }
    };
    auto compute = [&](int t) {
#pragma unroll
      for (int kk = 0; kk < 2; ++kk) {
        if (2 * t + kk < KS4) {
#pragma unroll
          for (int mb = 0; mb < 2; ++mb)
#pragma unroll
            for (int nb = 0; nb < 2; ++nb) acc[mb][nb] = mfma16(fb[t & 1][kk][nb], fa[t & 1][kk][mb], acc[mb][nb]);
        }
      }
    };
    BARE_BAR();
    STAMP(4,0);
    rdfrags(0);
#pragma unroll
    for (int mb = 0; mb < 2; ++mb)
#pragma unroll
      for (int nb = 0; nb < 2; ++nb)
        hr[mb][nb] = __builtin_amdgcn_raw_buffer_load_b64(
            hrs, (unsigned)((((rt4 * 4 + wr * 2 + mb) * 16 + ct4 * 2 + wc) * 64 + r + 16 * (2 * nb + (g >> 1))) * 16 + (g & 1) * 8), 0, 16);
#pragma unroll
    for (int s = 0; s < 11; ++s) {
      rdfrags(2 * s + 1);
      compute(2 * s);
      if (s + 1 < 11) {
        RAW_BAR();
        STAMP(4,1+s);
        rdfrags(2 * s + 2);
      }
      compute(2 * s + 1);
    }
#pragma unroll
    for (int mb = 0; mb < 2; ++mb)
#pragma unroll
      for (int nb = 0; nb < 2; ++nb) {
        const f32x4 o4 = acc[mb][nb];
        u32x2 o;
        o[0] = pack_bf(bf_lo(hr[mb][nb][0]) + o4[0], bf_hi(hr[mb][nb][0]) + o4[1]);
        o[1] = pack_bf(bf_lo(hr[mb][nb][1]) + o4[2], bf_hi(hr[mb][nb][1]) + o4[3]);
        *(u32x2*)(out + (size_t)(rt4 * 64 + wr * 32 + mb * 16 + r) * D_ + ct4 * 64 + wc * 32 + nb * 16 + g * 4) = o;
      }
    STAMP(4,14);
  }
}

extern "C" void kernel_launch(void* const* d_in, const int* in_sizes, int n_in, void* d_out, int out_size,
                              void* d_ws, size_t ws_size, hipStream_t stream) {
  const u16* x = (const u16*)d_in[0];
  const u16* wqkv = (const u16*)d_in[1];
  const u16* wo = (const u16*)d_in[2];
  const u16* wg = (const u16*)d_in[4];
  const u16* wu = (const u16*)d_in[5];
  const u16* wd = (const u16*)d_in[6];
  u16* out = (u16*)d_out;
  char* ws = (char*)d_ws;
  u16* pq = (u16*)(ws);
  u16* hf = (u16*)(ws + (2u << 20));
  u16* wp = (u16*)(ws + (4u << 20));
  float* part = (float*)(ws + (5u << 20));
  unsigned* cnt = (unsigned*)(ws + (6u << 20));
  float* csp = (float*)(ws + (6u << 20) + 65536);
  float* hst = (float*)(ws + (7u << 20));
  u16* hhf = (u16*)(ws + (8u << 20));
  k1_qkv<<<256, 512, 0, stream>>>(x, wqkv, pq, part, wg, wu, csp, cnt, wo, wd, out);
  kb_rest<<<256, 512, 0, stream>>>(x, pq, part, wo, wp, cnt, hf, hst, csp, wg, wu, wd, hhf, out);
}
```

```cpp
#include <hip/hip_runtime.h>
#include <hip/hip_bf16.h>
#include <stdint.h>

typedef unsigned short u16;
typedef __attribute__((ext_vector_type(8))) __bf16 bf16x8;
typedef __attribute__((ext_vector_type(2))) __bf16 bf16x2;
typedef __attribute__((ext_vector_type(4))) short s16x4;
typedef __attribute__((ext_vector_type(4))) float f32x4;
typedef __attribute__((ext_vector_type(2))) float f32x2;
typedef __attribute__((ext_vector_type(4))) unsigned u32x4;
typedef __attribute__((ext_vector_type(2))) unsigned u32x2;
typedef __attribute__((address_space(3))) s16x4* lds_s16x4_ptr;

#define DI __device__ __forceinline__
#define STAMP(kid,i)

constexpr int S_ = 2048, D_ = 512, F_ = 1376, H_ = 32;
constexpr int KS4 = F_ / 32;

DI float bf_lo(unsigned u) { return __uint_as_float(u << 16); }
DI float bf_hi(unsigned u) { return __uint_as_float(u & 0xffff0000u); }
DI unsigned pack_bf(float lo, float hi) {
  f32x2 f = {lo, hi};
  bf16x2 r = __builtin_convertvector(f, bf16x2);
  return __builtin_bit_cast(unsigned, r);
}
DI float bfr(float f) { return bf_lo(pack_bf(f, 0.f) & 0xffffu); }
DI f32x4 mfma16(u32x4 a, u32x4 b, f32x4 c) {
  return __builtin_amdgcn_mfma_f32_16x16x32_bf16(__builtin_bit_cast(bf16x8, a),
                                                 __builtin_bit_cast(bf16x8, b), c, 0, 0, 0);
}
DI float dot2(unsigned a, unsigned b, float c) {
  return __builtin_amdgcn_fdot2_f32_bf16(__builtin_bit_cast(bf16x2, a), __builtin_bit_cast(bf16x2, b), c, false);
}
DI u32x4 tr_frag(const char* lds0, const char* lds1) {
  s16x4 lo = __builtin_amdgcn_ds_read_tr16_b64_v4i16((lds_s16x4_ptr)lds0);
  s16x4 hi = __builtin_amdgcn_ds_read_tr16_b64_v4i16((lds_s16x4_ptr)lds1);
  u32x2 l2 = __builtin_bit_cast(u32x2, lo), h2 = __builtin_bit_cast(u32x2, hi);
  u32x4 r = {l2[0], l2[1], h2[0], h2[1]};
  return r;
}
DI unsigned lds_addr(char* l) { return (unsigned)(size_t)(__attribute__((address_space(3))) char*)l; }
DI void glds16(const void* sbase, unsigned voff, unsigned dst) {
  asm volatile("s_mov_b32 m0, %2\n\ts_nop 0\n\tglobal_load_lds_dwordx4 %0, %1"
               :: "v"(voff), "s"(sbase), "s"(dst) : "memory");
}
DI float red16(float s) {
  const unsigned u = __float_as_uint(s);
  auto r = __builtin_amdgcn_permlane16_swap(u, u, false, false);
  return __uint_as_float(r[0]) + __uint_as_float(r[1]);
}
DI float red32(float s) {
  const unsigned u = __float_as_uint(s);
  auto r = __builtin_amdgcn_permlane32_swap(u, u, false, false);
  return __uint_as_float(r[0]) + __uint_as_float(r[1]);
}
DI u32x4 merge16(u32x2 t0, u32x2 t1) {
  auto x = __builtin_amdgcn_permlane16_swap(t0[0], t1[0], false, false);
  auto y = __builtin_amdgcn_permlane16_swap(t0[1], t1[1], false, false);
  u32x4 v = {x[0], y[0], x[1], y[1]};
  return v;
}
#define WAIT_VM(n) asm volatile("s_waitcnt vmcnt(" #n ")" ::: "memory")
#define RAW_BAR() do { asm volatile("s_waitcnt lgkmcnt(0)" ::: "memory"); __builtin_amdgcn_s_barrier(); } while (0)
#define BARE_BAR() do { asm volatile("" ::: "memory"); __builtin_amdgcn_s_barrier(); } while (0)
template <int N> DI void wait_vm() { asm volatile("s_waitcnt vmcnt(%0)" ::"i"(N) : "memory"); }

DI float phi_f(float t) { return (t > 0.f ? t + 1.f : __expf(t)) + 1e-6f; }
DI void sq_frag(const u32x4& a, float& q) {
#pragma unroll
  for (int i = 0; i < 4; ++i) q = dot2(a[i], a[i], q);
}
#define ONES_FRAG ((u32x4){0x3f803f80u, 0x3f803f80u, 0x3f803f80u, 0x3f803f80u})

constexpr int K1_SLOT = 16384 + 12288, K1_NSLOT = 4;
__global__ __launch_bounds__(512) void k1_qkv(const u16* __restrict__ x, const u16* __restrict__ wqkv,
                                              u16* __restrict__ pq, float* __restrict__ part,
                                              const u16* __restrict__ wg, const u16* __restrict__ wu,
                                              float* __restrict__ csp, unsigned* __restrict__ cnt,
                                              const u16* __restrict__ wo, const u16* __restrict__ wd,
                                              const u16* outp) {
  __shared__ __attribute__((aligned(16))) char smem[K1_SLOT * K1_NSLOT];
  const int tid = threadIdx.x, lane = tid & 63;
  const int wave = __builtin_amdgcn_readfirstlane(tid >> 6);
  const int xcd = blockIdx.x & 7, idx = blockIdx.x >> 3;
  const int rt = (xcd >> 1) * 4 + (idx >> 3);
  const int cg = (xcd & 1) * 8 + (idx & 7);
  const int r = lane & 15, g = lane >> 4;
  STAMP(0,0);
  if (blockIdx.x == 0 && tid < 40) cnt[tid * 64] = 0u;
  if (wave >= 4) {
    const int lw = wave - 4;
    const unsigned lbase = lds_addr(smem);
    const unsigned aoffs = (unsigned)(lw * 32 + (lane >> 3)) * 1024u + (((lane & 7) ^ ((lane >> 4) & 3)) << 4);
    unsigned boffs[3];
#pragma unroll
    for (int i = 0; i < 3; ++i) {
      const int line = (lw * 3 + i) * 4 + g;
      const int kq = line / 3, cgrp = line % 3;
      const int k = kq * 4 + ((lane >> 2) & 3);
      const int ch = (lane & 3) ^ (((k >> 3) & 1) << 1);
      boffs[i] = (unsigned)(k * 1536 + cgrp * 512 + ch * 8) * 2u;
    }
    const char* abase = (const char*)(x + (size_t)rt * 128 * D_);
    const char* bbase = (const char*)(wqkv + cg * 32);
    auto stage = [&](int s) {
      const unsigned slot = lbase + (s % K1_NSLOT) * K1_SLOT;
#pragma unroll
      for (int i = 0; i < 4; ++i)
        glds16(abase + s * 128 + i * 8192, (i & 1) ? (aoffs ^ 64u) : aoffs, slot + lw * 4096 + i * 1024);
#pragma unroll
      for (int i = 0; i < 3; ++i)
        glds16(bbase + (size_t)s * 64 * 3072, boffs[i], slot + 16384 + (lw * 3 + i) * 1024);
    };
    stage(0); stage(1);
    wait_vm<7>();
    BARE_BAR();
    stage(2);
#pragma unroll
    for (int s = 0; s < 7; ++s) {
      if (s == 0 || s == 5) wait_vm<7>(); else if (s <= 4) wait_vm<14>(); else wait_vm<0>();
      BARE_BAR();
      if (s == 0) stage(3);
      if (s + 4 < 8) stage(s + 4);
    }
    const int W = blockIdx.x * 4 + lw;
    const int grp = W % 86, kp = (W / 86) & 7;
    u32x4 wv[4];
    {
      const u16* wsrc = (grp < 43 ? wg : wu) + (grp % 43) * 32 + (lane & 3) * 8 + (size_t)(kp * 64 + (lane >> 2)) * F_;
#pragma unroll
      for (int i = 0; i < 4; ++i) wv[i] = *(const u32x4*)(wsrc + (size_t)i * 16 * F_);
    }
    unsigned touch;
    {
      const int T = idx * 256 + lw * 64 + lane;
      const char* tp = (const char*)pq + (size_t)T * 4096;
      if (T >= 3584) tp = (const char*)outp + (size_t)(T - 3584) * 4096;
      if (T >= 4096) tp = (const char*)wo + (size_t)(T - 4096) * 4096;
      if (T >= 4224) tp = (const char*)wd + (size_t)(T - 4224) * 4096;
      if (T >= 4568) tp = (const char*)wg + (size_t)(T - 4568) * 4096;
      if (T >= 4912) tp = (const char*)wu + (size_t)(T - 4912) * 4096;
      if (T >= 5256) tp = (const char*)wu;
      touch = *(const volatile unsigned*)tp;
    }
    BARE_BAR();
    if (W < 86 * 8) {
      float c8[8];
#pragma unroll
      for (int j = 0; j < 4; ++j) {
        c8[2 * j] = (bf_lo(wv[0][j]) + bf_lo(wv[1][j])) + (bf_lo(wv[2][j]) + bf_lo(wv[3][j]));
        c8[2 * j + 1] = (bf_hi(wv[0][j]) + bf_hi(wv[1][j])) + (bf_hi(wv[2][j]) + bf_hi(wv[3][j]));
      }
#pragma unroll
      for (int j = 0; j < 8; ++j) {
        float v = c8[j];
        v += __shfl_xor(v, 4); v += __shfl_xor(v, 8);
        c8[j] = red32(red16(v));
      }
      if (lane < 4) {
        float* dst = csp + kp * 2752 + grp * 32 + lane * 8;
        f32x4 v0 = {c8[0], c8[1], c8[2], c8[3]}, v1 = {c8[4], c8[5], c8[6], c8[7]};
        *(f32x4*)dst = v0; *(f32x4*)(dst + 4) = v1;
      }
    }
    asm volatile("" :: "v"(touch));
    BARE_BAR();
    return;
  }
  const int wr = wave >> 1, wc = wave & 1;
  const int q4 = (lane & 15) >> 2, p = lane & 3;
  unsigned touch0;
  {
    const int t = wave * 64 + lane;
    const int lx = idx * 128 + (t & 127);
    const char* px = (const char*)(x + (size_t)(xcd >> 1) * 512 * D_) + (size_t)lx * 128;
    const int lw_ = idx * 192 + (t < 192 ? t : 0);
    const char* pw = (const char*)wqkv + (size_t)(lw_ / 12) * 3072 + ((lw_ % 12) >> 2) * 1024 + (xcd & 1) * 512 + (lw_ & 3) * 128;
    touch0 = *(const volatile unsigned*)pw;
    if (t < 128) touch0 += *(const volatile unsigned*)px;
    if (t >= 240) touch0 += *(const volatile unsigned*)((const char*)wo + (size_t)(blockIdx.x * 16 + t - 240) * 128);
    if (t >= 128 && t < 128 + 43) {
      const size_t o = (size_t)(blockIdx.x * 43 + t - 128) * 128;
      touch0 += *(const volatile unsigned*)((const char*)wg + o);
      touch0 += *(const volatile unsigned*)((const char*)wu + o);
      touch0 += *(const volatile unsigned*)((const char*)wd + o);
    }
  }
  const int aoff = (wr * 64 + r) * 128 + ((g ^ ((r >> 1) & 7)) << 4);
  const int aoff0 = aoff + wc * 4096, aoff1 = aoff + (wc ^ 1) * 4096;
  const int boff = 16384 + (2 * g) * 768 + q4 * 64 + ((wc ^ (g & 1)) << 5) + p * 8;
  const u32x4 ones = ONES_FRAG;
  f32x4 accq[4] = {}, acck[4] = {}, accv[4] = {};
  f32x4 rs[2] = {}, sqm[2] = {}, cq = {}, ck = {}, cv = {};
  u32x4 fa[2][4], fb[2][3];
  auto rdfrags = [&](int t) {
    const char* slot = smem + ((t >> 1) % K1_NSLOT) * K1_SLOT;
    const int kk = t & 1;
#pragma unroll
    for (int i = 0; i < 4; ++i)
      fa[kk][i] = *(const u32x4*)(slot + (((i < 2 ? aoff0 : aoff1) + (i & 1) * 2048) ^ (kk << 6)));
#pragma unroll
    for (int j = 0; j < 3; ++j)
      fb[kk][j] = tr_frag(slot + boff + kk * 6144 + j * 256, slot + boff + kk * 6144 + 768 + j * 256);
  };
  auto compute = [&](int t) {
    const int kk = t & 1;
    const u32x4* a = fa[kk];
    const u32x4 bq = fb[kk][0], bk = fb[kk][1], bv = fb[kk][2];
#pragma unroll
    for (int i = 0; i < 4; ++i) {
      accq[i] = mfma16(bq, a[i], accq[i]);
      acck[i] = mfma16(a[i], bk, acck[i]);
      accv[i] = mfma16(a[i], bv, accv[i]);
    }
    rs[0] = mfma16(ones, a[0], rs[0]);
    rs[1] = mfma16(ones, a[1], rs[1]);
    sqm[0] = mfma16(a[0], a[0], sqm[0]);
    sqm[1] = mfma16(a[1], a[1], sqm[1]);
    if (kk == wr) {
      cq = mfma16(bq, ones, cq);
      ck = mfma16(ones, bk, ck);
      cv = mfma16(ones, bv, cv);
    }
  };
  BARE_BAR();
  STAMP(0,2);
  rdfrags(0);
#pragma unroll
  for (int s = 0; s < 8; ++s) {
    rdfrags(2 * s + 1);
    compute(2 * s);
    __builtin_amdgcn_sched_barrier(0);
    if (s + 1 < 8) {
      RAW_BAR();
      STAMP(0,3+s);
      rdfrags(2 * s + 2);
    }
    compute(2 * s + 1);
    __builtin_amdgcn_sched_barrier(0);
  }
  STAMP(0,11);
  float* st = (float*)(smem);
  float* csl = (float*)(smem + 2048);
  float* st2 = (float*)(smem + 4096);
  float* red = (float*)(smem + 8192);
  {
#pragma unroll
    for (int i = 0; i < 2; ++i) {
      const float q01 = (r & 1) ? sqm[i][1] : sqm[i][0], q23 = (r & 1) ? sqm[i][3] : sqm[i][2];
      const float q = (r & 2) ? q23 : q01;
      if (g == (r >> 2)) {
        f32x2 v = {rs[i][0], q};
        *(f32x2*)(st + (wr * 64 + wc * 32 + i * 16 + r) * 2) = v;
      }
    }
    float* c = csl + (wr * 2 + wc) * 48;
    if (r == 0) *(f32x4*)(c + 4 * g) = cq;
    if (g == 0) { c[16 + r] = ck[0]; c[32 + r] = cv[0]; }
  }
  __syncthreads();
  STAMP(0,12);
  {
    const f32x2 a0 = *(const f32x2*)(st + (wr * 64 + lane) * 2);
    const float mean = a0[0] * (1.f / 512.f);
    const float var = fmaxf(a0[1] * (1.f / 512.f) - mean * mean, 0.f);
    f32x2 v = {mean, __builtin_amdgcn_rsqf(var + 1e-5f)};
    *(f32x2*)(st2 + (wave * 64 + lane) * 2) = v;
  }
  const f32x4 csq4 = *(const f32x4*)(csl + wc * 48 + 4 * g) + *(const f32x4*)(csl + (2 + wc) * 48 + 4 * g);
  const float csk = csl[wc * 48 + 16 + r] + csl[(2 + wc) * 48 + 16 + r];
  const float csv = csl[wc * 48 + 32 + r] + csl[(2 + wc) * 48 + 32 + r];
  f32x4 kv = {}, zz = {};
#pragma unroll
  for (int ih = 0; ih < 2; ++ih) {
    u32x4 pk, vv;
#pragma unroll
    for (int i2 = 0; i2 < 2; ++i2) {
      const int i = ih * 2 + i2;
      const int mbl = ((ih ^ wc) << 1) + i2;
      {
        const f32x2 mi = *(const f32x2*)(st2 + (wave * 64 + mbl * 16 + r) * 2);
        float qv[4];
#pragma unroll
        for (int j = 0; j < 4; ++j) qv[j] = phi_f((accq[i][j] - mi[0] * csq4[j]) * mi[1]);
        u32x2 o = {pack_bf(qv[0], qv[1]), pack_bf(qv[2], qv[3])};
        const int mblk = rt * 8 + wr * 4 + mbl;
        *(u32x2*)(pq + ((size_t)(mblk * 16 + cg) * 64 + r + 16 * (2 * wc + (g >> 1))) * 8 + (g & 1) * 4) = o;
      }
      {
        const f32x4 m0 = *(const f32x4*)(st2 + (wave * 64 + mbl * 16 + 4 * g) * 2);
        const f32x4 m1 = *(const f32x4*)(st2 + (wave * 64 + mbl * 16 + 4 * g) * 2 + 4);
        const float mean[4] = {m0[0], m0[2], m1[0], m1[2]}, inv[4] = {m0[1], m0[3], m1[1], m1[3]};
        float kk[4], vj[4];
#pragma unroll
        for (int j = 0; j < 4; ++j) {
          kk[j] = phi_f((acck[i][j] - mean[j] * csk) * inv[j]);
          vj[j] = (accv[i][j] - mean[j] * csv) * inv[j];
        }
        pk[2 * i2] = pack_bf(kk[0], kk[1]);
        pk[2 * i2 + 1] = pack_bf(kk[2], kk[3]);
        vv[2 * i2] = pack_bf(vj[0], vj[1]);
        vv[2 * i2 + 1] = pack_bf(vj[2], vj[3]);
      }
    }
    kv = mfma16(pk, vv, kv);
    zz = mfma16(pk, ones, zz);
  }
  {
    float* rd = red + (wc * 2 + wr) * 272;
#pragma unroll
    for (int j = 0; j < 4; ++j) rd[(4 * g + j) * 16 + r] = kv[j];
    if (r == 0) *(f32x4*)(rd + 256 + 4 * g) = zz;
  }
  __syncthreads();
  for (int i = tid; i < 544; i += 256) {
    const int hh = i >= 272 ? 1 : 0, e = i - hh * 272;
    const float* rr = red + hh * 2 * 272 + e;
    part[(size_t)((cg * 2 + hh) * 16 + rt) * 272 + e] = rr[0] + rr[272];
  }
  asm volatile("" :: "v"(touch0));
  STAMP(0,13);
}

typedef __attribute__((address_space(1))) unsigned gu32;
DI void poll_ge(unsigned* c, unsigned target) {
  for (unsigned spins = 0; spins < (1u << 17); ++spins) {
    const unsigned v = __builtin_amdgcn_readfirstlane(
        __hip_atomic_load((gu32*)c, __ATOMIC_RELAXED, __HIP_MEMORY_SCOPE_AGENT));
    if (v >= target) break;
    __builtin_amdgcn_s_sleep(1);
  }
  asm volatile("" ::: "memory");
}
DI void glds16_sc1(const void* sbase, unsigned voff, unsigned dst) {
  asm volatile("s_mov_b32 m0, %2\n\ts_nop 0\n\tglobal_load_lds_dwordx4 %0, %1 sc1"
               :: "v"(voff), "s"(sbase), "s"(dst) : "memory");
}
DI void glds16v(const char* gsrc, unsigned dst) {
  asm volatile("s_mov_b32 m0, %1\n\ts_nop 0\n\tglobal_load_lds_dwordx4 %0, off" :: "v"(gsrc), "s"(dst) : "memory");
}
constexpr int K3_SLOT = 16384 + 24576, K3_NSLOT = 4;

__global__ __launch_bounds__(512) void kb_rest(const u16* __restrict__ x, const u16* __restrict__ pq,
                                               const float* __restrict__ part, const u16* __restrict__ wo,
                                               u16* wp, unsigned* cnt, u16* hf, float* hst,
                                               const float* __restrict__ csp,
                                               const u16* __restrict__ wg, const u16* __restrict__ wu,
                                               const u16* __restrict__ wd, u16* hhf, u16* __restrict__ out) {
  __shared__ __attribute__((aligned(16))) char smem[K3_SLOT * K3_NSLOT];
  const int tid = threadIdx.x, lane = tid & 63;
  const int wave = __builtin_amdgcn_readfirstlane(tid >> 6);
  const int xcd = blockIdx.x & 7, idx = blockIdx.x >> 3;
  const int r = lane & 15, g = lane >> 4;
  const int q4 = (lane & 15) >> 2, p = lane & 3;
  const __amdgpu_buffer_rsrc_t hrs = __builtin_amdgcn_make_buffer_rsrc(hf, 0, S_ * D_ * 2, 0x00020000);
  const __amdgpu_buffer_rsrc_t srs = __builtin_amdgcn_make_buffer_rsrc(hst, 0, S_ * 32 * 4 + 16 * 2752 * 4, 0x00020000);
  unsigned* cnt_w = cnt;
  unsigned* cnt_h = cnt + 8 * 64;
  unsigned* cnt_hh = cnt + 24 * 64;
  STAMP(2,0);
  {
    const int rt = xcd * 4 + (idx >> 3);
    const int ct = idx & 7;
    const int kg = wave >> 2, rb = wave & 3;
    const int mblk = rt * 4 + rb;
    const __amdgpu_buffer_rsrc_t wrs = __builtin_amdgcn_make_buffer_rsrc(wp, 0, 524288 + 8192, 0x00020000);
    const int ph = blockIdx.x >> 3, pc = blockIdx.x & 7;
    float* kv = (float*)(smem + 65536 + 16384);
    float pv[16];
    if (tid < 272) {
#pragma unroll
      for (int pp = 0; pp < 16; ++pp) pv[pp] = part[(size_t)(ph * 16 + pp) * 272 + tid];
    }
    float w[16];
    if (tid < 256) {
#pragma unroll
      for (int e = 0; e < 16; ++e) w[e] = bf_lo(wo[(size_t)(ph * 16 + e) * D_ + pc * 64 + (tid & 63)]);
    }
    if (tid < 272) {
      float s = 0.f;
#pragma unroll
      for (int pp = 0; pp < 16; ++pp) s += pv[pp];
      kv[tid] = s;
    }
    __syncthreads();
    STAMP(2,1);
    if (tid < 256) {
      const int c = tid & 63, dq = tid >> 6;
      float o[4];
#pragma unroll
      for (int j = 0; j < 4; ++j) {
        float s = 0.f;
#pragma unroll
        for (int e4 = 0; e4 < 4; ++e4) {
          const f32x4 k4 = *(const f32x4*)(kv + (dq * 4 + j) * 16 + e4 * 4);
          s = fmaf(k4[0], w[e4 * 4], s); s = fmaf(k4[1], w[e4 * 4 + 1], s);
          s = fmaf(k4[2], w[e4 * 4 + 2], s); s = fmaf(k4[3], w[e4 * 4 + 3], s);
        }
        o[j] = s;
      }
      const int n = pc * 64 + c, nb = n >> 4, cc = n & 15;
      const int ks = ph >> 1, gg = (ph & 1) * 2 + (dq >> 1);
      u32x2 v = {pack_bf(o[0], o[1]), pack_bf(o[2], o[3])};
      __builtin_amdgcn_raw_buffer_store_b64(v, wrs, (unsigned)((nb * 16 + ks) * 64 + gg * 16 + cc) * 16u + (dq & 1) * 8u, 0, 16);
    } else if (tid < 258) {
      const float* zs = kv + 256 + (tid - 256) * 8;
      u32x4 v = {pack_bf(zs[0], zs[1]), pack_bf(zs[2], zs[3]), pack_bf(zs[4], zs[5]), pack_bf(zs[6], zs[7])};
      __builtin_amdgcn_raw_buffer_store_b128(v, wrs, 524288u + (unsigned)(pc * 512 + ph * 16 + (tid - 256) * 8) * 2u, 0, 16);
    }
    WAIT_VM(0);
    __syncthreads();
    if (tid == 0) __hip_atomic_fetch_add((gu32*)(cnt_w + pc * 64), 1u, __ATOMIC_RELAXED, __HIP_MEMORY_SCOPE_AGENT);
    STAMP(2,2);
    float cpart[8];
    const int ccol = ((rt & 1) * 8 + ct) * 172 + (tid - 272);
    const bool do_cs = tid >= 272 && tid < 444;
    if (do_cs) {
#pragma unroll
      for (int kp = 0; kp < 8; ++kp) cpart[kp] = csp[kp * 2752 + ccol];
    }
    u32x4 a[8];
#pragma unroll
    for (int i = 0; i < 8; ++i) a[i] = *(const u32x4*)(pq + ((size_t)(mblk * 16 + kg * 8 + i) * 64 + lane) * 8);
    u32x2 xr[2];
#pragma unroll
    for (int j = 0; j < 2; ++j)
      xr[j] = *(const u32x2*)(x + (size_t)(mblk * 16 + r) * D_ + ct * 64 + (kg * 2 + j) * 16 + g * 4);
    if (do_cs) {
      const float c = ((cpart[0] + cpart[1]) + (cpart[2] + cpart[3])) + ((cpart[4] + cpart[5]) + (cpart[6] + cpart[7]));
      __builtin_amdgcn_raw_buffer_store_b32(__float_as_uint(c), srs, (unsigned)(S_ * 32 * 4 + ((rt >> 1) * 2752 + ccol) * 4), 0, 16);
    }
    if (wave == 0) poll_ge(cnt_w + ct * 64, 32u);
    __syncthreads();
    STAMP(2,3);
    u32x4 zf[8], wv[8];
#pragma unroll
    for (int i = 0; i < 8; ++i)
      zf[i] = __builtin_amdgcn_raw_buffer_load_b128(wrs, 524288u + (unsigned)(ct * 512 + (kg * 8 + i) * 32 + g * 8) * 2u, 0, 16);
#pragma unroll
    for (int i = 0; i < 8; ++i)
      wv[i] = __builtin_amdgcn_raw_buffer_load_b128(wrs, (unsigned)(ct * 65536 + (wave * 8 + i) * 1024 + lane * 16), 0, 16);
    unsigned touch = 0;
    {
      asm volatile("" ::: "memory");
    }
#pragma unroll
    for (int i = 0; i < 8; ++i) {
      float s = dot2(a[i][0], zf[i][0], 0.f);
      s = dot2(a[i][1], zf[i][1], s);
      s = dot2(a[i][2], zf[i][2], s);
      s = dot2(a[i][3], zf[i][3], s);
      const float inv = __builtin_amdgcn_rcpf(red16(s) + 1e-6f);
#pragma unroll
      for (int j = 0; j < 4; ++j) a[i][j] = pack_bf(bf_lo(a[i][j]) * inv, bf_hi(a[i][j]) * inv);
    }
#pragma unroll
    for (int i = 0; i < 8; ++i) *(u32x4*)(smem + (wave * 8 + i) * 1024 + lane * 16) = wv[i];
    __syncthreads();
    STAMP(2,4);
    f32x4 acc[4] = {};
#pragma unroll
    for (int i = 0; i < 8; ++i) {
#pragma unroll
      for (int nb = 0; nb < 4; ++nb) {
        const u32x4 b = *(const u32x4*)(smem + (nb * 16 + kg * 8 + i) * 1024 + lane * 16);
        acc[nb] = mfma16(b, a[i], acc[nb]);
      }
    }
    f32x4* xch = (f32x4*)(smem + 65536);
#pragma unroll
    for (int j = 0; j < 2; ++j) xch[(wave * 2 + j) * 64 + lane] = kg ? acc[j] : acc[2 + j];
    __syncthreads();
    float hs = 0.f, hq = 0.f;
    u32x2 oo[2];
#pragma unroll
    for (int j = 0; j < 2; ++j) {
      const f32x4 o4 = (kg ? acc[2 + j] : acc[j]) + xch[((wave ^ 4) * 2 + j) * 64 + lane];
      u32x2 o;
      o[0] = pack_bf(bf_lo(xr[j][0]) + o4[0], bf_hi(xr[j][0]) + o4[1]);
      o[1] = pack_bf(bf_lo(xr[j][1]) + o4[2], bf_hi(xr[j][1]) + o4[3]);
      oo[j] = o;
      hs = dot2(o[0], 0x3f803f80u, hs); hs = dot2(o[1], 0x3f803f80u, hs);
      hq = dot2(o[0], o[0], hq); hq = dot2(o[1], o[1], hq);
    }
    __builtin_amdgcn_raw_buffer_store_b128(
        merge16(oo[0], oo[1]), hrs, (unsigned)(((mblk * 16 + ct * 2 + kg) * 64 + r + 16 * (2 * (g & 1) + (g >> 1))) * 16), 0, 16);
    hs = red32(red16(hs)); hq = red32(red16(hq));
    if (g == 0) {
      u32x2 v = {__float_as_uint(hs), __float_as_uint(hq)};
      __builtin_amdgcn_raw_buffer_store_b64(v, srs, (unsigned)(((ct * 2 + kg) * S_ + mblk * 16 + r) * 8), 0, 16);
    }
    asm volatile("" :: "v"(touch));
    WAIT_VM(0);
    __syncthreads();
    if (tid == 0) __hip_atomic_fetch_add((gu32*)(cnt_h + (rt >> 1) * 64), 1u, __ATOMIC_RELAXED, __HIP_MEMORY_SCOPE_AGENT);
    STAMP(2,5);
  }
  const int rt3 = (xcd >> 1) * 4 + (idx >> 3);
  const int cg = (xcd & 1) * 8 + (idx & 7);
  const int f0 = cg < 11 ? cg * 96 : 1056 + (cg - 11) * 64;
  const int nvalid = cg < 11 ? 6 : 4;
  const int rt4 = xcd * 4 + (idx >> 3);
  const int ct4 = idx & 7;
  if (wave >= 4) {
    const int lw = wave - 4;
    const unsigned lbase = lds_addr(smem);
    {
      const char* abase = (const char*)(hf + (size_t)((rt3 * 8 + lw * 2) * 16) * 512);
      const long wdelta = (const char*)wu - (const char*)wg;
      const char* bsrc[6];
#pragma unroll
      for (int i = 0; i < 6; ++i) {
        const int line = (lw * 6 + i) * 4 + g;
        const int kq = line / 6, cgrp = line % 6;
        const int k = kq * 4 + ((lane >> 2) & 3);
        const int ch = (lane & 3) ^ (((k >> 3) & 1) << 1);
        int c3 = cgrp < 3 ? cgrp : cgrp - 3;
        c3 = (c3 * 2 < nvalid) ? c3 : 0;
        bsrc[i] = (const char*)(wg + f0) + (unsigned)(k * F_ + c3 * 32 + ch * 8) * 2u + (cgrp >= 3 ? wdelta : 0);
      }
      auto stA = [&](int s) {
        const unsigned slot = lbase + (s % K3_NSLOT) * K3_SLOT;
#pragma unroll
        for (int i = 0; i < 4; ++i)
          glds16_sc1(abase + (i >> 1) * 16384 + (s * 2 + (i & 1)) * 1024, lane * 16, slot + (lw * 4 + i) * 1024);
      };
      auto stB = [&](int s) {
        const unsigned slot = lbase + (s % K3_NSLOT) * K3_SLOT;
#pragma unroll
        for (int i = 0; i < 6; ++i) glds16v(bsrc[i] + (size_t)s * 64 * F_ * 2, slot + 16384 + (lw * 6 + i) * 1024);
      };
      stB(0); stB(1); stB(2); stB(3);
      poll_ge(cnt_h + rt3 * 64, 16u);
      stA(0); stA(1); stA(2); stA(3);
      wait_vm<12>(); BARE_BAR();
      wait_vm<8>();  BARE_BAR(); stA(4); stB(4);
      wait_vm<14>(); BARE_BAR(); stA(5); stB(5);
      wait_vm<20>(); BARE_BAR(); stA(6); stB(6);
      wait_vm<20>(); BARE_BAR(); stA(7); stB(7);
      wait_vm<20>(); BARE_BAR();
      wait_vm<10>(); BARE_BAR();
      wait_vm<0>();  BARE_BAR();
    }
    {
      const char* abase = (const char*)(hhf + (size_t)((rt4 * 4 + lw) * KS4) * 512);
      const char* bbase = (const char*)(wd + (size_t)lw * 32 * D_ + ct4 * 64);
      const unsigned boffs = (unsigned)(((g >> 1) * 4 + ((lane >> 2) & 3)) * D_ + (g & 1) * 32 + (lane & 3) * 8) * 2u;
      auto stA = [&](int s) {
        const unsigned slot = lbase + (s & 3) * 32768;
#pragma unroll
        for (int i = 0; i < 4; ++i)
          glds16_sc1(abase + (s * 4 + ((s == 10 && i == 3) ? 2 : i)) * 1024, lane * 16, slot + lw * 4096 + i * 1024);
      };
      auto stB = [&](int s) {
        const unsigned slot = lbase + (s & 3) * 32768;
        const int so = (s == 10 && lw == 3) ? s - 1 : s;
#pragma unroll
        for (int i = 0; i < 4; ++i)
          glds16(bbase + (size_t)(so * 128 + i * 8) * D_ * 2, (i & 1) ? (boffs ^ 32u) : boffs, slot + 16384 + lw * 4096 + i * 1024);
      };
      stB(0); stB(1); stB(2);
      BARE_BAR();
      stB(3);
      poll_ge(cnt_hh + (rt4 >> 1) * 64, 16u);
      stA(0); stA(1); stA(2); stA(3);
      wait_vm<12>(); BARE_BAR();
      wait_vm<8>();  BARE_BAR(); stA(4); stB(4);
      wait_vm<12>(); BARE_BAR(); stA(5); stB(5);
#pragma unroll
      for (int s = 2; s < 10; ++s) {
        if (s <= 7) wait_vm<16>(); else if (s == 8) wait_vm<8>(); else wait_vm<0>();
        BARE_BAR();
        if (s + 4 < 11) { stA(s + 4); stB(s + 4); }
      }
    }
    return;
  }
  const int wr = wave >> 1, wc = wave & 1;
  {
    const int aoff = wr * 8192 + lane * 16;
    const int bbase2 = 16384 + (2 * g) * 1536 + q4 * 64 + p * 8;
    int boff[3];
#pragma unroll
    for (int j = 0; j < 3; ++j) {
      const int fbl = 3 * wc + j;
      boff[j] = bbase2 + (fbl >> 1) * 256 + (((fbl & 1) ^ (g & 1)) << 5);
    }
    u32x2 hp[16];
    f32x4 cg4[3], cu4[3];
    float mean_l = 0.f, inv2_l = 0.f;
    f32x4 accg[4][3] = {}, accu[4][3] = {};
    u32x4 fa[2][4], fb[2][6];
    auto rdfrags = [&](int t) {
      const char* slot = smem + ((t >> 1) % K3_NSLOT) * K3_SLOT;
      const int kk = t & 1;
#pragma unroll
      for (int i = 0; i < 4; ++i) fa[kk][i] = *(const u32x4*)(slot + aoff + i * 2048 + kk * 1024);
#pragma unroll
      for (int j = 0; j < 3; ++j) {
        fb[kk][j] = tr_frag(slot + boff[j] + kk * 12288, slot + boff[j] + kk * 12288 + 1536);
        fb[kk][3 + j] = tr_frag(slot + boff[j] + 768 + kk * 12288, slot + boff[j] + 768 + kk * 12288 + 1536);
      }
    };
    auto compute = [&](int t) {
      const int kk = t & 1;
      const u32x4* a = fa[kk];
#pragma unroll
      for (int j = 0; j < 3; ++j) {
        const u32x4 bg = fb[kk][j], bu = fb[kk][3 + j];
#pragma unroll
        for (int i = 0; i < 4; ++i) {
          accg[i][j] = mfma16(bg, a[i], accg[i][j]);
          accu[i][j] = mfma16(bu, a[i], accu[i][j]);
        }
      }
    };
    BARE_BAR();
    STAMP(3,0);
    rdfrags(0);
#pragma unroll
    for (int s = 0; s < 8; ++s) {
      rdfrags(2 * s + 1);
      compute(2 * s);
      __builtin_amdgcn_sched_barrier(0);
      if (s + 1 < 8) {
        RAW_BAR();
        STAMP(3,1+s);
        rdfrags(2 * s + 2);
      }
      if (s == 4) {
#pragma unroll
        for (int i = 0; i < 16; ++i)
          hp[i] = __builtin_amdgcn_raw_buffer_load_b64(srs, (unsigned)((i * S_ + rt3 * 128 + wr * 64 + lane) * 8), 0, 16);
      }
      if (s == 6) {
        f32x2 t2 = {};
#pragma unroll
        for (int i = 0; i < 16; ++i) t2 += __builtin_bit_cast(f32x2, hp[i]);
        mean_l = t2[0] * (1.f / 512.f);
        const float var = fmaxf(t2[1] * (1.f / 512.f) - mean_l * mean_l, 0.f);
        inv2_l = __builtin_amdgcn_rcpf(var + 1e-5f);
        asm volatile("" : "+v"(mean_l), "+v"(inv2_l));
#pragma unroll
        for (int j = 0; j < 3; ++j) {
          const int fbl = (3 * wc + j) < nvalid ? (3 * wc + j) : 0;
          const unsigned co = (unsigned)(S_ * 32 * 4 + (rt3 * 2752 + f0 + fbl * 16 + 4 * g) * 4);
          cg4[j] = __builtin_bit_cast(f32x4, __builtin_amdgcn_raw_buffer_load_b128(srs, co, 0, 16));
          cu4[j] = __builtin_bit_cast(f32x4, __builtin_amdgcn_raw_buffer_load_b128(srs, co + F_ * 4, 0, 16));
        }
      }
      compute(2 * s + 1);
      __builtin_amdgcn_sched_barrier(0);
    }
    STAMP(3,9);
    const __amdgpu_buffer_rsrc_t hhrs = __builtin_amdgcn_make_buffer_rsrc(hhf, 0, 128 * KS4 * 1024, 0x00020000);
#pragma unroll
    for (int ip = 0; ip < 2; ++ip) {
      float mean[2], inv2[2];
#pragma unroll
      for (int i2 = 0; i2 < 2; ++i2) {
        mean[i2] = __shfl(mean_l, (ip * 2 + i2) * 16 + r);
        inv2[i2] = __shfl(inv2_l, (ip * 2 + i2) * 16 + r);
      }
      const int mblk = rt3 * 8 + wr * 4 + ip * 2 + (g & 1);
#pragma unroll
      for (int j = 0; j < 3; ++j) {
        const int fbl = 3 * wc + j;
        u32x2 o[2];
#pragma unroll
        for (int i2 = 0; i2 < 2; ++i2) {
          const int i = ip * 2 + i2;
          float hv[4];
#pragma unroll
          for (int e = 0; e < 4; ++e)
            hv[e] = ((accg[i][j][e] - mean[i2] * cg4[j][e]) * inv2[i2]) * (accu[i][j][e] - mean[i2] * cu4[j][e]);
          o[i2][0] = pack_bf(hv[0], hv[1]); o[i2][1] = pack_bf(hv[2], hv[3]);
        }
        const u32x4 v = merge16(o[0], o[1]);
        if (fbl < nvalid) {
          const int fb_ = (f0 >> 4) + fbl;
          __builtin_amdgcn_raw_buffer_store_b128(
              v, hhrs, (unsigned)(((mblk * KS4 + (fb_ >> 1)) * 64 + r + 16 * (2 * (fb_ & 1) + (g >> 1))) * 16), 0, 16);
        }
      }
    }
    WAIT_VM(0);
    RAW_BAR();
    if (tid == 0) __hip_atomic_fetch_add((gu32*)(cnt_hh + rt3 * 64), 1u, __ATOMIC_RELAXED, __HIP_MEMORY_SCOPE_AGENT);
    STAMP(3,10);
  }
  {
    const int boff = 16384 + (2 * g) * 512 + wc * 256 + q4 * 64 + p * 8;
    const int aoff = wr * 8192 + lane * 16;
    u32x2 hr[2][2];
    f32x4 acc[2][2] = {};
    u32x4 fa[2][2][2], fb[2][2][2];
    auto rdfrags = [&](int t) {
      const char* slot = smem + ((t >> 1) & 3) * 32768;
#pragma unroll
      for (int kk = 0; kk < 2; ++kk) {
        const int ksl = (t & 1) * 2 + kk;
#pragma unroll
        for (int mb = 0; mb < 2; ++mb) fa[t & 1][kk][mb] = *(const u32x4*)(slot + aoff + mb * 4096 + ksl * 1024);
#pragma unroll
        for (int nb = 0; nb < 2; ++nb) {
          const int ho = (nb ^ (g & 1)) << 5;
          fb[t & 1][kk][nb] = tr_frag(slot + boff + ksl * 4096 + ho, slot + boff + ksl * 4096 + 512 + ho);
        }
      }
    };
    auto compute = [&](int t) {
#pragma unroll
      for (int kk = 0; kk < 2; ++kk) {
        if (2 * t + kk < KS4) {
#pragma unroll
          for (int mb = 0; mb < 2; ++mb)
#pragma unroll
            for (int nb = 0; nb < 2; ++nb) acc[mb][nb] = mfma16(fb[t & 1][kk][nb], fa[t & 1][kk][mb], acc[mb][nb]);
        }
      }
    };
    BARE_BAR();
    STAMP(4,0);
    rdfrags(0);
#pragma unroll
    for (int mb = 0; mb < 2; ++mb)
#pragma unroll
      for (int nb = 0; nb < 2; ++nb)
        hr[mb][nb] = __builtin_amdgcn_raw_buffer_load_b64(
            hrs, (unsigned)((((rt4 * 4 + wr * 2 + mb) * 16 + ct4 * 2 + wc) * 64 + r + 16 * (2 * nb + (g >> 1))) * 16 + (g & 1) * 8), 0, 16);
#pragma unroll
    for (int s = 0; s < 11; ++s) {
      rdfrags(2 * s + 1);
      compute(2 * s);
      if (s + 1 < 11) {
        RAW_BAR();
        STAMP(4,1+s);
        rdfrags(2 * s + 2);
      }
      compute(2 * s + 1);
    }
#pragma unroll
    for (int mb = 0; mb < 2; ++mb)
#pragma unroll
      for (int nb = 0; nb < 2; ++nb) {
        const f32x4 o4 = acc[mb][nb];
        u32x2 o;
        o[0] = pack_bf(bf_lo(hr[mb][nb][0]) + o4[0], bf_hi(hr[mb][nb][0]) + o4[1]);
        o[1] = pack_bf(bf_lo(hr[mb][nb][1]) + o4[2], bf_hi(hr[mb][nb][1]) + o4[3]);
        *(u32x2*)(out + (size_t)(rt4 * 64 + wr * 32 + mb * 16 + r) * D_ + ct4 * 64 + wc * 32 + nb * 16 + g * 4) = o;
      }
    STAMP(4,14);
  }
}

extern "C" void kernel_launch(void* const* d_in, const int* in_sizes, int n_in, void* d_out, int out_size,
                              void* d_ws, size_t ws_size, hipStream_t stream) {
  const u16* x = (const u16*)d_in[0];
  const u16* wqkv = (const u16*)d_in[1];
  const u16* wo = (const u16*)d_in[2];
  const u16* wg = (const u16*)d_in[4];
  const u16* wu = (const u16*)d_in[5];
  const u16* wd = (const u16*)d_in[6];
  u16* out = (u16*)d_out;
  char* ws = (char*)d_ws;
  u16* pq = (u16*)(ws);
  u16* hf = (u16*)(ws + (2u << 20));
  u16* wp = (u16*)(ws + (4u << 20));
  float* part = (float*)(ws + (5u << 20));
  unsigned* cnt = (unsigned*)(ws + (6u << 20));
  float* csp = (float*)(ws + (6u << 20) + 65536);
  float* hst = (float*)(ws + (7u << 20));
  u16* hhf = (u16*)(ws + (8u << 20));
  k1_qkv<<<256, 512, 0, stream>>>(x, wqkv, pq, part, wg, wu, csp, cnt, wo, wd, out);
  kb_rest<<<256, 512, 0, stream>>>(x, pq, part, wo, wp, cnt, hf, hst, csp, wg, wu, wd, hhf, out);
}
```

```cpp
#include <hip/hip_runtime.h>
#include <hip/hip_bf16.h>
#include <stdint.h>

typedef unsigned short u16;
typedef __attribute__((ext_vector_type(8))) __bf16 bf16x8;
typedef __attribute__((ext_vector_type(2))) __bf16 bf16x2;
typedef __attribute__((ext_vector_type(4))) short s16x4;
typedef __attribute__((ext_vector_type(4))) float f32x4;
typedef __attribute__((ext_vector_type(2))) float f32x2;
typedef __attribute__((ext_vector_type(4))) unsigned u32x4;
typedef __attribute__((ext_vector_type(2))) unsigned u32x2;
typedef __attribute__((address_space(3))) s16x4* lds_s16x4_ptr;

#define DI __device__ __forceinline__
#define STAMP(kid,i)

constexpr int S_ = 2048, D_ = 512, F_ = 1376, H_ = 32;
constexpr int KS4 = F_ / 32;

DI float bf_lo(unsigned u) { return __uint_as_float(u << 16); }
DI float bf_hi(unsigned u) { return __uint_as_float(u & 0xffff0000u); }
DI unsigned pack_bf(float lo, float hi) {
  f32x2 f = {lo, hi};
  bf16x2 r = __builtin_convertvector(f, bf16x2);
  return __builtin_bit_cast(unsigned, r);
}
DI float bfr(float f) { return bf_lo(pack_bf(f, 0.f) & 0xffffu); }
DI f32x4 mfma16(u32x4 a, u32x4 b, f32x4 c) {
  return __builtin_amdgcn_mfma_f32_16x16x32_bf16(__builtin_bit_cast(bf16x8, a),
                                                 __builtin_bit_cast(bf16x8, b), c, 0, 0, 0);
}
DI float dot2(unsigned a, unsigned b, float c) {
  return __builtin_amdgcn_fdot2_f32_bf16(__builtin_bit_cast(bf16x2, a), __builtin_bit_cast(bf16x2, b), c, false);
}
DI u32x4 tr_frag(const char* lds0, const char* lds1) {
  s16x4 lo = __builtin_amdgcn_ds_read_tr16_b64_v4i16((lds_s16x4_ptr)lds0);
  s16x4 hi = __builtin_amdgcn_ds_read_tr16_b64_v4i16((lds_s16x4_ptr)lds1);
  u32x2 l2 = __builtin_bit_cast(u32x2, lo), h2 = __builtin_bit_cast(u32x2, hi);
  u32x4 r = {l2[0], l2[1], h2[0], h2[1]};
  return r;
}
DI unsigned lds_addr(char* l) { return (unsigned)(size_t)(__attribute__((address_space(3))) char*)l; }
DI void glds16(const void* sbase, unsigned voff, unsigned dst) {
  asm volatile("s_mov_b32 m0, %2\n\ts_nop 0\n\tglobal_load_lds_dwordx4 %0, %1"
               :: "v"(voff), "s"(sbase), "s"(dst) : "memory");
}
DI float red16(float s) {
  const unsigned u = __float_as_uint(s);
  auto r = __builtin_amdgcn_permlane16_swap(u, u, false, false);
  return __uint_as_float(r[0]) + __uint_as_float(r[1]);
}
DI float red32(float s) {
  const unsigned u = __float_as_uint(s);
  auto r = __builtin_amdgcn_permlane32_swap(u, u, false, false);
  return __uint_as_float(r[0]) + __uint_as_float(r[1]);
}
DI u32x4 merge16(u32x2 t0, u32x2 t1) {
  auto x = __builtin_amdgcn_permlane16_swap(t0[0], t1[0], false, false);
  auto y = __builtin_amdgcn_permlane16_swap(t0[1], t1[1], false, false);
  u32x4 v = {x[0], y[0], x[1], y[1]};
  return v;
}
#define WAIT_VM(n) asm volatile("s_waitcnt vmcnt(" #n ")" ::: "memory")
#define RAW_BAR() do { asm volatile("s_waitcnt lgkmcnt(0)" ::: "memory"); __builtin_amdgcn_s_barrier(); } while (0)
#define BARE_BAR() do { asm volatile("" ::: "memory"); __builtin_amdgcn_s_barrier(); } while (0)
template <int N> DI void wait_vm() { asm volatile("s_waitcnt vmcnt(%0)" ::"i"(N) : "memory"); }

DI float phi_f(float t) { return (t > 0.f ? t + 1.f : __expf(t)) + 1e-6f; }
DI void sq_frag(const u32x4& a, float& q) {
#pragma unroll
  for (int i = 0; i < 4; ++i) q = dot2(a[i], a[i], q);
}
#define ONES_FRAG ((u32x4){0x3f803f80u, 0x3f803f80u, 0x3f803f80u, 0x3f803f80u})

constexpr int K1_SLOT = 16384 + 12288, K1_NSLOT = 4;
__global__ __launch_bounds__(512) void k1_qkv(const u16* __restrict__ x, const u16* __restrict__ wqkv,
                                              u16* __restrict__ pq, float* __restrict__ part,
                                              const u16* __restrict__ wg, const u16* __restrict__ wu,
                                              float* __restrict__ csp, unsigned* __restrict__ cnt,
                                              const u16* __restrict__ wo, const u16* __restrict__ wd,
                                              const u16* outp) {
  __shared__ __attribute__((aligned(16))) char smem[K1_SLOT * K1_NSLOT];
  const int tid = threadIdx.x, lane = tid & 63;
  const int wave = __builtin_amdgcn_readfirstlane(tid >> 6);
  const int xcd = blockIdx.x & 7, idx = blockIdx.x >> 3;
  const int rt = (xcd >> 1) * 4 + (idx >> 3);
  const int cg = (xcd & 1) * 8 + (idx & 7);
  const int r = lane & 15, g = lane >> 4;
  STAMP(0,0);
  if (blockIdx.x == 0 && tid < 40) cnt[tid * 64] = 0u;
  if (wave >= 4) {
    const int lw = wave - 4;
    const unsigned lbase = lds_addr(smem);
    const unsigned aoffs = (unsigned)(lw * 32 + (lane >> 3)) * 1024u + (((lane & 7) ^ ((lane >> 4) & 3)) << 4);
    unsigned boffs[3];
#pragma unroll
    for (int i = 0; i < 3; ++i) {
      const int line = (lw * 3 + i) * 4 + g;
      const int kq = line / 3, cgrp = line % 3;
      const int k = kq * 4 + ((lane >> 2) & 3);
      const int ch = (lane & 3) ^ (((k >> 3) & 1) << 1);
      boffs[i] = (unsigned)(k * 1536 + cgrp * 512 + ch * 8) * 2u;
    }
    const char* abase = (const char*)(x + (size_t)rt * 128 * D_);
    const char* bbase = (const char*)(wqkv + cg * 32);
    auto stage = [&](int s) {
      const unsigned slot = lbase + (s % K1_NSLOT) * K1_SLOT;
#pragma unroll
      for (int i = 0; i < 4; ++i)
        glds16(abase + s * 128 + i * 8192, (i & 1) ? (aoffs ^ 64u) : aoffs, slot + lw * 4096 + i * 1024);
#pragma unroll
      for (int i = 0; i < 3; ++i)
        glds16(bbase + (size_t)s * 64 * 3072, boffs[i], slot + 16384 + (lw * 3 + i) * 1024);
    };
    stage(0); stage(1);
    wait_vm<7>();
    BARE_BAR();
    stage(2);
#pragma unroll
    for (int s = 0; s < 7; ++s) {
      if (s == 0 || s == 5) wait_vm<7>(); else if (s <= 4) wait_vm<14>(); else wait_vm<0>();
      BARE_BAR();
      if (s == 0) stage(3);
      if (s + 4 < 8) stage(s + 4);
    }
    const int W = blockIdx.x * 4 + lw;
    const int grp = W % 86, kp = (W / 86) & 7;
    u32x4 wv[4];
    {
      const u16* wsrc = (grp < 43 ? wg : wu) + (grp % 43) * 32 + (lane & 3) * 8 + (size_t)(kp * 64 + (lane >> 2)) * F_;
#pragma unroll
      for (int i = 0; i < 4; ++i) wv[i] = *(const u32x4*)(wsrc + (size_t)i * 16 * F_);
    }
    BARE_BAR();
    if (W < 86 * 8) {
      float c8[8];
#pragma unroll
      for (int j = 0; j < 4; ++j) {
        c8[2 * j] = (bf_lo(wv[0][j]) + bf_lo(wv[1][j])) + (bf_lo(wv[2][j]) + bf_lo(wv[3][j]));
        c8[2 * j + 1] = (bf_hi(wv[0][j]) + bf_hi(wv[1][j])) + (bf_hi(wv[2][j]) + bf_hi(wv[3][j]));
      }
#pragma unroll
      for (int j = 0; j < 8; ++j) {
        float v = c8[j];
        v += __shfl_xor(v, 4); v += __shfl_xor(v, 8);
        c8[j] = red32(red16(v));
      }
      if (lane < 4) {
        float* dst = csp + kp * 2752 + grp * 32 + lane * 8;
        f32x4 v0 = {c8[0], c8[1], c8[2], c8[3]}, v1 = {c8[4], c8[5], c8[6], c8[7]};
        *(f32x4*)dst = v0; *(f32x4*)(dst + 4) = v1;
      }
    }
    BARE_BAR();
    return;
  }
  const int wr = wave >> 1, wc = wave & 1;
  const int q4 = (lane & 15) >> 2, p = lane & 3;
  unsigned touch0;
  {
    const int t = wave * 64 + lane;
    const int lx = idx * 128 + (t & 127);
    const char* px = (const char*)(x + (size_t)(xcd >> 1) * 512 * D_) + (size_t)lx * 128;
    const int lw_ = idx * 192 + (t < 192 ? t : 0);
    const char* pw = (const char*)wqkv + (size_t)(lw_ / 12) * 3072 + ((lw_ % 12) >> 2) * 1024 + (xcd & 1) * 512 + (lw_ & 3) * 128;
    touch0 = *(const volatile unsigned*)pw;
    if (t < 128) touch0 += *(const volatile unsigned*)px;
    if (t >= 240) touch0 += *(const volatile unsigned*)((const char*)wo + (size_t)(blockIdx.x * 16 + t - 240) * 128);
    if (t >= 128 && t < 128 + 43) {
      const size_t o = (size_t)(blockIdx.x * 43 + t - 128) * 128;
      touch0 += *(const volatile unsigned*)((const char*)wg + o);
      touch0 += *(const volatile unsigned*)((const char*)wu + o);
      touch0 += *(const volatile unsigned*)((const char*)wd + o);
    }
  }
  const int aoff = (wr * 64 + r) * 128 + ((g ^ ((r >> 1) & 7)) << 4);
  const int aoff0 = aoff + wc * 4096, aoff1 = aoff + (wc ^ 1) * 4096;
  const int boff = 16384 + (2 * g) * 768 + q4 * 64 + ((wc ^ (g & 1)) << 5) + p * 8;
  const u32x4 ones = ONES_FRAG;
  f32x4 accq[4] = {}, acck[4] = {}, accv[4] = {};
  f32x4 rs[2] = {}, sqm[2] = {}, cq = {}, ck = {}, cv = {};
  u32x4 fa[2][4], fb[2][3];
  auto rdfrags = [&](int t) {
    const char* slot = smem + ((t >> 1) % K1_NSLOT) * K1_SLOT;
    const int kk = t & 1;
#pragma unroll
    for (int i = 0; i < 4; ++i)
      fa[kk][i] = *(const u32x4*)(slot + (((i < 2 ? aoff0 : aoff1) + (i & 1) * 2048) ^ (kk << 6)));
#pragma unroll
    for (int j = 0; j < 3; ++j)
      fb[kk][j] = tr_frag(slot + boff + kk * 6144 + j * 256, slot + boff + kk * 6144 + 768 + j * 256);
  };
  auto compute = [&](int t) {
    const int kk = t & 1;
    const u32x4* a = fa[kk];
    const u32x4 bq = fb[kk][0], bk = fb[kk][1], bv = fb[kk][2];
#pragma unroll
    for (int i = 0; i < 4; ++i) {
      accq[i] = mfma16(bq, a[i], accq[i]);
      acck[i] = mfma16(a[i], bk, acck[i]);
      accv[i] = mfma16(a[i], bv, accv[i]);
    }
    rs[0] = mfma16(ones, a[0], rs[0]);
    rs[1] = mfma16(ones, a[1], rs[1]);
    sqm[0] = mfma16(a[0], a[0], sqm[0]);
    sqm[1] = mfma16(a[1], a[1], sqm[1]);
    if (kk == wr) {
      cq = mfma16(bq, ones, cq);
      ck = mfma16(ones, bk, ck);
      cv = mfma16(ones, bv, cv);
    }
  };
  BARE_BAR();
  STAMP(0,2);
  rdfrags(0);
#pragma unroll
  for (int s = 0; s < 8; ++s) {
    rdfrags(2 * s + 1);
    compute(2 * s);
    __builtin_amdgcn_sched_barrier(0);
    if (s + 1 < 8) {
      RAW_BAR();
      STAMP(0,3+s);
      rdfrags(2 * s + 2);
    }
    compute(2 * s + 1);
    __builtin_amdgcn_sched_barrier(0);
  }
  STAMP(0,11);
  float* st = (float*)(smem);
  float* csl = (float*)(smem + 2048);
  float* st2 = (float*)(smem + 4096);
  float* red = (float*)(smem + 8192);
  {
#pragma unroll
    for (int i = 0; i < 2; ++i) {
      const float q01 = (r & 1) ? sqm[i][1] : sqm[i][0], q23 = (r & 1) ? sqm[i][3] : sqm[i][2];
      const float q = (r & 2) ? q23 : q01;
      if (g == (r >> 2)) {
        f32x2 v = {rs[i][0], q};
        *(f32x2*)(st + (wr * 64 + wc * 32 + i * 16 + r) * 2) = v;
      }
    }
    float* c = csl + (wr * 2 + wc) * 48;
    if (r == 0) *(f32x4*)(c + 4 * g) = cq;
    if (g == 0) { c[16 + r] = ck[0]; c[32 + r] = cv[0]; }
  }
  __syncthreads();
  STAMP(0,12);
  {
    const f32x2 a0 = *(const f32x2*)(st + (wr * 64 + lane) * 2);
    const float mean = a0[0] * (1.f / 512.f);
    const float var = fmaxf(a0[1] * (1.f / 512.f) - mean * mean, 0.f);
    f32x2 v = {mean, __builtin_amdgcn_rsqf(var + 1e-5f)};
    *(f32x2*)(st2 + (wave * 64 + lane) * 2) = v;
  }
  const f32x4 csq4 = *(const f32x4*)(csl + wc * 48 + 4 * g) + *(const f32x4*)(csl + (2 + wc) * 48 + 4 * g);
  const float csk = csl[wc * 48 + 16 + r] + csl[(2 + wc) * 48 + 16 + r];
  const float csv = csl[wc * 48 + 32 + r] + csl[(2 + wc) * 48 + 32 + r];
  f32x4 kv = {}, zz = {};
#pragma unroll
  for (int ih = 0; ih < 2; ++ih) {
    u32x4 pk, vv;
#pragma unroll
    for (int i2 = 0; i2 < 2; ++i2) {
      const int i = ih * 2 + i2;
      const int mbl = ((ih ^ wc) << 1) + i2;
      {
        const f32x2 mi = *(const f32x2*)(st2 + (wave * 64 + mbl * 16 + r) * 2);
        float qv[4];
#pragma unroll
        for (int j = 0; j < 4; ++j) qv[j] = phi_f((accq[i][j] - mi[0] * csq4[j]) * mi[1]);
        u32x2 o = {pack_bf(qv[0], qv[1]), pack_bf(qv[2], qv[3])};
        const int mblk = rt * 8 + wr * 4 + mbl;
        *(u32x2*)(pq + ((size_t)(mblk * 16 + cg) * 64 + r + 16 * (2 * wc + (g >> 1))) * 8 + (g & 1) * 4) = o;
      }
      {
        const f32x4 m0 = *(const f32x4*)(st2 + (wave * 64 + mbl * 16 + 4 * g) * 2);
        const f32x4 m1 = *(const f32x4*)(st2 + (wave * 64 + mbl * 16 + 4 * g) * 2 + 4);
        const float mean[4] = {m0[0], m0[2], m1[0], m1[2]}, inv[4] = {m0[1], m0[3], m1[1], m1[3]};
        float kk[4], vj[4];
#pragma unroll
        for (int j = 0; j < 4; ++j) {
          kk[j] = phi_f((acck[i][j] - mean[j] * csk) * inv[j]);
          vj[j] = (accv[i][j] - mean[j] * csv) * inv[j];
        }
        pk[2 * i2] = pack_bf(kk[0], kk[1]);
        pk[2 * i2 + 1] = pack_bf(kk[2], kk[3]);
        vv[2 * i2] = pack_bf(vj[0], vj[1]);
        vv[2 * i2 + 1] = pack_bf(vj[2], vj[3]);
      }
    }
    kv = mfma16(pk, vv, kv);
    zz = mfma16(pk, ones, zz);
  }
  {
    float* rd = red + (wc * 2 + wr) * 272;
#pragma unroll
    for (int j = 0; j < 4; ++j) rd[(4 * g + j) * 16 + r] = kv[j];
    if (r == 0) *(f32x4*)(rd + 256 + 4 * g) = zz;
  }
  __syncthreads();
  for (int i = tid; i < 544; i += 256) {
    const int hh = i >= 272 ? 1 : 0, e = i - hh * 272;
    const float* rr = red + hh * 2 * 272 + e;
    part[(size_t)((cg * 2 + hh) * 16 + rt) * 272 + e] = rr[0] + rr[272];
  }
  asm volatile("" :: "v"(touch0));
  STAMP(0,13);
}

typedef __attribute__((address_space(1))) unsigned gu32;
DI void poll_ge(unsigned* c, unsigned target) {
  for (unsigned spins = 0; spins < (1u << 17); ++spins) {
    const unsigned v = __builtin_amdgcn_readfirstlane(
        __hip_atomic_load((gu32*)c, __ATOMIC_RELAXED, __HIP_MEMORY_SCOPE_AGENT));
    if (v >= target) break;
    __builtin_amdgcn_s_sleep(1);
  }
  asm volatile("" ::: "memory");
}
DI void glds16_sc1(const void* sbase, unsigned voff, unsigned dst) {
  asm volatile("s_mov_b32 m0, %2\n\ts_nop 0\n\tglobal_load_lds_dwordx4 %0, %1 sc1"
               :: "v"(voff), "s"(sbase), "s"(dst) : "memory");
}
DI void glds16v(const char* gsrc, unsigned dst) {
  asm volatile("s_mov_b32 m0, %1\n\ts_nop 0\n\tglobal_load_lds_dwordx4 %0, off" :: "v"(gsrc), "s"(dst) : "memory");
}
constexpr int K3_SLOT = 16384 + 24576, K3_NSLOT = 4;

__global__ __launch_bounds__(512) void kb_rest(const u16* __restrict__ x, const u16* __restrict__ pq,
                                               const float* __restrict__ part, const u16* __restrict__ wo,
                                               u16* wp, unsigned* cnt, u16* hf, float* hst,
                                               const float* __restrict__ csp,
                                               const u16* __restrict__ wg, const u16* __restrict__ wu,
                                               const u16* __restrict__ wd, u16* hhf, u16* __restrict__ out) {
  __shared__ __attribute__((aligned(16))) char smem[K3_SLOT * K3_NSLOT];
  const int tid = threadIdx.x, lane = tid & 63;
  const int wave = __builtin_amdgcn_readfirstlane(tid >> 6);
  const int xcd = blockIdx.x & 7, idx = blockIdx.x >> 3;
  const int r = lane & 15, g = lane >> 4;
  const int q4 = (lane & 15) >> 2, p = lane & 3;
  const __amdgpu_buffer_rsrc_t hrs = __builtin_amdgcn_make_buffer_rsrc(hf, 0, S_ * D_ * 2, 0x00020000);
  const __amdgpu_buffer_rsrc_t srs = __builtin_amdgcn_make_buffer_rsrc(hst, 0, S_ * 32 * 4 + 16 * 2752 * 4, 0x00020000);
  unsigned* cnt_w = cnt;
  unsigned* cnt_h = cnt + 8 * 64;
  unsigned* cnt_hh = cnt + 24 * 64;
  STAMP(2,0);
  {
    const int rt = xcd * 4 + (idx >> 3);
    const int ct = idx & 7;
    const int kg = wave >> 2, rb = wave & 3;
    const int mblk = rt * 4 + rb;
    const __amdgpu_buffer_rsrc_t wrs = __builtin_amdgcn_make_buffer_rsrc(wp, 0, 524288 + 8192, 0x00020000);
    const int ph = blockIdx.x >> 3, pc = blockIdx.x & 7;
    float* kv = (float*)(smem + 65536 + 16384);
    float pv[16];
    if (tid < 272) {
#pragma unroll
      for (int pp = 0; pp < 16; ++pp) pv[pp] = part[(size_t)(ph * 16 + pp) * 272 + tid];
    }
    float w[16];
    if (tid < 256) {
#pragma unroll
      for (int e = 0; e < 16; ++e) w[e] = bf_lo(wo[(size_t)(ph * 16 + e) * D_ + pc * 64 + (tid & 63)]);
    }
    if (tid < 272) {
      float s = 0.f;
#pragma unroll
      for (int pp = 0; pp < 16; ++pp) s += pv[pp];
      kv[tid] = s;
    }
    __syncthreads();
    STAMP(2,1);
    if (tid < 256) {
      const int c = tid & 63, dq = tid >> 6;
      float o[4];
#pragma unroll
      for (int j = 0; j < 4; ++j) {
        float s = 0.f;
#pragma unroll
        for (int e4 = 0; e4 < 4; ++e4) {
          const f32x4 k4 = *(const f32x4*)(kv + (dq * 4 + j) * 16 + e4 * 4);
          s = fmaf(k4[0], w[e4 * 4], s); s = fmaf(k4[1], w[e4 * 4 + 1], s);
          s = fmaf(k4[2], w[e4 * 4 + 2], s); s = fmaf(k4[3], w[e4 * 4 + 3], s);
        }
        o[j] = s;
      }
      const int n = pc * 64 + c, nb = n >> 4, cc = n & 15;
      const int ks = ph >> 1, gg = (ph & 1) * 2 + (dq >> 1);
      u32x2 v = {pack_bf(o[0], o[1]), pack_bf(o[2], o[3])};
      __builtin_amdgcn_raw_buffer_store_b64(v, wrs, (unsigned)((nb * 16 + ks) * 64 + gg * 16 + cc) * 16u + (dq & 1) * 8u, 0, 16);
    } else if (tid < 258) {
      const float* zs = kv + 256 + (tid - 256) * 8;
      u32x4 v = {pack_bf(zs[0], zs[1]), pack_bf(zs[2], zs[3]), pack_bf(zs[4], zs[5]), pack_bf(zs[6], zs[7])};
      __builtin_amdgcn_raw_buffer_store_b128(v, wrs, 524288u + (unsigned)(pc * 512 + ph * 16 + (tid - 256) * 8) * 2u, 0, 16);
    }
    WAIT_VM(0);
    __syncthreads();
    if (tid == 0) __hip_atomic_fetch_add((gu32*)(cnt_w + pc * 64), 1u, __ATOMIC_RELAXED, __HIP_MEMORY_SCOPE_AGENT);
    STAMP(2,2);
    float cpart[8];
    const int ccol = ((rt & 1) * 8 + ct) * 172 + (tid - 272);
    const bool do_cs = tid >= 272 && tid < 444;
    if (do_cs) {
#pragma unroll
      for (int kp = 0; kp < 8; ++kp) cpart[kp] = csp[kp * 2752 + ccol];
    }
    u32x4 a[8];
#pragma unroll
    for (int i = 0; i < 8; ++i) a[i] = *(const u32x4*)(pq + ((size_t)(mblk * 16 + kg * 8 + i) * 64 + lane) * 8);
    u32x2 xr[2];
#pragma unroll
    for (int j = 0; j < 2; ++j)
      xr[j] = *(const u32x2*)(x + (size_t)(mblk * 16 + r) * D_ + ct * 64 + (kg * 2 + j) * 16 + g * 4);
    if (do_cs) {
      const float c = ((cpart[0] + cpart[1]) + (cpart[2] + cpart[3])) + ((cpart[4] + cpart[5]) + (cpart[6] + cpart[7]));
      __builtin_amdgcn_raw_buffer_store_b32(__float_as_uint(c), srs, (unsigned)(S_ * 32 * 4 + ((rt >> 1) * 2752 + ccol) * 4), 0, 16);
    }
    if (wave == 0) poll_ge(cnt_w + ct * 64, 32u);
    __syncthreads();
    STAMP(2,3);
    u32x4 zf[8], wv[8];
#pragma unroll
    for (int i = 0; i < 8; ++i)
      zf[i] = __builtin_amdgcn_raw_buffer_load_b128(wrs, 524288u + (unsigned)(ct * 512 + (kg * 8 + i) * 32 + g * 8) * 2u, 0, 16);
#pragma unroll
    for (int i = 0; i < 8; ++i)
      wv[i] = __builtin_amdgcn_raw_buffer_load_b128(wrs, (unsigned)(ct * 65536 + (wave * 8 + i) * 1024 + lane * 16), 0, 16);
    unsigned touch = 0;
    {
      asm volatile("" ::: "memory");
    }
#pragma unroll
    for (int i = 0; i < 8; ++i) {
      float s = dot2(a[i][0], zf[i][0], 0.f);
      s = dot2(a[i][1], zf[i][1], s);
      s = dot2(a[i][2], zf[i][2], s);
      s = dot2(a[i][3], zf[i][3], s);
      const float inv = __builtin_amdgcn_rcpf(red16(s) + 1e-6f);
#pragma unroll
      for (int j = 0; j < 4; ++j) a[i][j] = pack_bf(bf_lo(a[i][j]) * inv, bf_hi(a[i][j]) * inv);
    }
#pragma unroll
    for (int i = 0; i < 8; ++i) *(u32x4*)(smem + (wave * 8 + i) * 1024 + lane * 16) = wv[i];
    __syncthreads();
    STAMP(2,4);
    f32x4 acc[4] = {};
#pragma unroll
    for (int i = 0; i < 8; ++i) {
#pragma unroll
      for (int nb = 0; nb < 4; ++nb) {
        const u32x4 b = *(const u32x4*)(smem + (nb * 16 + kg * 8 + i) * 1024 + lane * 16);
        acc[nb] = mfma16(b, a[i], acc[nb]);
      }
    }
    f32x4* xch = (f32x4*)(smem + 65536);
#pragma unroll
    for (int j = 0; j < 2; ++j) xch[(wave * 2 + j) * 64 + lane] = kg ? acc[j] : acc[2 + j];
    __syncthreads();
    float hs = 0.f, hq = 0.f;
    u32x2 oo[2];
#pragma unroll
    for (int j = 0; j < 2; ++j) {
      const f32x4 o4 = (kg ? acc[2 + j] : acc[j]) + xch[((wave ^ 4) * 2 + j) * 64 + lane];
      u32x2 o;
      o[0] = pack_bf(bf_lo(xr[j][0]) + o4[0], bf_hi(xr[j][0]) + o4[1]);
      o[1] = pack_bf(bf_lo(xr[j][1]) + o4[2], bf_hi(xr[j][1]) + o4[3]);
      oo[j] = o;
      hs = dot2(o[0], 0x3f803f80u, hs); hs = dot2(o[1], 0x3f803f80u, hs);
      hq = dot2(o[0], o[0], hq); hq = dot2(o[1], o[1], hq);
    }
    __builtin_amdgcn_raw_buffer_store_b128(
        merge16(oo[0], oo[1]), hrs, (unsigned)(((mblk * 16 + ct * 2 + kg) * 64 + r + 16 * (2 * (g & 1) + (g >> 1))) * 16), 0, 16);
    hs = red32(red16(hs)); hq = red32(red16(hq));
    if (g == 0) {
      u32x2 v = {__float_as_uint(hs), __float_as_uint(hq)};
      __builtin_amdgcn_raw_buffer_store_b64(v, srs, (unsigned)(((ct * 2 + kg) * S_ + mblk * 16 + r) * 8), 0, 16);
    }
    asm volatile("" :: "v"(touch));
    WAIT_VM(0);
    __syncthreads();
    if (tid == 0) __hip_atomic_fetch_add((gu32*)(cnt_h + (rt >> 1) * 64), 1u, __ATOMIC_RELAXED, __HIP_MEMORY_SCOPE_AGENT);
    STAMP(2,5);
  }
  const int rt3 = (xcd >> 1) * 4 + (idx >> 3);
  const int cg = (xcd & 1) * 8 + (idx & 7);
  const int f0 = cg < 11 ? cg * 96 : 1056 + (cg - 11) * 64;
  const int nvalid = cg < 11 ? 6 : 4;
  const int rt4 = xcd * 4 + (idx >> 3);
  const int ct4 = idx & 7;
  if (wave >= 4) {
    const int lw = wave - 4;
    const unsigned lbase = lds_addr(smem);
    {
      const char* abase = (const char*)(hf + (size_t)((rt3 * 8 + lw * 2) * 16) * 512);
      const long wdelta = (const char*)wu - (const char*)wg;
      const char* bsrc[6];
#pragma unroll
      for (int i = 0; i < 6; ++i) {
        const int line = (lw * 6 + i) * 4 + g;
        const int kq = line / 6, cgrp = line % 6;
        const int k = kq * 4 + ((lane >> 2) & 3);
        const int ch = (lane & 3) ^ (((k >> 3) & 1) << 1);
        int c3 = cgrp < 3 ? cgrp : cgrp - 3;
        c3 = (c3 * 2 < nvalid) ? c3 : 0;
        bsrc[i] = (const char*)(wg + f0) + (unsigned)(k * F_ + c3 * 32 + ch * 8) * 2u + (cgrp >= 3 ? wdelta : 0);
      }
      auto stA = [&](int s) {
        const unsigned slot = lbase + (s % K3_NSLOT) * K3_SLOT;
#pragma unroll
        for (int i = 0; i < 4; ++i)
          glds16_sc1(abase + (i >> 1) * 16384 + (s * 2 + (i & 1)) * 1024, lane * 16, slot + (lw * 4 + i) * 1024);
      };
      auto stB = [&](int s) {
        const unsigned slot = lbase + (s % K3_NSLOT) * K3_SLOT;
#pragma unroll
        for (int i = 0; i < 6; ++i) glds16v(bsrc[i] + (size_t)s * 64 * F_ * 2, slot + 16384 + (lw * 6 + i) * 1024);
      };
      stB(0); stB(1); stB(2); stB(3);
      poll_ge(cnt_h + rt3 * 64, 16u);
      stA(0); stA(1); stA(2); stA(3);
      wait_vm<12>(); BARE_BAR();
      wait_vm<8>();  BARE_BAR(); stA(4); stB(4);
      wait_vm<14>(); BARE_BAR(); stA(5); stB(5);
      wait_vm<20>(); BARE_BAR(); stA(6); stB(6);
      wait_vm<20>(); BARE_BAR(); stA(7); stB(7);
      wait_vm<20>(); BARE_BAR();
      wait_vm<10>(); BARE_BAR();
      wait_vm<0>();  BARE_BAR();
    }
    {
      const char* abase = (const char*)(hhf + (size_t)((rt4 * 4 + lw) * KS4) * 512);
      const char* bbase = (const char*)(wd + (size_t)lw * 32 * D_ + ct4 * 64);
      const unsigned boffs = (unsigned)(((g >> 1) * 4 + ((lane >> 2) & 3)) * D_ + (g & 1) * 32 + (lane & 3) * 8) * 2u;
      auto stA = [&](int s) {
        const unsigned slot = lbase + (s & 3) * 32768;
#pragma unroll
        for (int i = 0; i < 4; ++i)
          glds16_sc1(abase + (s * 4 + ((s == 10 && i == 3) ? 2 : i)) * 1024, lane * 16, slot + lw * 4096 + i * 1024);
      };
      auto stB = [&](int s) {
        const unsigned slot = lbase + (s & 3) * 32768;
        const int so = (s == 10 && lw == 3) ? s - 1 : s;
#pragma unroll
        for (int i = 0; i < 4; ++i)
          glds16(bbase + (size_t)(so * 128 + i * 8) * D_ * 2, (i & 1) ? (boffs ^ 32u) : boffs, slot + 16384 + lw * 4096 + i * 1024);
      };
      stB(0); stB(1); stB(2);
      BARE_BAR();
      stB(3);
      poll_ge(cnt_hh + (rt4 >> 1) * 64, 16u);
      stA(0); stA(1); stA(2); stA(3);
      wait_vm<12>(); BARE_BAR();
      wait_vm<8>();  BARE_BAR(); stA(4); stB(4);
      wait_vm<12>(); BARE_BAR(); stA(5); stB(5);
#pragma unroll
      for (int s = 2; s < 10; ++s) {
        if (s <= 7) wait_vm<16>(); else if (s == 8) wait_vm<8>(); else wait_vm<0>();
        BARE_BAR();
        if (s + 4 < 11) { stA(s + 4); stB(s + 4); }
      }
    }
    return;
  }
  const int wr = wave >> 1, wc = wave & 1;
  {
    const int aoff = wr * 8192 + lane * 16;
    const int bbase2 = 16384 + (2 * g) * 1536 + q4 * 64 + p * 8;
    int boff[3];
#pragma unroll
    for (int j = 0; j < 3; ++j) {
      const int fbl = 3 * wc + j;
      boff[j] = bbase2 + (fbl >> 1) * 256 + (((fbl & 1) ^ (g & 1)) << 5);
    }
    u32x2 hp[16];
    f32x4 cg4[3], cu4[3];
    float mean_l = 0.f, inv2_l = 0.f;
    f32x4 accg[4][3] = {}, accu[4][3] = {};
    u32x4 fa[2][4], fb[2][6];
    auto rdfrags = [&](int t) {
      const char* slot = smem + ((t >> 1) % K3_NSLOT) * K3_SLOT;
      const int kk = t & 1;
#pragma unroll
      for (int i = 0; i < 4; ++i) fa[kk][i] = *(const u32x4*)(slot + aoff + i * 2048 + kk * 1024);
#pragma unroll
      for (int j = 0; j < 3; ++j) {
        fb[kk][j] = tr_frag(slot + boff[j] + kk * 12288, slot + boff[j] + kk * 12288 + 1536);
        fb[kk][3 + j] = tr_frag(slot + boff[j] + 768 + kk * 12288, slot + boff[j] + 768 + kk * 12288 + 1536);
      }
    };
    auto compute = [&](int t) {
      const int kk = t & 1;
      const u32x4* a = fa[kk];
#pragma unroll
      for (int j = 0; j < 3; ++j) {
        const u32x4 bg = fb[kk][j], bu = fb[kk][3 + j];
#pragma unroll
        for (int i = 0; i < 4; ++i) {
          accg[i][j] = mfma16(bg, a[i], accg[i][j]);
          accu[i][j] = mfma16(bu, a[i], accu[i][j]);
        }
      }
    };
    BARE_BAR();
    STAMP(3,0);
    rdfrags(0);
#pragma unroll
    for (int s = 0; s < 8; ++s) {
      rdfrags(2 * s + 1);
      compute(2 * s);
      __builtin_amdgcn_sched_barrier(0);
      if (s + 1 < 8) {
        RAW_BAR();
        STAMP(3,1+s);
        rdfrags(2 * s + 2);
      }
      if (s == 4) {
#pragma unroll
        for (int i = 0; i < 16; ++i)
          hp[i] = __builtin_amdgcn_raw_buffer_load_b64(srs, (unsigned)((i * S_ + rt3 * 128 + wr * 64 + lane) * 8), 0, 16);
      }
      if (s == 6) {
        f32x2 t2 = {};
#pragma unroll
        for (int i = 0; i < 16; ++i) t2 += __builtin_bit_cast(f32x2, hp[i]);
        mean_l = t2[0] * (1.f / 512.f);
        const float var = fmaxf(t2[1] * (1.f / 512.f) - mean_l * mean_l, 0.f);
        inv2_l = __builtin_amdgcn_rcpf(var + 1e-5f);
        asm volatile("" : "+v"(mean_l), "+v"(inv2_l));
#pragma unroll
        for (int j = 0; j < 3; ++j) {
          const int fbl = (3 * wc + j) < nvalid ? (3 * wc + j) : 0;
          const unsigned co = (unsigned)(S_ * 32 * 4 + (rt3 * 2752 + f0 + fbl * 16 + 4 * g) * 4);
          cg4[j] = __builtin_bit_cast(f32x4, __builtin_amdgcn_raw_buffer_load_b128(srs, co, 0, 16));
          cu4[j] = __builtin_bit_cast(f32x4, __builtin_amdgcn_raw_buffer_load_b128(srs, co + F_ * 4, 0, 16));
        }
      }
      compute(2 * s + 1);
      __builtin_amdgcn_sched_barrier(0);
    }
    STAMP(3,9);
    const __amdgpu_buffer_rsrc_t hhrs = __builtin_amdgcn_make_buffer_rsrc(hhf, 0, 128 * KS4 * 1024, 0x00020000);
#pragma unroll
    for (int ip = 0; ip < 2; ++ip) {
      float mean[2], inv2[2];
#pragma unroll
      for (int i2 = 0; i2 < 2; ++i2) {
        mean[i2] = __shfl(mean_l, (ip * 2 + i2) * 16 + r);
        inv2[i2] = __shfl(inv2_l, (ip * 2 + i2) * 16 + r);
      }
      const int mblk = rt3 * 8 + wr * 4 + ip * 2 + (g & 1);
#pragma unroll
      for (int j = 0; j < 3; ++j) {
        const int fbl = 3 * wc + j;
        u32x2 o[2];
#pragma unroll
        for (int i2 = 0; i2 < 2; ++i2) {
          const int i = ip * 2 + i2;
          float hv[4];
#pragma unroll
          for (int e = 0; e < 4; ++e)
            hv[e] = ((accg[i][j][e] - mean[i2] * cg4[j][e]) * inv2[i2]) * (accu[i][j][e] - mean[i2] * cu4[j][e]);
          o[i2][0] = pack_bf(hv[0], hv[1]); o[i2][1] = pack_bf(hv[2], hv[3]);
        }
        const u32x4 v = merge16(o[0], o[1]);
        if (fbl < nvalid) {
          const int fb_ = (f0 >> 4) + fbl;
          __builtin_amdgcn_raw_buffer_store_b128(
              v, hhrs, (unsigned)(((mblk * KS4 + (fb_ >> 1)) * 64 + r + 16 * (2 * (fb_ & 1) + (g >> 1))) * 16), 0, 16);
        }
      }
    }
    WAIT_VM(0);
    RAW_BAR();
    if (tid == 0) __hip_atomic_fetch_add((gu32*)(cnt_hh + rt3 * 64), 1u, __ATOMIC_RELAXED, __HIP_MEMORY_SCOPE_AGENT);
    STAMP(3,10);
  }
  {
    const int boff = 16384 + (2 * g) * 512 + wc * 256 + q4 * 64 + p * 8;
    const int aoff = wr * 8192 + lane * 16;
    u32x2 hr[2][2];
    f32x4 acc[2][2] = {};
    u32x4 fa[2][2][2], fb[2][2][2];
    auto rdfrags = [&](int t) {
      const char* slot = smem + ((t >> 1) & 3) * 32768;
#pragma unroll
      for (int kk = 0; kk < 2; ++kk) {
        const int ksl = (t & 1) * 2 + kk;
#pragma unroll
        for (int mb = 0; mb < 2; ++mb) fa[t & 1][kk][mb] = *(const u32x4*)(slot + aoff + mb * 4096 + ksl * 1024);
#pragma unroll
        for (int nb = 0; nb < 2; ++nb) {
          const int ho = (nb ^ (g & 1)) << 5;
          fb[t & 1][kk][nb] = tr_frag(slot + boff + ksl * 4096 + ho, slot + boff + ksl * 4096 + 512 + ho);
        }
      }
    };
    auto compute = [&](int t) {
#pragma unroll
      for (int kk = 0; kk < 2; ++kk) {
        if (2 * t + kk < KS4) {
#pragma unroll
          for (int mb = 0; mb < 2; ++mb)
#pragma unroll
            for (int nb = 0; nb < 2; ++nb) acc[mb][nb] = mfma16(fb[t & 1][kk][nb], fa[t & 1][kk][mb], acc[mb][nb]);
        }
      }
    };
    BARE_BAR();
    STAMP(4,0);
    rdfrags(0);
#pragma unroll
    for (int mb = 0; mb < 2; ++mb)
#pragma unroll
      for (int nb = 0; nb < 2; ++nb)
        hr[mb][nb] = __builtin_amdgcn_raw_buffer_load_b64(
            hrs, (unsigned)((((rt4 * 4 + wr * 2 + mb) * 16 + ct4 * 2 + wc) * 64 + r + 16 * (2 * nb + (g >> 1))) * 16 + (g & 1) * 8), 0, 16);
#pragma unroll
    for (int s = 0; s < 11; ++s) {
      rdfrags(2 * s + 1);
      compute(2 * s);
      if (s + 1 < 11) {
        RAW_BAR();
        STAMP(4,1+s);
        rdfrags(2 * s + 2);
      }
      compute(2 * s + 1);
    }
#pragma unroll
    for (int mb = 0; mb < 2; ++mb)
#pragma unroll
      for (int nb = 0; nb < 2; ++nb) {
        const f32x4 o4 = acc[mb][nb];
        u32x2 o;
        o[0] = pack_bf(bf_lo(hr[mb][nb][0]) + o4[0], bf_hi(hr[mb][nb][0]) + o4[1]);
        o[1] = pack_bf(bf_lo(hr[mb][nb][1]) + o4[2], bf_hi(hr[mb][nb][1]) + o4[3]);
        *(u32x2*)(out + (size_t)(rt4 * 64 + wr * 32 + mb * 16 + r) * D_ + ct4 * 64 + wc * 32 + nb * 16 + g * 4) = o;
      }
    STAMP(4,14);
  }
}

extern "C" void kernel_launch(void* const* d_in, const int* in_sizes, int n_in, void* d_out, int out_size,
                              void* d_ws, size_t ws_size, hipStream_t stream) {
  const u16* x = (const u16*)d_in[0];
  const u16* wqkv = (const u16*)d_in[1];
  const u16* wo = (const u16*)d_in[2];
  const u16* wg = (const u16*)d_in[4];
  const u16* wu = (const u16*)d_in[5];
  const u16* wd = (const u16*)d_in[6];
  u16* out = (u16*)d_out;
  char* ws = (char*)d_ws;
  u16* pq = (u16*)(ws);
  u16* hf = (u16*)(ws + (2u << 20));
  u16* wp = (u16*)(ws + (4u << 20));
  float* part = (float*)(ws + (5u << 20));
  unsigned* cnt = (unsigned*)(ws + (6u << 20));
  float* csp = (float*)(ws + (6u << 20) + 65536);
  float* hst = (float*)(ws + (7u << 20));
  u16* hhf = (u16*)(ws + (8u << 20));
  k1_qkv<<<256, 512, 0, stream>>>(x, wqkv, pq, part, wg, wu, csp, cnt, wo, wd, out);
  kb_rest<<<256, 512, 0, stream>>>(x, pq, part, wo, wp, cnt, hf, hst, csp, wg, wu, wd, hhf, out);
}
```

```cpp
#include <hip/hip_runtime.h>
#include <hip/hip_bf16.h>
#include <stdint.h>

typedef unsigned short u16;
typedef __attribute__((ext_vector_type(8))) __bf16 bf16x8;
typedef __attribute__((ext_vector_type(2))) __bf16 bf16x2;
typedef __attribute__((ext_vector_type(4))) short s16x4;
typedef __attribute__((ext_vector_type(4))) float f32x4;
typedef __attribute__((ext_vector_type(2))) float f32x2;
typedef __attribute__((ext_vector_type(4))) unsigned u32x4;
typedef __attribute__((ext_vector_type(2))) unsigned u32x2;
typedef __attribute__((address_space(3))) s16x4* lds_s16x4_ptr;

#define DI __device__ __forceinline__
#define STAMP(kid,i)

constexpr int S_ = 2048, D_ = 512, F_ = 1376, H_ = 32;
constexpr int KS4 = F_ / 32;

DI float bf_lo(unsigned u) { return __uint_as_float(u << 16); }
DI float bf_hi(unsigned u) { return __uint_as_float(u & 0xffff0000u); }
DI unsigned pack_bf(float lo, float hi) {
  f32x2 f = {lo, hi};
  bf16x2 r = __builtin_convertvector(f, bf16x2);
  return __builtin_bit_cast(unsigned, r);
}
DI float bfr(float f) { return bf_lo(pack_bf(f, 0.f) & 0xffffu); }
DI f32x4 mfma16(u32x4 a, u32x4 b, f32x4 c) {
  return __builtin_amdgcn_mfma_f32_16x16x32_bf16(__builtin_bit_cast(bf16x8, a),
                                                 __builtin_bit_cast(bf16x8, b), c, 0, 0, 0);
}
DI float dot2(unsigned a, unsigned b, float c) {
  return __builtin_amdgcn_fdot2_f32_bf16(__builtin_bit_cast(bf16x2, a), __builtin_bit_cast(bf16x2, b), c, false);
}
DI u32x4 tr_frag(const char* lds0, const char* lds1) {
  s16x4 lo = __builtin_amdgcn_ds_read_tr16_b64_v4i16((lds_s16x4_ptr)lds0);
  s16x4 hi = __builtin_amdgcn_ds_read_tr16_b64_v4i16((lds_s16x4_ptr)lds1);
  u32x2 l2 = __builtin_bit_cast(u32x2, lo), h2 = __builtin_bit_cast(u32x2, hi);
  u32x4 r = {l2[0], l2[1], h2[0], h2[1]};
  return r;
}
DI unsigned lds_addr(char* l) { return (unsigned)(size_t)(__attribute__((address_space(3))) char*)l; }
DI void glds16(const void* sbase, unsigned voff, unsigned dst) {
  asm volatile("s_mov_b32 m0, %2\n\ts_nop 0\n\tglobal_load_lds_dwordx4 %0, %1"
               :: "v"(voff), "s"(sbase), "s"(dst) : "memory");
}
DI float red16(float s) {
  const unsigned u = __float_as_uint(s);
  auto r = __builtin_amdgcn_permlane16_swap(u, u, false, false);
  return __uint_as_float(r[0]) + __uint_as_float(r[1]);
}
DI float red32(float s) {
  const unsigned u = __float_as_uint(s);
  auto r = __builtin_amdgcn_permlane32_swap(u, u, false, false);
  return __uint_as_float(r[0]) + __uint_as_float(r[1]);
}
DI u32x4 merge16(u32x2 t0, u32x2 t1) {
  auto x = __builtin_amdgcn_permlane16_swap(t0[0], t1[0], false, false);
  auto y = __builtin_amdgcn_permlane16_swap(t0[1], t1[1], false, false);
  u32x4 v = {x[0], y[0], x[1], y[1]};
  return v;
}
#define WAIT_VM(n) asm volatile("s_waitcnt vmcnt(" #n ")" ::: "memory")
#define RAW_BAR() do { asm volatile("s_waitcnt lgkmcnt(0)" ::: "memory"); __builtin_amdgcn_s_barrier(); } while (0)
#define BARE_BAR() do { asm volatile("" ::: "memory"); __builtin_amdgcn_s_barrier(); } while (0)
template <int N> DI void wait_vm() { asm volatile("s_waitcnt vmcnt(%0)" ::"i"(N) : "memory"); }

DI float phi_f(float t) { return (t > 0.f ? t + 1.f : __expf(t)) + 1e-6f; }
DI void sq_frag(const u32x4& a, float& q) {
#pragma unroll
  for (int i = 0; i < 4; ++i) q = dot2(a[i], a[i], q);
}
#define ONES_FRAG ((u32x4){0x3f803f80u, 0x3f803f80u, 0x3f803f80u, 0x3f803f80u})

constexpr int K1_SLOT = 16384 + 12288, K1_NSLOT = 4;
__global__ __launch_bounds__(512) void k1_qkv(const u16* __restrict__ x, const u16* __restrict__ wqkv,
                                              u16* __restrict__ pq, float* __restrict__ part,
                                              const u16* __restrict__ wg, const u16* __restrict__ wu,
                                              float* __restrict__ csp, unsigned* __restrict__ cnt,
                                              const u16* __restrict__ wo, const u16* __restrict__ wd) {
  __shared__ __attribute__((aligned(16))) char smem[K1_SLOT * K1_NSLOT];
  const int tid = threadIdx.x, lane = tid & 63;
  const int wave = __builtin_amdgcn_readfirstlane(tid >> 6);
  const int xcd = blockIdx.x & 7, idx = blockIdx.x >> 3;
  const int rt = (xcd >> 1) * 4 + (idx >> 3);
  const int cg = (xcd & 1) * 8 + (idx & 7);
  const int r = lane & 15, g = lane >> 4;
  STAMP(0,0);
  if (blockIdx.x == 0 && tid < 40) cnt[tid * 64] = 0u;
  if (wave >= 4) {
    const int lw = wave - 4;
    const unsigned lbase = lds_addr(smem);
    const unsigned aoffs = (unsigned)(lw * 32 + (lane >> 3)) * 1024u + (((lane & 7) ^ ((lane >> 4) & 3)) << 4);
    unsigned boffs[3];
#pragma unroll
    for (int i = 0; i < 3; ++i) {
      const int line = (lw * 3 + i) * 4 + g;
      const int kq = line / 3, cgrp = line % 3;
      const int k = kq * 4 + ((lane >> 2) & 3);
      const int ch = (lane & 3) ^ (((k >> 3) & 1) << 1);
      boffs[i] = (unsigned)(k * 1536 + cgrp * 512 + ch * 8) * 2u;
    }
    const char* abase = (const char*)(x + (size_t)rt * 128 * D_);
    const char* bbase = (const char*)(wqkv + cg * 32);
    auto stage = [&](int s) {
      const unsigned slot = lbase + (s % K1_NSLOT) * K1_SLOT;
#pragma unroll
      for (int i = 0; i < 4; ++i)
        glds16(abase + s * 128 + i * 8192, (i & 1) ? (aoffs ^ 64u) : aoffs, slot + lw * 4096 + i * 1024);
#pragma unroll
      for (int i = 0; i < 3; ++i)
        glds16(bbase + (size_t)s * 64 * 3072, boffs[i], slot + 16384 + (lw * 3 + i) * 1024);
    };
    stage(0); stage(1);
    wait_vm<7>();
    BARE_BAR();
    stage(2);
#pragma unroll
    for (int s = 0; s < 7; ++s) {
      if (s == 0 || s == 5) wait_vm<7>(); else if (s <= 4) wait_vm<14>(); else wait_vm<0>();
      BARE_BAR();
      if (s == 0) stage(3);
      if (s + 4 < 8) stage(s + 4);
    }
    const int W = blockIdx.x * 4 + lw;
    const int grp = W % 86, kp = (W / 86) & 7;
    u32x4 wv[4];
    {
      const u16* wsrc = (grp < 43 ? wg : wu) + (grp % 43) * 32 + (lane & 3) * 8 + (size_t)(kp * 64 + (lane >> 2)) * F_;
#pragma unroll
      for (int i = 0; i < 4; ++i) wv[i] = *(const u32x4*)(wsrc + (size_t)i * 16 * F_);
    }
    BARE_BAR();
    if (W < 86 * 8) {
      float c8[8];
#pragma unroll
      for (int j = 0; j < 4; ++j) {
        c8[2 * j] = (bf_lo(wv[0][j]) + bf_lo(wv[1][j])) + (bf_lo(wv[2][j]) + bf_lo(wv[3][j]));
        c8[2 * j + 1] = (bf_hi(wv[0][j]) + bf_hi(wv[1][j])) + (bf_hi(wv[2][j]) + bf_hi(wv[3][j]));
      }
#pragma unroll
      for (int j = 0; j < 8; ++j) {
        float v = c8[j];
        v += __shfl_xor(v, 4); v += __shfl_xor(v, 8);
        c8[j] = red32(red16(v));
      }
      if (lane < 4) {
        float* dst = csp + kp * 2752 + grp * 32 + lane * 8;
        f32x4 v0 = {c8[0], c8[1], c8[2], c8[3]}, v1 = {c8[4], c8[5], c8[6], c8[7]};
        *(f32x4*)dst = v0; *(f32x4*)(dst + 4) = v1;
      }
    }
    BARE_BAR();
    return;
  }
  const int wr = wave >> 1, wc = wave & 1;
  const int q4 = (lane & 15) >> 2, p = lane & 3;
  unsigned tv0, tv1, tv2, tv3, tv4;
  {
    const int t = wave * 64 + lane;
    const int lx = idx * 128 + (t & 127);
    const char* px = (const char*)(x + (size_t)(xcd >> 1) * 512 * D_) + (size_t)lx * 128;
    const int lw_ = idx * 192 + (t < 192 ? t : 0);
    const char* pw = (const char*)wqkv + (size_t)(lw_ / 12) * 3072 + ((lw_ % 12) >> 2) * 1024 + (xcd & 1) * 512 + (lw_ & 3) * 128;
    if (t >= 240) px = (const char*)wo + (size_t)(blockIdx.x * 16 + t - 240) * 128;
    const size_t o = (size_t)(blockIdx.x * 43 + ((wave * 11 + (lane < 11 ? lane : 0)) < 43 ? (wave * 11 + (lane < 11 ? lane : 0)) : 0)) * 128;
    tv0 = *(const unsigned*)pw;
    tv1 = *(const unsigned*)px;
    tv2 = *(const unsigned*)((const char*)wg + o);
    tv3 = *(const unsigned*)((const char*)wu + o);
    tv4 = *(const unsigned*)((const char*)wd + o);
  }
  const int aoff = (wr * 64 + r) * 128 + ((g ^ ((r >> 1) & 7)) << 4);
  const int aoff0 = aoff + wc * 4096, aoff1 = aoff + (wc ^ 1) * 4096;
  const int boff = 16384 + (2 * g) * 768 + q4 * 64 + ((wc ^ (g & 1)) << 5) + p * 8;
  const u32x4 ones = ONES_FRAG;
  f32x4 accq[4] = {}, acck[4] = {}, accv[4] = {};
  f32x4 rs[2] = {}, sqm[2] = {}, cq = {}, ck = {}, cv = {};
  u32x4 fa[2][4], fb[2][3];
  auto rdfrags = [&](int t) {
    const char* slot = smem + ((t >> 1) % K1_NSLOT) * K1_SLOT;
    const int kk = t & 1;
#pragma unroll
    for (int i = 0; i < 4; ++i)
      fa[kk][i] = *(const u32x4*)(slot + (((i < 2 ? aoff0 : aoff1) + (i & 1) * 2048) ^ (kk << 6)));
#pragma unroll
    for (int j = 0; j < 3; ++j)
      fb[kk][j] = tr_frag(slot + boff + kk * 6144 + j * 256, slot + boff + kk * 6144 + 768 + j * 256);
  };
  auto compute = [&](int t) {
    const int kk = t & 1;
    const u32x4* a = fa[kk];
    const u32x4 bq = fb[kk][0], bk = fb[kk][1], bv = fb[kk][2];
#pragma unroll
    for (int i = 0; i < 4; ++i) {
      accq[i] = mfma16(bq, a[i], accq[i]);
      acck[i] = mfma16(a[i], bk, acck[i]);
      accv[i] = mfma16(a[i], bv, accv[i]);
    }
    rs[0] = mfma16(ones, a[0], rs[0]);
    rs[1] = mfma16(ones, a[1], rs[1]);
    sqm[0] = mfma16(a[0], a[0], sqm[0]);
    sqm[1] = mfma16(a[1], a[1], sqm[1]);
    if (kk == wr) {
      cq = mfma16(bq, ones, cq);
      ck = mfma16(ones, bk, ck);
      cv = mfma16(ones, bv, cv);
    }
  };
  BARE_BAR();
  STAMP(0,2);
  rdfrags(0);
#pragma unroll
  for (int s = 0; s < 8; ++s) {
    rdfrags(2 * s + 1);
    compute(2 * s);
    __builtin_amdgcn_sched_barrier(0);
    if (s + 1 < 8) {
      RAW_BAR();
      STAMP(0,3+s);
      rdfrags(2 * s + 2);
    }
    compute(2 * s + 1);
    __builtin_amdgcn_sched_barrier(0);
  }
  STAMP(0,11);
  float* st = (float*)(smem);
  float* csl = (float*)(smem + 2048);
  float* st2 = (float*)(smem + 4096);
  float* red = (float*)(smem + 8192);
  {
#pragma unroll
    for (int i = 0; i < 2; ++i) {
      const float q01 = (r & 1) ? sqm[i][1] : sqm[i][0], q23 = (r & 1) ? sqm[i][3] : sqm[i][2];
      const float q = (r & 2) ? q23 : q01;
      if (g == (r >> 2)) {
        f32x2 v = {rs[i][0], q};
        *(f32x2*)(st + (wr * 64 + wc * 32 + i * 16 + r) * 2) = v;
      }
    }
    float* c = csl + (wr * 2 + wc) * 48;
    if (r == 0) *(f32x4*)(c + 4 * g) = cq;
    if (g == 0) { c[16 + r] = ck[0]; c[32 + r] = cv[0]; }
  }
  __syncthreads();
  STAMP(0,12);
  {
    const f32x2 a0 = *(const f32x2*)(st + (wr * 64 + lane) * 2);
    const float mean = a0[0] * (1.f / 512.f);
    const float var = fmaxf(a0[1] * (1.f / 512.f) - mean * mean, 0.f);
    f32x2 v = {mean, __builtin_amdgcn_rsqf(var + 1e-5f)};
    *(f32x2*)(st2 + (wave * 64 + lane) * 2) = v;
  }
  const f32x4 csq4 = *(const f32x4*)(csl + wc * 48 + 4 * g) + *(const f32x4*)(csl + (2 + wc) * 48 + 4 * g);
  const float csk = csl[wc * 48 + 16 + r] + csl[(2 + wc) * 48 + 16 + r];
  const float csv = csl[wc * 48 + 32 + r] + csl[(2 + wc) * 48 + 32 + r];
  f32x4 kv = {}, zz = {};
#pragma unroll
  for (int ih = 0; ih < 2; ++ih) {
    u32x4 pk, vv;
#pragma unroll
    for (int i2 = 0; i2 < 2; ++i2) {
      const int i = ih * 2 + i2;
      const int mbl = ((ih ^ wc) << 1) + i2;
      {
        const f32x2 mi = *(const f32x2*)(st2 + (wave * 64 + mbl * 16 + r) * 2);
        float qv[4];
#pragma unroll
        for (int j = 0; j < 4; ++j) qv[j] = phi_f((accq[i][j] - mi[0] * csq4[j]) * mi[1]);
        u32x2 o = {pack_bf(qv[0], qv[1]), pack_bf(qv[2], qv[3])};
        const int mblk = rt * 8 + wr * 4 + mbl;
        *(u32x2*)(pq + ((size_t)(mblk * 16 + cg) * 64 + r + 16 * (2 * wc + (g >> 1))) * 8 + (g & 1) * 4) = o;
      }
      {
        const f32x4 m0 = *(const f32x4*)(st2 + (wave * 64 + mbl * 16 + 4 * g) * 2);
        const f32x4 m1 = *(const f32x4*)(st2 + (wave * 64 + mbl * 16 + 4 * g) * 2 + 4);
        const float mean[4] = {m0[0], m0[2], m1[0], m1[2]}, inv[4] = {m0[1], m0[3], m1[1], m1[3]};
        float kk[4], vj[4];
#pragma unroll
        for (int j = 0; j < 4; ++j) {
          kk[j] = phi_f((acck[i][j] - mean[j] * csk) * inv[j]);
          vj[j] = (accv[i][j] - mean[j] * csv) * inv[j];
        }
        pk[2 * i2] = pack_bf(kk[0], kk[1]);
        pk[2 * i2 + 1] = pack_bf(kk[2], kk[3]);
        vv[2 * i2] = pack_bf(vj[0], vj[1]);
        vv[2 * i2 + 1] = pack_bf(vj[2], vj[3]);
      }
    }
    kv = mfma16(pk, vv, kv);
    zz = mfma16(pk, ones, zz);
  }
  {
    float* rd = red + (wc * 2 + wr) * 272;
#pragma unroll
    for (int j = 0; j < 4; ++j) rd[(4 * g + j) * 16 + r] = kv[j];
    if (r == 0) *(f32x4*)(rd + 256 + 4 * g) = zz;
  }
  __syncthreads();
  for (int i = tid; i < 544; i += 256) {
    const int hh = i >= 272 ? 1 : 0, e = i - hh * 272;
    const float* rr = red + hh * 2 * 272 + e;
    part[(size_t)((cg * 2 + hh) * 16 + rt) * 272 + e] = rr[0] + rr[272];
  }
  asm volatile("" :: "v"(tv0), "v"(tv1), "v"(tv2), "v"(tv3), "v"(tv4));
  STAMP(0,13);
}

typedef __attribute__((address_space(1))) unsigned gu32;
DI void poll_ge(unsigned* c, unsigned target) {
  for (unsigned spins = 0; spins < (1u << 17); ++spins) {
    const unsigned v = __builtin_amdgcn_readfirstlane(
        __hip_atomic_load((gu32*)c, __ATOMIC_RELAXED, __HIP_MEMORY_SCOPE_AGENT));
    if (v >= target) break;
    __builtin_amdgcn_s_sleep(1);
  }
  asm volatile("" ::: "memory");
}
DI void glds16_sc1(const void* sbase, unsigned voff, unsigned dst) {
  asm volatile("s_mov_b32 m0, %2\n\ts_nop 0\n\tglobal_load_lds_dwordx4 %0, %1 sc1"
               :: "v"(voff), "s"(sbase), "s"(dst) : "memory");
}
DI void glds16v(const char* gsrc, unsigned dst) {
  asm volatile("s_mov_b32 m0, %1\n\ts_nop 0\n\tglobal_load_lds_dwordx4 %0, off" :: "v"(gsrc), "s"(dst) : "memory");
}
constexpr int K3_SLOT = 16384 + 24576, K3_NSLOT = 4;

__global__ __launch_bounds__(512) void kb_rest(const u16* __restrict__ x, const u16* __restrict__ pq,
                                               const float* __restrict__ part, const u16* __restrict__ wo,
                                               u16* wp, unsigned* cnt, u16* hf, float* hst,
                                               const float* __restrict__ csp,
                                               const u16* __restrict__ wg, const u16* __restrict__ wu,
                                               const u16* __restrict__ wd, u16* hhf, u16* __restrict__ out) {
  __shared__ __attribute__((aligned(16))) char smem[K3_SLOT * K3_NSLOT];
  const int tid = threadIdx.x, lane = tid & 63;
  const int wave = __builtin_amdgcn_readfirstlane(tid >> 6);
  const int xcd = blockIdx.x & 7, idx = blockIdx.x >> 3;
  const int r = lane & 15, g = lane >> 4;
  const int q4 = (lane & 15) >> 2, p = lane & 3;
  const __amdgpu_buffer_rsrc_t hrs = __builtin_amdgcn_make_buffer_rsrc(hf, 0, S_ * D_ * 2, 0x00020000);
  const __amdgpu_buffer_rsrc_t srs = __builtin_amdgcn_make_buffer_rsrc(hst, 0, S_ * 32 * 4 + 16 * 2752 * 4, 0x00020000);
  unsigned* cnt_w = cnt;
  unsigned* cnt_h = cnt + 8 * 64;
  unsigned* cnt_hh = cnt + 24 * 64;
  STAMP(2,0);
  {
    const int rt = xcd * 4 + (idx >> 3);
    const int ct = idx & 7;
    const int kg = wave >> 2, rb = wave & 3;
    const int mblk = rt * 4 + rb;
    const __amdgpu_buffer_rsrc_t wrs = __builtin_amdgcn_make_buffer_rsrc(wp, 0, 524288 + 8192, 0x00020000);
    const int ph = blockIdx.x >> 3, pc = blockIdx.x & 7;
    float* kv = (float*)(smem + 65536 + 16384);
    float pv[16];
    if (tid < 272) {
#pragma unroll
      for (int pp = 0; pp < 16; ++pp) pv[pp] = part[(size_t)(ph * 16 + pp) * 272 + tid];
    }
    float w[16];
    if (tid < 256) {
#pragma unroll
      for (int e = 0; e < 16; ++e) w[e] = bf_lo(wo[(size_t)(ph * 16 + e) * D_ + pc * 64 + (tid & 63)]);
    }
    if (tid < 272) {
      float s = 0.f;
#pragma unroll
      for (int pp = 0; pp < 16; ++pp) s += pv[pp];
      kv[tid] = s;
    }
    __syncthreads();
    STAMP(2,1);
    if (tid < 256) {
      const int c = tid & 63, dq = tid >> 6;
      float o[4];
#pragma unroll
      for (int j = 0; j < 4; ++j) {
        float s = 0.f;
#pragma unroll
        for (int e4 = 0; e4 < 4; ++e4) {
          const f32x4 k4 = *(const f32x4*)(kv + (dq * 4 + j) * 16 + e4 * 4);
          s = fmaf(k4[0], w[e4 * 4], s); s = fmaf(k4[1], w[e4 * 4 + 1], s);
          s = fmaf(k4[2], w[e4 * 4 + 2], s); s = fmaf(k4[3], w[e4 * 4 + 3], s);
        }
        o[j] = s;
      }
      const int n = pc * 64 + c, nb = n >> 4, cc = n & 15;
      const int ks = ph >> 1, gg = (ph & 1) * 2 + (dq >> 1);
      u32x2 v = {pack_bf(o[0], o[1]), pack_bf(o[2], o[3])};
      __builtin_amdgcn_raw_buffer_store_b64(v, wrs, (unsigned)((nb * 16 + ks) * 64 + gg * 16 + cc) * 16u + (dq & 1) * 8u, 0, 16);
    } else if (tid < 258) {
      const float* zs = kv + 256 + (tid - 256) * 8;
      u32x4 v = {pack_bf(zs[0], zs[1]), pack_bf(zs[2], zs[3]), pack_bf(zs[4], zs[5]), pack_bf(zs[6], zs[7])};
      __builtin_amdgcn_raw_buffer_store_b128(v, wrs, 524288u + (unsigned)(pc * 512 + ph * 16 + (tid - 256) * 8) * 2u, 0, 16);
    }
    WAIT_VM(0);
    __syncthreads();
    if (tid == 0) __hip_atomic_fetch_add((gu32*)(cnt_w + pc * 64), 1u, __ATOMIC_RELAXED, __HIP_MEMORY_SCOPE_AGENT);
    STAMP(2,2);
    float cpart[8];
    const int ccol = ((rt & 1) * 8 + ct) * 172 + (tid - 272);
    const bool do_cs = tid >= 272 && tid < 444;
    if (do_cs) {
#pragma unroll
      for (int kp = 0; kp < 8; ++kp) cpart[kp] = csp[kp * 2752 + ccol];
    }
    u32x4 a[8];
#pragma unroll
    for (int i = 0; i < 8; ++i) a[i] = *(const u32x4*)(pq + ((size_t)(mblk * 16 + kg * 8 + i) * 64 + lane) * 8);
    u32x2 xr[2];
#pragma unroll
    for (int j = 0; j < 2; ++j)
      xr[j] = *(const u32x2*)(x + (size_t)(mblk * 16 + r) * D_ + ct * 64 + (kg * 2 + j) * 16 + g * 4);
    if (do_cs) {
      const float c = ((cpart[0] + cpart[1]) + (cpart[2] + cpart[3])) + ((cpart[4] + cpart[5]) + (cpart[6] + cpart[7]));
      __builtin_amdgcn_raw_buffer_store_b32(__float_as_uint(c), srs, (unsigned)(S_ * 32 * 4 + ((rt >> 1) * 2752 + ccol) * 4), 0, 16);
    }
    if (wave == 0) poll_ge(cnt_w + ct * 64, 32u);
    __syncthreads();
    STAMP(2,3);
    u32x4 zf[8], wv[8];
#pragma unroll
    for (int i = 0; i < 8; ++i)
      zf[i] = __builtin_amdgcn_raw_buffer_load_b128(wrs, 524288u + (unsigned)(ct * 512 + (kg * 8 + i) * 32 + g * 8) * 2u, 0, 16);
#pragma unroll
    for (int i = 0; i < 8; ++i)
      wv[i] = __builtin_amdgcn_raw_buffer_load_b128(wrs, (unsigned)(ct * 65536 + (wave * 8 + i) * 1024 + lane * 16), 0, 16);
#pragma unroll
    for (int i = 0; i < 8; ++i) {
      float s = dot2(a[i][0], zf[i][0], 0.f);
      s = dot2(a[i][1], zf[i][1], s);
      s = dot2(a[i][2], zf[i][2], s);
      s = dot2(a[i][3], zf[i][3], s);
      const float inv = __builtin_amdgcn_rcpf(red16(s) + 1e-6f);
#pragma unroll
      for (int j = 0; j < 4; ++j) a[i][j] = pack_bf(bf_lo(a[i][j]) * inv, bf_hi(a[i][j]) * inv);
    }
#pragma unroll
    for (int i = 0; i < 8; ++i) *(u32x4*)(smem + (wave * 8 + i) * 1024 + lane * 16) = wv[i];
    __syncthreads();
    STAMP(2,4);
    f32x4 acc[4] = {};
#pragma unroll
    for (int i = 0; i < 8; ++i) {
#pragma unroll
      for (int nb = 0; nb < 4; ++nb) {
        const u32x4 b = *(const u32x4*)(smem + (nb * 16 + kg * 8 + i) * 1024 + lane * 16);
        acc[nb] = mfma16(b, a[i], acc[nb]);
      }
    }
    f32x4* xch = (f32x4*)(smem + 65536);
#pragma unroll
    for (int j = 0; j < 2; ++j) xch[(wave * 2 + j) * 64 + lane] = kg ? acc[j] : acc[2 + j];
    __syncthreads();
    float hs = 0.f, hq = 0.f;
    u32x2 oo[2];
#pragma unroll
    for (int j = 0; j < 2; ++j) {
      const f32x4 o4 = (kg ? acc[2 + j] : acc[j]) + xch[((wave ^ 4) * 2 + j) * 64 + lane];
      u32x2 o;
      o[0] = pack_bf(bf_lo(xr[j][0]) + o4[0], bf_hi(xr[j][0]) + o4[1]);
      o[1] = pack_bf(bf_lo(xr[j][1]) + o4[2], bf_hi(xr[j][1]) + o4[3]);
      oo[j] = o;
      hs = dot2(o[0], 0x3f803f80u, hs); hs = dot2(o[1], 0x3f803f80u, hs);
      hq = dot2(o[0], o[0], hq); hq = dot2(o[1], o[1], hq);
    }
    __builtin_amdgcn_raw_buffer_store_b128(
        merge16(oo[0], oo[1]), hrs, (unsigned)(((mblk * 16 + ct * 2 + kg) * 64 + r + 16 * (2 * (g & 1) + (g >> 1))) * 16), 0, 16);
    hs = red32(red16(hs)); hq = red32(red16(hq));
    if (g == 0) {
      u32x2 v = {__float_as_uint(hs), __float_as_uint(hq)};
      __builtin_amdgcn_raw_buffer_store_b64(v, srs, (unsigned)(((ct * 2 + kg) * S_ + mblk * 16 + r) * 8), 0, 16);
    }
    WAIT_VM(0);
    __syncthreads();
    if (tid == 0) __hip_atomic_fetch_add((gu32*)(cnt_h + (rt >> 1) * 64), 1u, __ATOMIC_RELAXED, __HIP_MEMORY_SCOPE_AGENT);
    STAMP(2,5);
  }
  const int rt3 = (xcd >> 1) * 4 + (idx >> 3);
  const int cg = (xcd & 1) * 8 + (idx & 7);
  const int f0 = cg < 11 ? cg * 96 : 1056 + (cg - 11) * 64;
  const int nvalid = cg < 11 ? 6 : 4;
  const int rt4 = xcd * 4 + (idx >> 3);
  const int ct4 = idx & 7;
  if (wave >= 4) {
    const int lw = wave - 4;
    const unsigned lbase = lds_addr(smem);
    {
      const char* abase = (const char*)(hf + (size_t)((rt3 * 8 + lw * 2) * 16) * 512);
      const long wdelta = (const char*)wu - (const char*)wg;
      const char* bsrc[6];
#pragma unroll
      for (int i = 0; i < 6; ++i) {
        const int line = (lw * 6 + i) * 4 + g;
        const int kq = line / 6, cgrp = line % 6;
        const int k = kq * 4 + ((lane >> 2) & 3);
        const int ch = (lane & 3) ^ (((k >> 3) & 1) << 1);
        int c3 = cgrp < 3 ? cgrp : cgrp - 3;
        c3 = (c3 * 2 < nvalid) ? c3 : 0;
        bsrc[i] = (const char*)(wg + f0) + (unsigned)(k * F_ + c3 * 32 + ch * 8) * 2u + (cgrp >= 3 ? wdelta : 0);
      }
      auto stA = [&](int s) {
        const unsigned slot = lbase + (s % K3_NSLOT) * K3_SLOT;
#pragma unroll
        for (int i = 0; i < 4; ++i)
          glds16_sc1(abase + (i >> 1) * 16384 + (s * 2 + (i & 1)) * 1024, lane * 16, slot + (lw * 4 + i) * 1024);
      };
      auto stB = [&](int s) {
        const unsigned slot = lbase + (s % K3_NSLOT) * K3_SLOT;
#pragma unroll
        for (int i = 0; i < 6; ++i) glds16v(bsrc[i] + (size_t)s * 64 * F_ * 2, slot + 16384 + (lw * 6 + i) * 1024);
      };
      stB(0); stB(1); stB(2); stB(3);
      poll_ge(cnt_h + rt3 * 64, 16u);
      stA(0); stA(1); stA(2); stA(3);
      wait_vm<12>(); BARE_BAR();
      wait_vm<8>();  BARE_BAR(); stA(4); stB(4);
      wait_vm<14>(); BARE_BAR(); stA(5); stB(5);
      wait_vm<20>(); BARE_BAR(); stA(6); stB(6);
      wait_vm<20>(); BARE_BAR(); stA(7); stB(7);
      wait_vm<20>(); BARE_BAR();
      wait_vm<10>(); BARE_BAR();
      wait_vm<0>();  BARE_BAR();
    }
    {
      const char* abase = (const char*)(hhf + (size_t)((rt4 * 4 + lw) * KS4) * 512);
      const char* bbase = (const char*)(wd + (size_t)lw * 32 * D_ + ct4 * 64);
      const unsigned boffs = (unsigned)(((g >> 1) * 4 + ((lane >> 2) & 3)) * D_ + (g & 1) * 32 + (lane & 3) * 8) * 2u;
      auto stA = [&](int s) {
        const unsigned slot = lbase + (s & 3) * 32768;
#pragma unroll
        for (int i = 0; i < 4; ++i)
          glds16_sc1(abase + (s * 4 + ((s == 10 && i == 3) ? 2 : i)) * 1024, lane * 16, slot + lw * 4096 + i * 1024);
      };
      auto stB = [&](int s) {
        const unsigned slot = lbase + (s & 3) * 32768;
        const int so = (s == 10 && lw == 3) ? s - 1 : s;
#pragma unroll
        for (int i = 0; i < 4; ++i)
          glds16(bbase + (size_t)(so * 128 + i * 8) * D_ * 2, (i & 1) ? (boffs ^ 32u) : boffs, slot + 16384 + lw * 4096 + i * 1024);
      };
      stB(0); stB(1); stB(2);
      BARE_BAR();
      stB(3);
      poll_ge(cnt_hh + (rt4 >> 1) * 64, 16u);
      stA(0); stA(1); stA(2); stA(3);
      wait_vm<12>(); BARE_BAR();
      wait_vm<8>();  BARE_BAR(); stA(4); stB(4);
      wait_vm<12>(); BARE_BAR(); stA(5); stB(5);
#pragma unroll
      for (int s = 2; s < 10; ++s) {
        if (s <= 7) wait_vm<16>(); else if (s == 8) wait_vm<8>(); else wait_vm<0>();
        BARE_BAR();
        if (s + 4 < 11) { stA(s + 4); stB(s + 4); }
      }
    }
    return;
  }
  const int wr = wave >> 1, wc = wave & 1;
  {
    const int aoff = wr * 8192 + lane * 16;
    const int bbase2 = 16384 + (2 * g) * 1536 + q4 * 64 + p * 8;
    int boff[3];
#pragma unroll
    for (int j = 0; j < 3; ++j) {
      const int fbl = 3 * wc + j;
      boff[j] = bbase2 + (fbl >> 1) * 256 + (((fbl & 1) ^ (g & 1)) << 5);
    }
    u32x2 hp[16];
    f32x4 cg4[3], cu4[3];
    float mean_l = 0.f, inv2_l = 0.f;
    f32x4 accg[4][3] = {}, accu[4][3] = {};
    u32x4 fa[2][4], fb[2][6];
    auto rdfrags = [&](int t) {
      const char* slot = smem + ((t >> 1) % K3_NSLOT) * K3_SLOT;
      const int kk = t & 1;
#pragma unroll
      for (int i = 0; i < 4; ++i) fa[kk][i] = *(const u32x4*)(slot + aoff + i * 2048 + kk * 1024);
#pragma unroll
      for (int j = 0; j < 3; ++j) {
        fb[kk][j] = tr_frag(slot + boff[j] + kk * 12288, slot + boff[j] + kk * 12288 + 1536);
        fb[kk][3 + j] = tr_frag(slot + boff[j] + 768 + kk * 12288, slot + boff[j] + 768 + kk * 12288 + 1536);
      }
    };
    auto compute = [&](int t) {
      const int kk = t & 1;
      const u32x4* a = fa[kk];
#pragma unroll
      for (int j = 0; j < 3; ++j) {
        const u32x4 bg = fb[kk][j], bu = fb[kk][3 + j];
#pragma unroll
        for (int i = 0; i < 4; ++i) {
          accg[i][j] = mfma16(bg, a[i], accg[i][j]);
          accu[i][j] = mfma16(bu, a[i], accu[i][j]);
        }
      }
    };
    BARE_BAR();
    STAMP(3,0);
    rdfrags(0);
#pragma unroll
    for (int s = 0; s < 8; ++s) {
      rdfrags(2 * s + 1);
      compute(2 * s);
      __builtin_amdgcn_sched_barrier(0);
      if (s + 1 < 8) {
        RAW_BAR();
        STAMP(3,1+s);
        rdfrags(2 * s + 2);
      }
      if (s == 4) {
#pragma unroll
        for (int i = 0; i < 16; ++i)
          hp[i] = __builtin_amdgcn_raw_buffer_load_b64(srs, (unsigned)((i * S_ + rt3 * 128 + wr * 64 + lane) * 8), 0, 16);
      }
      if (s == 6) {
        f32x2 t2 = {};
#pragma unroll
        for (int i = 0; i < 16; ++i) t2 += __builtin_bit_cast(f32x2, hp[i]);
        mean_l = t2[0] * (1.f / 512.f);
        const float var = fmaxf(t2[1] * (1.f / 512.f) - mean_l * mean_l, 0.f);
        inv2_l = __builtin_amdgcn_rcpf(var + 1e-5f);
        asm volatile("" : "+v"(mean_l), "+v"(inv2_l));
#pragma unroll
        for (int j = 0; j < 3; ++j) {
          const int fbl = (3 * wc + j) < nvalid ? (3 * wc + j) : 0;
          const unsigned co = (unsigned)(S_ * 32 * 4 + (rt3 * 2752 + f0 + fbl * 16 + 4 * g) * 4);
          cg4[j] = __builtin_bit_cast(f32x4, __builtin_amdgcn_raw_buffer_load_b128(srs, co, 0, 16));
          cu4[j] = __builtin_bit_cast(f32x4, __builtin_amdgcn_raw_buffer_load_b128(srs, co + F_ * 4, 0, 16));
        }
      }
      compute(2 * s + 1);
      __builtin_amdgcn_sched_barrier(0);
    }
    STAMP(3,9);
    const __amdgpu_buffer_rsrc_t hhrs = __builtin_amdgcn_make_buffer_rsrc(hhf, 0, 128 * KS4 * 1024, 0x00020000);
#pragma unroll
    for (int ip = 0; ip < 2; ++ip) {
      float mean[2], inv2[2];
#pragma unroll
      for (int i2 = 0; i2 < 2; ++i2) {
        mean[i2] = __shfl(mean_l, (ip * 2 + i2) * 16 + r);
        inv2[i2] = __shfl(inv2_l, (ip * 2 + i2) * 16 + r);
      }
      const int mblk = rt3 * 8 + wr * 4 + ip * 2 + (g & 1);
#pragma unroll
      for (int j = 0; j < 3; ++j) {
        const int fbl = 3 * wc + j;
        u32x2 o[2];
#pragma unroll
        for (int i2 = 0; i2 < 2; ++i2) {
          const int i = ip * 2 + i2;
          float hv[4];
#pragma unroll
          for (int e = 0; e < 4; ++e)
            hv[e] = ((accg[i][j][e] - mean[i2] * cg4[j][e]) * inv2[i2]) * (accu[i][j][e] - mean[i2] * cu4[j][e]);
          o[i2][0] = pack_bf(hv[0], hv[1]); o[i2][1] = pack_bf(hv[2], hv[3]);
        }
        const u32x4 v = merge16(o[0], o[1]);
        if (fbl < nvalid) {
          const int fb_ = (f0 >> 4) + fbl;
          __builtin_amdgcn_raw_buffer_store_b128(
              v, hhrs, (unsigned)(((mblk * KS4 + (fb_ >> 1)) * 64 + r + 16 * (2 * (fb_ & 1) + (g >> 1))) * 16), 0, 16);
        }
      }
    }
    WAIT_VM(0);
    RAW_BAR();
    if (tid == 0) __hip_atomic_fetch_add((gu32*)(cnt_hh + rt3 * 64), 1u, __ATOMIC_RELAXED, __HIP_MEMORY_SCOPE_AGENT);
    STAMP(3,10);
  }
  {
    const int boff = 16384 + (2 * g) * 512 + wc * 256 + q4 * 64 + p * 8;
    const int aoff = wr * 8192 + lane * 16;
    u32x2 hr[2][2];
    f32x4 acc[2][2] = {};
    u32x4 fa[2][2][2], fb[2][2][2];
    auto rdfrags = [&](int t) {
      const char* slot = smem + ((t >> 1) & 3) * 32768;
#pragma unroll
      for (int kk = 0; kk < 2; ++kk) {
        const int ksl = (t & 1) * 2 + kk;
#pragma unroll
        for (int mb = 0; mb < 2; ++mb) fa[t & 1][kk][mb] = *(const u32x4*)(slot + aoff + mb * 4096 + ksl * 1024);
#pragma unroll
        for (int nb = 0; nb < 2; ++nb) {
          const int ho = (nb ^ (g & 1)) << 5;
          fb[t & 1][kk][nb] = tr_frag(slot + boff + ksl * 4096 + ho, slot + boff + ksl * 4096 + 512 + ho);
        }
      }
    };
    auto compute = [&](int t) {
#pragma unroll
      for (int kk = 0; kk < 2; ++kk) {
        if (2 * t + kk < KS4) {
#pragma unroll
          for (int mb = 0; mb < 2; ++mb)
#pragma unroll
            for (int nb = 0; nb < 2; ++nb) acc[mb][nb] = mfma16(fb[t & 1][kk][nb], fa[t & 1][kk][mb], acc[mb][nb]);
        }
      }
    };
    BARE_BAR();
    STAMP(4,0);
    rdfrags(0);
#pragma unroll
    for (int mb = 0; mb < 2; ++mb)
#pragma unroll
      for (int nb = 0; nb < 2; ++nb)
        hr[mb][nb] = __builtin_amdgcn_raw_buffer_load_b64(
            hrs, (unsigned)((((rt4 * 4 + wr * 2 + mb) * 16 + ct4 * 2 + wc) * 64 + r + 16 * (2 * nb + (g >> 1))) * 16 + (g & 1) * 8), 0, 16);
#pragma unroll
    for (int s = 0; s < 11; ++s) {
      rdfrags(2 * s + 1);
      compute(2 * s);
      if (s + 1 < 11) {
        RAW_BAR();
        STAMP(4,1+s);
        rdfrags(2 * s + 2);
      }
      compute(2 * s + 1);
    }
#pragma unroll
    for (int mb = 0; mb < 2; ++mb)
#pragma unroll
      for (int nb = 0; nb < 2; ++nb) {
        const f32x4 o4 = acc[mb][nb];
        u32x2 o;
        o[0] = pack_bf(bf_lo(hr[mb][nb][0]) + o4[0], bf_hi(hr[mb][nb][0]) + o4[1]);
        o[1] = pack_bf(bf_lo(hr[mb][nb][1]) + o4[2], bf_hi(hr[mb][nb][1]) + o4[3]);
        *(u32x2*)(out + (size_t)(rt4 * 64 + wr * 32 + mb * 16 + r) * D_ + ct4 * 64 + wc * 32 + nb * 16 + g * 4) = o;
      }
    STAMP(4,14);
  }
}

extern "C" void kernel_launch(void* const* d_in, const int* in_sizes, int n_in, void* d_out, int out_size,
                              void* d_ws, size_t ws_size, hipStream_t stream) {
  const u16* x = (const u16*)d_in[0];
  const u16* wqkv = (const u16*)d_in[1];
  const u16* wo = (const u16*)d_in[2];
  const u16* wg = (const u16*)d_in[4];
  const u16* wu = (const u16*)d_in[5];
  const u16* wd = (const u16*)d_in[6];
  u16* out = (u16*)d_out;
  char* ws = (char*)d_ws;
  u16* pq = (u16*)(ws);
  u16* hf = (u16*)(ws + (2u << 20));
  u16* wp = (u16*)(ws + (4u << 20));
  float* part = (float*)(ws + (5u << 20));
  unsigned* cnt = (unsigned*)(ws + (6u << 20));
  float* csp = (float*)(ws + (6u << 20) + 65536);
  float* hst = (float*)(ws + (7u << 20));
  u16* hhf = (u16*)(ws + (8u << 20));
  k1_qkv<<<256, 512, 0, stream>>>(x, wqkv, pq, part, wg, wu, csp, cnt, wo, wd);
  kb_rest<<<256, 512, 0, stream>>>(x, pq, part, wo, wp, cnt, hf, hst, csp, wg, wu, wd, hhf, out);
}
```

```cpp
#include <hip/hip_runtime.h>
#include <hip/hip_bf16.h>
#include <stdint.h>

typedef unsigned short u16;
typedef __attribute__((ext_vector_type(8))) __bf16 bf16x8;
typedef __attribute__((ext_vector_type(2))) __bf16 bf16x2;
typedef __attribute__((ext_vector_type(4))) short s16x4;
typedef __attribute__((ext_vector_type(4))) float f32x4;
typedef __attribute__((ext_vector_type(2))) float f32x2;
typedef __attribute__((ext_vector_type(4))) unsigned u32x4;
typedef __attribute__((ext_vector_type(2))) unsigned u32x2;
typedef __attribute__((address_space(3))) s16x4* lds_s16x4_ptr;

#define DI __device__ __forceinline__
#define STAMP(kid,i)

constexpr int S_ = 2048, D_ = 512, F_ = 1376, H_ = 32;
constexpr int KS4 = F_ / 32;

DI float bf_lo(unsigned u) { return __uint_as_float(u << 16); }
DI float bf_hi(unsigned u) { return __uint_as_float(u & 0xffff0000u); }
DI unsigned pack_bf(float lo, float hi) {
  f32x2 f = {lo, hi};
  bf16x2 r = __builtin_convertvector(f, bf16x2);
  return __builtin_bit_cast(unsigned, r);
}
DI float bfr(float f) { return bf_lo(pack_bf(f, 0.f) & 0xffffu); }
DI f32x4 mfma16(u32x4 a, u32x4 b, f32x4 c) {
  return __builtin_amdgcn_mfma_f32_16x16x32_bf16(__builtin_bit_cast(bf16x8, a),
                                                 __builtin_bit_cast(bf16x8, b), c, 0, 0, 0);
}
DI float dot2(unsigned a, unsigned b, float c) {
  return __builtin_amdgcn_fdot2_f32_bf16(__builtin_bit_cast(bf16x2, a), __builtin_bit_cast(bf16x2, b), c, false);
}
DI u32x4 tr_frag(const char* lds0, const char* lds1) {
  s16x4 lo = __builtin_amdgcn_ds_read_tr16_b64_v4i16((lds_s16x4_ptr)lds0);
  s16x4 hi = __builtin_amdgcn_ds_read_tr16_b64_v4i16((lds_s16x4_ptr)lds1);
  u32x2 l2 = __builtin_bit_cast(u32x2, lo), h2 = __builtin_bit_cast(u32x2, hi);
  u32x4 r = {l2[0], l2[1], h2[0], h2[1]};
  return r;
}
DI unsigned lds_addr(char* l) { return (unsigned)(size_t)(__attribute__((address_space(3))) char*)l; }
DI void glds16(const void* sbase, unsigned voff, unsigned dst) {
  asm volatile("s_mov_b32 m0, %2\n\ts_nop 0\n\tglobal_load_lds_dwordx4 %0, %1"
               :: "v"(voff), "s"(sbase), "s"(dst) : "memory");
}
DI float red16(float s) {
  const unsigned u = __float_as_uint(s);
  auto r = __builtin_amdgcn_permlane16_swap(u, u, false, false);
  return __uint_as_float(r[0]) + __uint_as_float(r[1]);
}
DI float red32(float s) {
  const unsigned u = __float_as_uint(s);
  auto r = __builtin_amdgcn_permlane32_swap(u, u, false, false);
  return __uint_as_float(r[0]) + __uint_as_float(r[1]);
}
DI u32x4 merge16(u32x2 t0, u32x2 t1) {
  auto x = __builtin_amdgcn_permlane16_swap(t0[0], t1[0], false, false);
  auto y = __builtin_amdgcn_permlane16_swap(t0[1], t1[1], false, false);
  u32x4 v = {x[0], y[0], x[1], y[1]};
  return v;
}
#define WAIT_VM(n) asm volatile("s_waitcnt vmcnt(" #n ")" ::: "memory")
#define RAW_BAR() do { asm volatile("s_waitcnt lgkmcnt(0)" ::: "memory"); __builtin_amdgcn_s_barrier(); } while (0)
#define BARE_BAR() do { asm volatile("" ::: "memory"); __builtin_amdgcn_s_barrier(); } while (0)
template <int N> DI void wait_vm() { asm volatile("s_waitcnt vmcnt(%0)" ::"i"(N) : "memory"); }

DI float phi_f(float t) { return (t > 0.f ? t + 1.f : __expf(t)) + 1e-6f; }
DI void sq_frag(const u32x4& a, float& q) {
#pragma unroll
  for (int i = 0; i < 4; ++i) q = dot2(a[i], a[i], q);
}
#define ONES_FRAG ((u32x4){0x3f803f80u, 0x3f803f80u, 0x3f803f80u, 0x3f803f80u})

constexpr int K1_SLOT = 16384 + 12288, K1_NSLOT = 4;
__global__ __launch_bounds__(512) void k1_qkv(const u16* __restrict__ x, const u16* __restrict__ wqkv,
                                              u16* __restrict__ pq, float* __restrict__ part,
                                              const u16* __restrict__ wg, const u16* __restrict__ wu,
                                              float* __restrict__ csp, unsigned* __restrict__ cnt,
                                              const u16* __restrict__ wo, const u16* __restrict__ wd) {
  __shared__ __attribute__((aligned(16))) char smem[K1_SLOT * K1_NSLOT];
  const int tid = threadIdx.x, lane = tid & 63;
  const int wave = __builtin_amdgcn_readfirstlane(tid >> 6);
  const int xcd = blockIdx.x & 7, idx = blockIdx.x >> 3;
  const int rt = (xcd >> 1) * 4 + (idx >> 3);
  const int cg = (xcd & 1) * 8 + (idx & 7);
  const int r = lane & 15, g = lane >> 4;
  STAMP(0,0);
  if (blockIdx.x == 0 && tid < 40) cnt[tid * 64] = 0u;
  if (wave >= 4) {
    const int lw = wave - 4;
    const unsigned lbase = lds_addr(smem);
    const unsigned aoffs = (unsigned)(lw * 32 + (lane >> 3)) * 1024u + (((lane & 7) ^ ((lane >> 4) & 3)) << 4);
    unsigned boffs[3];
#pragma unroll
    for (int i = 0; i < 3; ++i) {
      const int line = (lw * 3 + i) * 4 + g;
      const int kq = line / 3, cgrp = line % 3;
      const int k = kq * 4 + ((lane >> 2) & 3);
      const int ch = (lane & 3) ^ (((k >> 3) & 1) << 1);
      boffs[i] = (unsigned)(k * 1536 + cgrp * 512 + ch * 8) * 2u;
    }
    const char* abase = (const char*)(x + (size_t)rt * 128 * D_);
    const char* bbase = (const char*)(wqkv + cg * 32);
    auto stage = [&](int s) {
      const unsigned slot = lbase + (s % K1_NSLOT) * K1_SLOT;
#pragma unroll
      for (int i = 0; i < 4; ++i)
        glds16(abase + s * 128 + i * 8192, (i & 1) ? (aoffs ^ 64u) : aoffs, slot + lw * 4096 + i * 1024);
#pragma unroll
      for (int i = 0; i < 3; ++i)
        glds16(bbase + (size_t)s * 64 * 3072, boffs[i], slot + 16384 + (lw * 3 + i) * 1024);
    };
    stage(0); stage(1);
    wait_vm<7>();
    BARE_BAR();
    stage(2);
#pragma unroll
    for (int s = 0; s < 7; ++s) {
      if (s == 0 || s == 5) wait_vm<7>(); else if (s <= 4) wait_vm<14>(); else wait_vm<0>();
      BARE_BAR();
      if (s == 0) stage(3);
      if (s + 4 < 8) stage(s + 4);
    }
    const int W = blockIdx.x * 4 + lw;
    const int grp = W % 86, kp = (W / 86) & 7;
    u32x4 wv[4];
    {
      const u16* wsrc = (grp < 43 ? wg : wu) + (grp % 43) * 32 + (lane & 3) * 8 + (size_t)(kp * 64 + (lane >> 2)) * F_;
#pragma unroll
      for (int i = 0; i < 4; ++i) wv[i] = *(const u32x4*)(wsrc + (size_t)i * 16 * F_);
    }
    BARE_BAR();
    if (W < 86 * 8) {
      float c8[8];
#pragma unroll
      for (int j = 0; j < 4; ++j) {
        c8[2 * j] = (bf_lo(wv[0][j]) + bf_lo(wv[1][j])) + (bf_lo(wv[2][j]) + bf_lo(wv[3][j]));
        c8[2 * j + 1] = (bf_hi(wv[0][j]) + bf_hi(wv[1][j])) + (bf_hi(wv[2][j]) + bf_hi(wv[3][j]));
      }
#pragma unroll
      for (int j = 0; j < 8; ++j) {
        float v = c8[j];
        v += __shfl_xor(v, 4); v += __shfl_xor(v, 8);
        c8[j] = red32(red16(v));
      }
      if (lane < 4) {
        float* dst = csp + kp * 2752 + grp * 32 + lane * 8;
        f32x4 v0 = {c8[0], c8[1], c8[2], c8[3]}, v1 = {c8[4], c8[5], c8[6], c8[7]};
        *(f32x4*)dst = v0; *(f32x4*)(dst + 4) = v1;
      }
    }
    BARE_BAR();
    return;
  }
  const int wr = wave >> 1, wc = wave & 1;
  const int q4 = (lane & 15) >> 2, p = lane & 3;
  unsigned tv0, tv1, tv2, tv3, tv4;
  {
    const int t = wave * 64 + lane;
    const int lx = idx * 128 + (t & 127);
    const char* px = (const char*)(x + (size_t)(xcd >> 1) * 512 * D_) + (size_t)lx * 128;
    const int lw_ = idx * 192 + (t < 192 ? t : 0);
    const char* pw = (const char*)wqkv + (size_t)(lw_ / 12) * 3072 + ((lw_ % 12) >> 2) * 1024 + (xcd & 1) * 512 + (lw_ & 3) * 128;
    if (t >= 240) px = (const char*)wo + (size_t)(blockIdx.x * 16 + t - 240) * 128;
    const size_t o = (size_t)(blockIdx.x * 43 + ((wave * 11 + (lane < 11 ? lane : 0)) < 43 ? (wave * 11 + (lane < 11 ? lane : 0)) : 0)) * 128;
    tv0 = *(const unsigned*)pw;
    tv1 = *(const unsigned*)px;
    tv2 = *(const unsigned*)((const char*)wg + o);
    tv3 = *(const unsigned*)((const char*)wu + o);
    tv4 = *(const unsigned*)((const char*)wd + o);
  }
  const int aoff = (wr * 64 + r) * 128 + ((g ^ ((r >> 1) & 7)) << 4);
  const int aoff0 = aoff + wc * 4096, aoff1 = aoff + (wc ^ 1) * 4096;
  const int boff = 16384 + (2 * g) * 768 + q4 * 64 + ((wc ^ (g & 1)) << 5) + p * 8;
  const u32x4 ones = ONES_FRAG;
  f32x4 accq[4] = {}, acck[4] = {}, accv[4] = {};
  f32x4 rs[2] = {}, sqm[2] = {}, cq = {}, ck = {}, cv = {};
  u32x4 fa[2][4], fb[2][3];
  auto rdfrags = [&](int t) {
    const char* slot = smem + ((t >> 1) % K1_NSLOT) * K1_SLOT;
    const int kk = t & 1;
#pragma unroll
    for (int i = 0; i < 4; ++i)
      fa[kk][i] = *(const u32x4*)(slot + (((i < 2 ? aoff0 : aoff1) + (i & 1) * 2048) ^ (kk << 6)));
#pragma unroll
    for (int j = 0; j < 3; ++j)
      fb[kk][j] = tr_frag(slot + boff + kk * 6144 + j * 256, slot + boff + kk * 6144 + 768 + j * 256);
  };
  auto compute = [&](int t) {
    const int kk = t & 1;
    const u32x4* a = fa[kk];
    const u32x4 bq = fb[kk][0], bk = fb[kk][1], bv = fb[kk][2];
#pragma unroll
    for (int i = 0; i < 4; ++i) {
      accq[i] = mfma16(bq, a[i], accq[i]);
      acck[i] = mfma16(a[i], bk, acck[i]);
      accv[i] = mfma16(a[i], bv, accv[i]);
    }
    rs[0] = mfma16(ones, a[0], rs[0]);
    rs[1] = mfma16(ones, a[1], rs[1]);
    sqm[0] = mfma16(a[0], a[0], sqm[0]);
    sqm[1] = mfma16(a[1], a[1], sqm[1]);
    if (kk == wr) {
      cq = mfma16(bq, ones, cq);
      ck = mfma16(ones, bk, ck);
      cv = mfma16(ones, bv, cv);
    }
  };
  BARE_BAR();
  STAMP(0,2);
  rdfrags(0);
#pragma unroll
  for (int s = 0; s < 8; ++s) {
    rdfrags(2 * s + 1);
    compute(2 * s);
    __builtin_amdgcn_sched_barrier(0);
    if (s + 1 < 8) {
      RAW_BAR();
      STAMP(0,3+s);
      rdfrags(2 * s + 2);
    }
    compute(2 * s + 1);
    __builtin_amdgcn_sched_barrier(0);
  }
  STAMP(0,11);
  float* st = (float*)(smem);
  float* csl = (float*)(smem + 2048);
  float* st2 = (float*)(smem + 4096);
  float* red = (float*)(smem + 8192);
  {
#pragma unroll
    for (int i = 0; i < 2; ++i) {
      const float q01 = (r & 1) ? sqm[i][1] : sqm[i][0], q23 = (r & 1) ? sqm[i][3] : sqm[i][2];
      const float q = (r & 2) ? q23 : q01;
      if (g == (r >> 2)) {
        f32x2 v = {rs[i][0], q};
        *(f32x2*)(st + (wr * 64 + wc * 32 + i * 16 + r) * 2) = v;
      }
    }
    float* c = csl + (wr * 2 + wc) * 48;
    if (r == 0) *(f32x4*)(c + 4 * g) = cq;
    if (g == 0) { c[16 + r] = ck[0]; c[32 + r] = cv[0]; }
  }
  __syncthreads();
  STAMP(0,12);
  {
    const f32x2 a0 = *(const f32x2*)(st + (wr * 64 + lane) * 2);
    const float mean = a0[0] * (1.f / 512.f);
    const float var = fmaxf(a0[1] * (1.f / 512.f) - mean * mean, 0.f);
    f32x2 v = {mean, __builtin_amdgcn_rsqf(var + 1e-5f)};
    *(f32x2*)(st2 + (wave * 64 + lane) * 2) = v;
  }
  const f32x4 csq4 = *(const f32x4*)(csl + wc * 48 + 4 * g) + *(const f32x4*)(csl + (2 + wc) * 48 + 4 * g);
  const float csk = csl[wc * 48 + 16 + r] + csl[(2 + wc) * 48 + 16 + r];
  const float csv = csl[wc * 48 + 32 + r] + csl[(2 + wc) * 48 + 32 + r];
  f32x4 kv = {}, zz = {};
#pragma unroll
  for (int ih = 0; ih < 2; ++ih) {
    u32x4 pk, vv;
#pragma unroll
    for (int i2 = 0; i2 < 2; ++i2) {
      const int i = ih * 2 + i2;
      const int mbl = ((ih ^ wc) << 1) + i2;
      {
        const f32x2 mi = *(const f32x2*)(st2 + (wave * 64 + mbl * 16 + r) * 2);
        float qv[4];
#pragma unroll
        for (int j = 0; j < 4; ++j) qv[j] = phi_f((accq[i][j] - mi[0] * csq4[j]) * mi[1]);
        u32x2 o = {pack_bf(qv[0], qv[1]), pack_bf(qv[2], qv[3])};
        const int mblk = rt * 8 + wr * 4 + mbl;
        *(u32x2*)(pq + ((size_t)(mblk * 16 + cg) * 64 + r + 16 * (2 * wc + (g >> 1))) * 8 + (g & 1) * 4) = o;
      }
      {
        const f32x4 m0 = *(const f32x4*)(st2 + (wave * 64 + mbl * 16 + 4 * g) * 2);
        const f32x4 m1 = *(const f32x4*)(st2 + (wave * 64 + mbl * 16 + 4 * g) * 2 + 4);
        const float mean[4] = {m0[0], m0[2], m1[0], m1[2]}, inv[4] = {m0[1], m0[3], m1[1], m1[3]};
        float kk[4], vj[4];
#pragma unroll
        for (int j = 0; j < 4; ++j) {
          kk[j] = phi_f((acck[i][j] - mean[j] * csk) * inv[j]);
          vj[j] = (accv[i][j] - mean[j] * csv) * inv[j];
        }
        pk[2 * i2] = pack_bf(kk[0], kk[1]);
        pk[2 * i2 + 1] = pack_bf(kk[2], kk[3]);
        vv[2 * i2] = pack_bf(vj[0], vj[1]);
        vv[2 * i2 + 1] = pack_bf(vj[2], vj[3]);
      }
    }
    kv = mfma16(pk, vv, kv);
    zz = mfma16(pk, ones, zz);
  }
  {
    float* rd = red + (wc * 2 + wr) * 272;
#pragma unroll
    for (int j = 0; j < 4; ++j) rd[(4 * g + j) * 16 + r] = kv[j];
    if (r == 0) *(f32x4*)(rd + 256 + 4 * g) = zz;
  }
  __syncthreads();
  for (int i = tid; i < 544; i += 256) {
    const int hh = i >= 272 ? 1 : 0, e = i - hh * 272;
    const float* rr = red + hh * 2 * 272 + e;
    part[(size_t)((cg * 2 + hh) * 16 + rt) * 272 + e] = rr[0] + rr[272];
  }
  asm volatile("" :: "v"(tv0), "v"(tv1), "v"(tv2), "v"(tv3), "v"(tv4));
  STAMP(0,13);
}

typedef __attribute__((address_space(1))) unsigned gu32;
DI void poll_ge(unsigned* c, unsigned target) {
  for (unsigned spins = 0; spins < (1u << 17); ++spins) {
    const unsigned v = __builtin_amdgcn_readfirstlane(
        __hip_atomic_load((gu32*)c, __ATOMIC_RELAXED, __HIP_MEMORY_SCOPE_AGENT));
    if (v >= target) break;
    __builtin_amdgcn_s_sleep(1);
  }
  asm volatile("" ::: "memory");
}
DI void glds16_sc1(const void* sbase, unsigned voff, unsigned dst) {
  asm volatile("s_mov_b32 m0, %2\n\ts_nop 0\n\tglobal_load_lds_dwordx4 %0, %1 sc1"
               :: "v"(voff), "s"(sbase), "s"(dst) : "memory");
}
DI void glds16v(const char* gsrc, unsigned dst) {
  asm volatile("s_mov_b32 m0, %1\n\ts_nop 0\n\tglobal_load_lds_dwordx4 %0, off" :: "v"(gsrc), "s"(dst) : "memory");
}
constexpr int K3_SLOT = 16384 + 24576, K3_NSLOT = 4;

__global__ __launch_bounds__(512) void kb_rest(const u16* __restrict__ x, const u16* __restrict__ pq,
                                               const float* __restrict__ part, const u16* __restrict__ wo,
                                               u16* wp, unsigned* cnt, u16* hf, float* hst,
                                               const float* __restrict__ csp,
                                               const u16* __restrict__ wg, const u16* __restrict__ wu,
                                               const u16* __restrict__ wd, u16* hhf, u16* __restrict__ out) {
  __shared__ __attribute__((aligned(16))) char smem[K3_SLOT * K3_NSLOT];
  const int tid = threadIdx.x, lane = tid & 63;
  const int wave = __builtin_amdgcn_readfirstlane(tid >> 6);
  const int xcd = blockIdx.x & 7, idx = blockIdx.x >> 3;
  const int r = lane & 15, g = lane >> 4;
  const int q4 = (lane & 15) >> 2, p = lane & 3;
  const __amdgpu_buffer_rsrc_t hrs = __builtin_amdgcn_make_buffer_rsrc(hf, 0, S_ * D_ * 2, 0x00020000);
  const __amdgpu_buffer_rsrc_t srs = __builtin_amdgcn_make_buffer_rsrc(hst, 0, S_ * 32 * 4 + 16 * 2752 * 4, 0x00020000);
  unsigned* cnt_w = cnt;
  unsigned* cnt_h = cnt + 8 * 64;
  unsigned* cnt_hh = cnt + 24 * 64;
  STAMP(2,0);
  {
    const int rt = xcd * 4 + (idx >> 3);
    const int ct = idx & 7;
    const int kg = wave >> 2, rb = wave & 3;
    const int mblk = rt * 4 + rb;
    const __amdgpu_buffer_rsrc_t wrs = __builtin_amdgcn_make_buffer_rsrc(wp, 0, 524288 + 8192, 0x00020000);
    const int ph = blockIdx.x >> 3, pc = blockIdx.x & 7;
    float* kv = (float*)(smem + 65536 + 16384);
    float pv[16];
    if (tid < 272) {
#pragma unroll
      for (int pp = 0; pp < 16; ++pp) pv[pp] = part[(size_t)(ph * 16 + pp) * 272 + tid];
    }
    float w[16];
    if (tid < 256) {
#pragma unroll
      for (int e = 0; e < 16; ++e) w[e] = bf_lo(wo[(size_t)(ph * 16 + e) * D_ + pc * 64 + (tid & 63)]);
    }
    if (tid < 272) {
      float s = 0.f;
#pragma unroll
      for (int pp = 0; pp < 16; ++pp) s += pv[pp];
      kv[tid] = s;
    }
    __syncthreads();
    STAMP(2,1);
    if (tid < 256) {
      const int c = tid & 63, dq = tid >> 6;
      float o[4];
#pragma unroll
      for (int j = 0; j < 4; ++j) {
        float s = 0.f;
#pragma unroll
        for (int e4 = 0; e4 < 4; ++e4) {
          const f32x4 k4 = *(const f32x4*)(kv + (dq * 4 + j) * 16 + e4 * 4);
          s = fmaf(k4[0], w[e4 * 4], s); s = fmaf(k4[1], w[e4 * 4 + 1], s);
          s = fmaf(k4[2], w[e4 * 4 + 2], s); s = fmaf(k4[3], w[e4 * 4 + 3], s);
        }
        o[j] = s;
      }
      const int n = pc * 64 + c, nb = n >> 4, cc = n & 15;
      const int ks = ph >> 1, gg = (ph & 1) * 2 + (dq >> 1);
      u32x2 v = {pack_bf(o[0], o[1]), pack_bf(o[2], o[3])};
      __builtin_amdgcn_raw_buffer_store_b64(v, wrs, (unsigned)((nb * 16 + ks) * 64 + gg * 16 + cc) * 16u + (dq & 1) * 8u, 0, 16);
    } else if (tid < 258) {
      const float* zs = kv + 256 + (tid - 256) * 8;
      u32x4 v = {pack_bf(zs[0], zs[1]), pack_bf(zs[2], zs[3]), pack_bf(zs[4], zs[5]), pack_bf(zs[6], zs[7])};
      __builtin_amdgcn_raw_buffer_store_b128(v, wrs, 524288u + (unsigned)(pc * 512 + ph * 16 + (tid - 256) * 8) * 2u, 0, 16);
    }
    WAIT_VM(0);
    __syncthreads();
    if (tid == 0) __hip_atomic_fetch_add((gu32*)(cnt_w + pc * 64), 1u, __ATOMIC_RELAXED, __HIP_MEMORY_SCOPE_AGENT);
    STAMP(2,2);
    float cpart[8];
    const int ccol = ((rt & 1) * 8 + ct) * 172 + (tid - 272);
    const bool do_cs = tid >= 272 && tid < 444;
    if (do_cs) {
#pragma unroll
      for (int kp = 0; kp < 8; ++kp) cpart[kp] = csp[kp * 2752 + ccol];
    }
    u32x4 a[8];
#pragma unroll
    for (int i = 0; i < 8; ++i) a[i] = *(const u32x4*)(pq + ((size_t)(mblk * 16 + kg * 8 + i) * 64 + lane) * 8);
    u32x2 xr[2];
#pragma unroll
    for (int j = 0; j < 2; ++j)
      xr[j] = *(const u32x2*)(x + (size_t)(mblk * 16 + r) * D_ + ct * 64 + (kg * 2 + j) * 16 + g * 4);
    if (do_cs) {
      const float c = ((cpart[0] + cpart[1]) + (cpart[2] + cpart[3])) + ((cpart[4] + cpart[5]) + (cpart[6] + cpart[7]));
      __builtin_amdgcn_raw_buffer_store_b32(__float_as_uint(c), srs, (unsigned)(S_ * 32 * 4 + ((rt >> 1) * 2752 + ccol) * 4), 0, 16);
    }
    unsigned tw0 = 0, tw1 = 0;
    if (wave >= 1) {
      const int t = (wave - 1) * 64 + lane;
      tw0 = *(const unsigned*)((const char*)wd + (size_t)(idx * 344 + (t < 344 ? t : 0)) * 128);
      const int t2 = t < 384 ? t : 0;
      const int m = (t2 / 12) & 1, rr = idx * 16 + (t2 / 24), ln = t2 % 12;
      const size_t off = (size_t)rr * (F_ * 2) + (xcd & 1) * 1536 + (size_t)ln * 128;
      const size_t lim = (size_t)rr * (F_ * 2) + F_ * 2 - 4;
      tw1 = *(const unsigned*)((const char*)(m ? wu : wg) + (off < lim ? off : lim));
    }
    if (wave == 0) poll_ge(cnt_w + ct * 64, 32u);
    __syncthreads();
    STAMP(2,3);
    u32x4 zf[8], wv[8];
#pragma unroll
    for (int i = 0; i < 8; ++i)
      zf[i] = __builtin_amdgcn_raw_buffer_load_b128(wrs, 524288u + (unsigned)(ct * 512 + (kg * 8 + i) * 32 + g * 8) * 2u, 0, 16);
#pragma unroll
    for (int i = 0; i < 8; ++i)
      wv[i] = __builtin_amdgcn_raw_buffer_load_b128(wrs, (unsigned)(ct * 65536 + (wave * 8 + i) * 1024 + lane * 16), 0, 16);
#pragma unroll
    for (int i = 0; i < 8; ++i) {
      float s = dot2(a[i][0], zf[i][0], 0.f);
      s = dot2(a[i][1], zf[i][1], s);
      s = dot2(a[i][2], zf[i][2], s);
      s = dot2(a[i][3], zf[i][3], s);
      const float inv = __builtin_amdgcn_rcpf(red16(s) + 1e-6f);
#pragma unroll
      for (int j = 0; j < 4; ++j) a[i][j] = pack_bf(bf_lo(a[i][j]) * inv, bf_hi(a[i][j]) * inv);
    }
#pragma unroll
    for (int i = 0; i < 8; ++i) *(u32x4*)(smem + (wave * 8 + i) * 1024 + lane * 16) = wv[i];
    __syncthreads();
    STAMP(2,4);
    f32x4 acc[4] = {};
#pragma unroll
    for (int i = 0; i < 8; ++i) {
#pragma unroll
      for (int nb = 0; nb < 4; ++nb) {
        const u32x4 b = *(const u32x4*)(smem + (nb * 16 + kg * 8 + i) * 1024 + lane * 16);
        acc[nb] = mfma16(b, a[i], acc[nb]);
      }
    }
    f32x4* xch = (f32x4*)(smem + 65536);
#pragma unroll
    for (int j = 0; j < 2; ++j) xch[(wave * 2 + j) * 64 + lane] = kg ? acc[j] : acc[2 + j];
    __syncthreads();
    float hs = 0.f, hq = 0.f;
    u32x2 oo[2];
#pragma unroll
    for (int j = 0; j < 2; ++j) {
      const f32x4 o4 = (kg ? acc[2 + j] : acc[j]) + xch[((wave ^ 4) * 2 + j) * 64 + lane];
      u32x2 o;
      o[0] = pack_bf(bf_lo(xr[j][0]) + o4[0], bf_hi(xr[j][0]) + o4[1]);
      o[1] = pack_bf(bf_lo(xr[j][1]) + o4[2], bf_hi(xr[j][1]) + o4[3]);
      oo[j] = o;
      hs = dot2(o[0], 0x3f803f80u, hs); hs = dot2(o[1], 0x3f803f80u, hs);
      hq = dot2(o[0], o[0], hq); hq = dot2(o[1], o[1], hq);
    }
    __builtin_amdgcn_raw_buffer_store_b128(
        merge16(oo[0], oo[1]), hrs, (unsigned)(((mblk * 16 + ct * 2 + kg) * 64 + r + 16 * (2 * (g & 1) + (g >> 1))) * 16), 0, 16);
    hs = red32(red16(hs)); hq = red32(red16(hq));
    if (g == 0) {
      u32x2 v = {__float_as_uint(hs), __float_as_uint(hq)};
      __builtin_amdgcn_raw_buffer_store_b64(v, srs, (unsigned)(((ct * 2 + kg) * S_ + mblk * 16 + r) * 8), 0, 16);
    }
    asm volatile("" :: "v"(tw0), "v"(tw1));
    WAIT_VM(0);
    __syncthreads();
    if (tid == 0) __hip_atomic_fetch_add((gu32*)(cnt_h + (rt >> 1) * 64), 1u, __ATOMIC_RELAXED, __HIP_MEMORY_SCOPE_AGENT);
    STAMP(2,5);
  }
  const int rt3 = (xcd >> 1) * 4 + (idx >> 3);
  const int cg = (xcd & 1) * 8 + (idx & 7);
  const int f0 = cg < 11 ? cg * 96 : 1056 + (cg - 11) * 64;
  const int nvalid = cg < 11 ? 6 : 4;
  const int rt4 = xcd * 4 + (idx >> 3);
  const int ct4 = idx & 7;
  if (wave >= 4) {
    const int lw = wave - 4;
    const unsigned lbase = lds_addr(smem);
    {
      const char* abase = (const char*)(hf + (size_t)((rt3 * 8 + lw * 2) * 16) * 512);
      const long wdelta = (const char*)wu - (const char*)wg;
      const char* bsrc[6];
#pragma unroll
      for (int i = 0; i < 6; ++i) {
        const int line = (lw * 6 + i) * 4 + g;
        const int kq = line / 6, cgrp = line % 6;
        const int k = kq * 4 + ((lane >> 2) & 3);
        const int ch = (lane & 3) ^ (((k >> 3) & 1) << 1);
        int c3 = cgrp < 3 ? cgrp : cgrp - 3;
        c3 = (c3 * 2 < nvalid) ? c3 : 0;
        bsrc[i] = (const char*)(wg + f0) + (unsigned)(k * F_ + c3 * 32 + ch * 8) * 2u + (cgrp >= 3 ? wdelta : 0);
      }
      auto stA = [&](int s) {
        const unsigned slot = lbase + (s % K3_NSLOT) * K3_SLOT;
#pragma unroll
        for (int i = 0; i < 4; ++i)
          glds16_sc1(abase + (i >> 1) * 16384 + (s * 2 + (i & 1)) * 1024, lane * 16, slot + (lw * 4 + i) * 1024);
      };
      auto stB = [&](int s) {
        const unsigned slot = lbase + (s % K3_NSLOT) * K3_SLOT;
#pragma unroll
        for (int i = 0; i < 6; ++i) glds16v(bsrc[i] + (size_t)s * 64 * F_ * 2, slot + 16384 + (lw * 6 + i) * 1024);
      };
      stB(0); stB(1); stB(2); stB(3);
      poll_ge(cnt_h + rt3 * 64, 16u);
      stA(0); stA(1); stA(2); stA(3);
      wait_vm<12>(); BARE_BAR();
      wait_vm<8>();  BARE_BAR(); stA(4); stB(4);
      wait_vm<14>(); BARE_BAR(); stA(5); stB(5);
      wait_vm<20>(); BARE_BAR(); stA(6); stB(6);
      wait_vm<20>(); BARE_BAR(); stA(7); stB(7);
      wait_vm<20>(); BARE_BAR();
      wait_vm<10>(); BARE_BAR();
      wait_vm<0>();  BARE_BAR();
    }
    {
      const char* abase = (const char*)(hhf + (size_t)((rt4 * 4 + lw) * KS4) * 512);
      const char* bbase = (const char*)(wd + (size_t)lw * 32 * D_ + ct4 * 64);
      const unsigned boffs = (unsigned)(((g >> 1) * 4 + ((lane >> 2) & 3)) * D_ + (g & 1) * 32 + (lane & 3) * 8) * 2u;
      auto stA = [&](int s) {
        const unsigned slot = lbase + (s & 3) * 32768;
#pragma unroll
        for (int i = 0; i < 4; ++i)
          glds16_sc1(abase + (s * 4 + ((s == 10 && i == 3) ? 2 : i)) * 1024, lane * 16, slot + lw * 4096 + i * 1024);
      };
      auto stB = [&](int s) {
        const unsigned slot = lbase + (s & 3) * 32768;
        const int so = (s == 10 && lw == 3) ? s - 1 : s;
#pragma unroll
        for (int i = 0; i < 4; ++i)
          glds16(bbase + (size_t)(so * 128 + i * 8) * D_ * 2, (i & 1) ? (boffs ^ 32u) : boffs, slot + 16384 + lw * 4096 + i * 1024);
      };
      stB(0); stB(1); stB(2);
      BARE_BAR();
      stB(3);
      poll_ge(cnt_hh + (rt4 >> 1) * 64, 16u);
      stA(0); stA(1); stA(2); stA(3);
      wait_vm<12>(); BARE_BAR();
      wait_vm<8>();  BARE_BAR(); stA(4); stB(4);
      wait_vm<12>(); BARE_BAR(); stA(5); stB(5);
#pragma unroll
      for (int s = 2; s < 10; ++s) {
        if (s <= 7) wait_vm<16>(); else if (s == 8) wait_vm<8>(); else wait_vm<0>();
        BARE_BAR();
        if (s + 4 < 11) { stA(s + 4); stB(s + 4); }
      }
    }
    return;
  }
  const int wr = wave >> 1, wc = wave & 1;
  {
    const int aoff = wr * 8192 + lane * 16;
    const int bbase2 = 16384 + (2 * g) * 1536 + q4 * 64 + p * 8;
    int boff[3];
#pragma unroll
    for (int j = 0; j < 3; ++j) {
      const int fbl = 3 * wc + j;
      boff[j] = bbase2 + (fbl >> 1) * 256 + (((fbl & 1) ^ (g & 1)) << 5);
    }
    u32x2 hp[16];
    f32x4 cg4[3], cu4[3];
    float mean_l = 0.f, inv2_l = 0.f;
    f32x4 accg[4][3] = {}, accu[4][3] = {};
    u32x4 fa[2][4], fb[2][6];
    auto rdfrags = [&](int t) {
      const char* slot = smem + ((t >> 1) % K3_NSLOT) * K3_SLOT;
      const int kk = t & 1;
#pragma unroll
      for (int i = 0; i < 4; ++i) fa[kk][i] = *(const u32x4*)(slot + aoff + i * 2048 + kk * 1024);
#pragma unroll
      for (int j = 0; j < 3; ++j) {
        fb[kk][j] = tr_frag(slot + boff[j] + kk * 12288, slot + boff[j] + kk * 12288 + 1536);
        fb[kk][3 + j] = tr_frag(slot + boff[j] + 768 + kk * 12288, slot + boff[j] + 768 + kk * 12288 + 1536);
      }
    };
    auto compute = [&](int t) {
      const int kk = t & 1;
      const u32x4* a = fa[kk];
#pragma unroll
      for (int j = 0; j < 3; ++j) {
        const u32x4 bg = fb[kk][j], bu = fb[kk][3 + j];
#pragma unroll
        for (int i = 0; i < 4; ++i) {
          accg[i][j] = mfma16(bg, a[i], accg[i][j]);
          accu[i][j] = mfma16(bu, a[i], accu[i][j]);
        }
      }
    };
    BARE_BAR();
    STAMP(3,0);
    rdfrags(0);
#pragma unroll
    for (int s = 0; s < 8; ++s) {
      rdfrags(2 * s + 1);
      compute(2 * s);
      __builtin_amdgcn_sched_barrier(0);
      if (s + 1 < 8) {
        RAW_BAR();
        STAMP(3,1+s);
        rdfrags(2 * s + 2);
      }
      if (s == 4) {
#pragma unroll
        for (int i = 0; i < 16; ++i)
          hp[i] = __builtin_amdgcn_raw_buffer_load_b64(srs, (unsigned)((i * S_ + rt3 * 128 + wr * 64 + lane) * 8), 0, 16);
      }
      if (s == 6) {
        f32x2 t2 = {};
#pragma unroll
        for (int i = 0; i < 16; ++i) t2 += __builtin_bit_cast(f32x2, hp[i]);
        mean_l = t2[0] * (1.f / 512.f);
        const float var = fmaxf(t2[1] * (1.f / 512.f) - mean_l * mean_l, 0.f);
        inv2_l = __builtin_amdgcn_rcpf(var + 1e-5f);
        asm volatile("" : "+v"(mean_l), "+v"(inv2_l));
#pragma unroll
        for (int j = 0; j < 3; ++j) {
          const int fbl = (3 * wc + j) < nvalid ? (3 * wc + j) : 0;
          const unsigned co = (unsigned)(S_ * 32 * 4 + (rt3 * 2752 + f0 + fbl * 16 + 4 * g) * 4);
          cg4[j] = __builtin_bit_cast(f32x4, __builtin_amdgcn_raw_buffer_load_b128(srs, co, 0, 16));
          cu4[j] = __builtin_bit_cast(f32x4, __builtin_amdgcn_raw_buffer_load_b128(srs, co + F_ * 4, 0, 16));
        }
      }
      compute(2 * s + 1);
      __builtin_amdgcn_sched_barrier(0);
    }
    STAMP(3,9);
    const __amdgpu_buffer_rsrc_t hhrs = __builtin_amdgcn_make_buffer_rsrc(hhf, 0, 128 * KS4 * 1024, 0x00020000);
#pragma unroll
    for (int ip = 0; ip < 2; ++ip) {
      float mean[2], inv2[2];
#pragma unroll
      for (int i2 = 0; i2 < 2; ++i2) {
        mean[i2] = __shfl(mean_l, (ip * 2 + i2) * 16 + r);
        inv2[i2] = __shfl(inv2_l, (ip * 2 + i2) * 16 + r);
      }
      const int mblk = rt3 * 8 + wr * 4 + ip * 2 + (g & 1);
#pragma unroll
      for (int j = 0; j < 3; ++j) {
        const int fbl = 3 * wc + j;
        u32x2 o[2];
#pragma unroll
        for (int i2 = 0; i2 < 2; ++i2) {
          const int i = ip * 2 + i2;
          float hv[4];
#pragma unroll
          for (int e = 0; e < 4; ++e)
            hv[e] = ((accg[i][j][e] - mean[i2] * cg4[j][e]) * inv2[i2]) * (accu[i][j][e] - mean[i2] * cu4[j][e]);
          o[i2][0] = pack_bf(hv[0], hv[1]); o[i2][1] = pack_bf(hv[2], hv[3]);
        }
        const u32x4 v = merge16(o[0], o[1]);
        if (fbl < nvalid) {
          const int fb_ = (f0 >> 4) + fbl;
          __builtin_amdgcn_raw_buffer_store_b128(
              v, hhrs, (unsigned)(((mblk * KS4 + (fb_ >> 1)) * 64 + r + 16 * (2 * (fb_ & 1) + (g >> 1))) * 16), 0, 16);
        }
      }
    }
    WAIT_VM(0);
    RAW_BAR();
    if (tid == 0) __hip_atomic_fetch_add((gu32*)(cnt_hh + rt3 * 64), 1u, __ATOMIC_RELAXED, __HIP_MEMORY_SCOPE_AGENT);
    STAMP(3,10);
  }
  {
    const int boff = 16384 + (2 * g) * 512 + wc * 256 + q4 * 64 + p * 8;
    const int aoff = wr * 8192 + lane * 16;
    u32x2 hr[2][2];
    f32x4 acc[2][2] = {};
    u32x4 fa[2][2][2], fb[2][2][2];
    auto rdfrags = [&](int t) {
      const char* slot = smem + ((t >> 1) & 3) * 32768;
#pragma unroll
      for (int kk = 0; kk < 2; ++kk) {
        const int ksl = (t & 1) * 2 + kk;
#pragma unroll
        for (int mb = 0; mb < 2; ++mb) fa[t & 1][kk][mb] = *(const u32x4*)(slot + aoff + mb * 4096 + ksl * 1024);
#pragma unroll
        for (int nb = 0; nb < 2; ++nb) {
          const int ho = (nb ^ (g & 1)) << 5;
          fb[t & 1][kk][nb] = tr_frag(slot + boff + ksl * 4096 + ho, slot + boff + ksl * 4096 + 512 + ho);
        }
      }
    };
    auto compute = [&](int t) {
#pragma unroll
      for (int kk = 0; kk < 2; ++kk) {
        if (2 * t + kk < KS4) {
#pragma unroll
          for (int mb = 0; mb < 2; ++mb)
#pragma unroll
            for (int nb = 0; nb < 2; ++nb) acc[mb][nb] = mfma16(fb[t & 1][kk][nb], fa[t & 1][kk][mb], acc[mb][nb]);
        }
      }
    };
    BARE_BAR();
    STAMP(4,0);
    rdfrags(0);
#pragma unroll
    for (int mb = 0; mb < 2; ++mb)
#pragma unroll
      for (int nb = 0; nb < 2; ++nb)
        hr[mb][nb] = __builtin_amdgcn_raw_buffer_load_b64(
            hrs, (unsigned)((((rt4 * 4 + wr * 2 + mb) * 16 + ct4 * 2 + wc) * 64 + r + 16 * (2 * nb + (g >> 1))) * 16 + (g & 1) * 8), 0, 16);
#pragma unroll
    for (int s = 0; s < 11; ++s) {
      rdfrags(2 * s + 1);
      compute(2 * s);
      if (s + 1 < 11) {
        RAW_BAR();
        STAMP(4,1+s);
        rdfrags(2 * s + 2);
      }
      compute(2 * s + 1);
    }
#pragma unroll
    for (int mb = 0; mb < 2; ++mb)
#pragma unroll
      for (int nb = 0; nb < 2; ++nb) {
        const f32x4 o4 = acc[mb][nb];
        u32x2 o;
        o[0] = pack_bf(bf_lo(hr[mb][nb][0]) + o4[0], bf_hi(hr[mb][nb][0]) + o4[1]);
        o[1] = pack_bf(bf_lo(hr[mb][nb][1]) + o4[2], bf_hi(hr[mb][nb][1]) + o4[3]);
        *(u32x2*)(out + (size_t)(rt4 * 64 + wr * 32 + mb * 16 + r) * D_ + ct4 * 64 + wc * 32 + nb * 16 + g * 4) = o;
      }
    STAMP(4,14);
  }
}

extern "C" void kernel_launch(void* const* d_in, const int* in_sizes, int n_in, void* d_out, int out_size,
                              void* d_ws, size_t ws_size, hipStream_t stream) {
  const u16* x = (const u16*)d_in[0];
  const u16* wqkv = (const u16*)d_in[1];
  const u16* wo = (const u16*)d_in[2];
  const u16* wg = (const u16*)d_in[4];
  const u16* wu = (const u16*)d_in[5];
  const u16* wd = (const u16*)d_in[6];
  u16* out = (u16*)d_out;
  char* ws = (char*)d_ws;
  u16* pq = (u16*)(ws);
  u16* hf = (u16*)(ws + (2u << 20));
  u16* wp = (u16*)(ws + (4u << 20));
  float* part = (float*)(ws + (5u << 20));
  unsigned* cnt = (unsigned*)(ws + (6u << 20));
  float* csp = (float*)(ws + (6u << 20) + 65536);
  float* hst = (float*)(ws + (7u << 20));
  u16* hhf = (u16*)(ws + (8u << 20));
  k1_qkv<<<256, 512, 0, stream>>>(x, wqkv, pq, part, wg, wu, csp, cnt, wo, wd);
  kb_rest<<<256, 512, 0, stream>>>(x, pq, part, wo, wp, cnt, hf, hst, csp, wg, wu, wd, hhf, out);
}
```

```cpp
#include <hip/hip_runtime.h>
#include <hip/hip_bf16.h>
#include <stdint.h>

typedef unsigned short u16;
typedef __attribute__((ext_vector_type(8))) __bf16 bf16x8;
typedef __attribute__((ext_vector_type(2))) __bf16 bf16x2;
typedef __attribute__((ext_vector_type(4))) short s16x4;
typedef __attribute__((ext_vector_type(4))) float f32x4;
typedef __attribute__((ext_vector_type(2))) float f32x2;
typedef __attribute__((ext_vector_type(4))) unsigned u32x4;
typedef __attribute__((ext_vector_type(2))) unsigned u32x2;
typedef __attribute__((address_space(3))) s16x4* lds_s16x4_ptr;

#define DI __device__ __forceinline__
#define STAMP(kid,i)

constexpr int S_ = 2048, D_ = 512, F_ = 1376, H_ = 32;
constexpr int KS4 = F_ / 32;

DI float bf_lo(unsigned u) { return __uint_as_float(u << 16); }
DI float bf_hi(unsigned u) { return __uint_as_float(u & 0xffff0000u); }
DI unsigned pack_bf(float lo, float hi) {
  f32x2 f = {lo, hi};
  bf16x2 r = __builtin_convertvector(f, bf16x2);
  return __builtin_bit_cast(unsigned, r);
}
DI float bfr(float f) { return bf_lo(pack_bf(f, 0.f) & 0xffffu); }
DI f32x4 mfma16(u32x4 a, u32x4 b, f32x4 c) {
  return __builtin_amdgcn_mfma_f32_16x16x32_bf16(__builtin_bit_cast(bf16x8, a),
                                                 __builtin_bit_cast(bf16x8, b), c, 0, 0, 0);
}
DI float dot2(unsigned a, unsigned b, float c) {
  return __builtin_amdgcn_fdot2_f32_bf16(__builtin_bit_cast(bf16x2, a), __builtin_bit_cast(bf16x2, b), c, false);
}
DI u32x4 tr_frag(const char* lds0, const char* lds1) {
  s16x4 lo = __builtin_amdgcn_ds_read_tr16_b64_v4i16((lds_s16x4_ptr)lds0);
  s16x4 hi = __builtin_amdgcn_ds_read_tr16_b64_v4i16((lds_s16x4_ptr)lds1);
  u32x2 l2 = __builtin_bit_cast(u32x2, lo), h2 = __builtin_bit_cast(u32x2, hi);
  u32x4 r = {l2[0], l2[1], h2[0], h2[1]};
  return r;
}
DI unsigned lds_addr(char* l) { return (unsigned)(size_t)(__attribute__((address_space(3))) char*)l; }
DI void glds16(const void* sbase, unsigned voff, unsigned dst) {
  asm volatile("s_mov_b32 m0, %2\n\ts_nop 0\n\tglobal_load_lds_dwordx4 %0, %1"
               :: "v"(voff), "s"(sbase), "s"(dst) : "memory");
}
DI float red16(float s) {
  const unsigned u = __float_as_uint(s);
  auto r = __builtin_amdgcn_permlane16_swap(u, u, false, false);
  return __uint_as_float(r[0]) + __uint_as_float(r[1]);
}
DI float red32(float s) {
  const unsigned u = __float_as_uint(s);
  auto r = __builtin_amdgcn_permlane32_swap(u, u, false, false);
  return __uint_as_float(r[0]) + __uint_as_float(r[1]);
}
DI u32x4 merge16(u32x2 t0, u32x2 t1) {
  auto x = __builtin_amdgcn_permlane16_swap(t0[0], t1[0], false, false);
  auto y = __builtin_amdgcn_permlane16_swap(t0[1], t1[1], false, false);
  u32x4 v = {x[0], y[0], x[1], y[1]};
  return v;
}
#define WAIT_VM(n) asm volatile("s_waitcnt vmcnt(" #n ")" ::: "memory")
#define RAW_BAR() do { asm volatile("s_waitcnt lgkmcnt(0)" ::: "memory"); __builtin_amdgcn_s_barrier(); } while (0)
#define BARE_BAR() do { asm volatile("" ::: "memory"); __builtin_amdgcn_s_barrier(); } while (0)
template <int N> DI void wait_vm() { asm volatile("s_waitcnt vmcnt(%0)" ::"i"(N) : "memory"); }

DI float phi_f(float t) { return (t > 0.f ? t + 1.f : __expf(t)) + 1e-6f; }
DI void sq_frag(const u32x4& a, float& q) {
#pragma unroll
  for (int i = 0; i < 4; ++i) q = dot2(a[i], a[i], q);
}
#define ONES_FRAG ((u32x4){0x3f803f80u, 0x3f803f80u, 0x3f803f80u, 0x3f803f80u})

constexpr int K1_SLOT = 16384 + 12288, K1_NSLOT = 4;
__global__ __launch_bounds__(512) void k1_qkv(const u16* __restrict__ x, const u16* __restrict__ wqkv,
                                              u16* __restrict__ pq, float* __restrict__ part,
                                              const u16* __restrict__ wg, const u16* __restrict__ wu,
                                              float* __restrict__ csp, unsigned* __restrict__ cnt,
                                              const u16* __restrict__ wo, const u16* __restrict__ wd) {
  __shared__ __attribute__((aligned(16))) char smem[K1_SLOT * K1_NSLOT];
  const int tid = threadIdx.x, lane = tid & 63;
  const int wave = __builtin_amdgcn_readfirstlane(tid >> 6);
  const int xcd = blockIdx.x & 7, idx = blockIdx.x >> 3;
  const int rt = (xcd >> 1) * 4 + (idx >> 3);
  const int cg = (xcd & 1) * 8 + (idx & 7);
  const int r = lane & 15, g = lane >> 4;
  STAMP(0,0);
  if (blockIdx.x == 0 && tid < 40) cnt[tid * 64] = 0u;
  if (wave >= 4) {
    const int lw = wave - 4;
    const unsigned lbase = lds_addr(smem);
    const unsigned aoffs = (unsigned)(lw * 32 + (lane >> 3)) * 1024u + (((lane & 7) ^ ((lane >> 4) & 3)) << 4);
    unsigned boffs[3];
#pragma unroll
    for (int i = 0; i < 3; ++i) {
      const int line = (lw * 3 + i) * 4 + g;
      const int kq = line / 3, cgrp = line % 3;
      const int k = kq * 4 + ((lane >> 2) & 3);
      const int ch = (lane & 3) ^ (((k >> 3) & 1) << 1);
      boffs[i] = (unsigned)(k * 1536 + cgrp * 512 + ch * 8) * 2u;
    }
    const char* abase = (const char*)(x + (size_t)rt * 128 * D_);
    const char* bbase = (const char*)(wqkv + cg * 32);
    auto stage = [&](int s) {
      const unsigned slot = lbase + (s % K1_NSLOT) * K1_SLOT;
#pragma unroll
      for (int i = 0; i < 4; ++i)
        glds16(abase + s * 128 + i * 8192, (i & 1) ? (aoffs ^ 64u) : aoffs, slot + lw * 4096 + i * 1024);
#pragma unroll
      for (int i = 0; i < 3; ++i)
        glds16(bbase + (size_t)s * 64 * 3072, boffs[i], slot + 16384 + (lw * 3 + i) * 1024);
    };
    stage(0); stage(1);
    wait_vm<7>();
    BARE_BAR();
    stage(2);
#pragma unroll
    for (int s = 0; s < 7; ++s) {
      if (s == 0 || s == 5) wait_vm<7>(); else if (s <= 4) wait_vm<14>(); else wait_vm<0>();
      BARE_BAR();
      if (s == 0) stage(3);
      if (s + 4 < 8) stage(s + 4);
    }
    const int W = blockIdx.x * 4 + lw;
    const int grp = W % 86, kp = (W / 86) & 7;
    u32x4 wv[4];
    {
      const u16* wsrc = (grp < 43 ? wg : wu) + (grp % 43) * 32 + (lane & 3) * 8 + (size_t)(kp * 64 + (lane >> 2)) * F_;
#pragma unroll
      for (int i = 0; i < 4; ++i) wv[i] = *(const u32x4*)(wsrc + (size_t)i * 16 * F_);
    }
    BARE_BAR();
    if (W < 86 * 8) {
      float c8[8];
#pragma unroll
      for (int j = 0; j < 4; ++j) {
        c8[2 * j] = (bf_lo(wv[0][j]) + bf_lo(wv[1][j])) + (bf_lo(wv[2][j]) + bf_lo(wv[3][j]));
        c8[2 * j + 1] = (bf_hi(wv[0][j]) + bf_hi(wv[1][j])) + (bf_hi(wv[2][j]) + bf_hi(wv[3][j]));
      }
#pragma unroll
      for (int j = 0; j < 8; ++j) {
        float v = c8[j];
        v += __shfl_xor(v, 4); v += __shfl_xor(v, 8);
        c8[j] = red32(red16(v));
      }
      if (lane < 4) {
        float* dst = csp + kp * 2752 + grp * 32 + lane * 8;
        f32x4 v0 = {c8[0], c8[1], c8[2], c8[3]}, v1 = {c8[4], c8[5], c8[6], c8[7]};
        *(f32x4*)dst = v0; *(f32x4*)(dst + 4) = v1;
      }
    }
    BARE_BAR();
    return;
  }
  const int wr = wave >> 1, wc = wave & 1;
  const int q4 = (lane & 15) >> 2, p = lane & 3;
  unsigned tv0, tv1, tv2, tv3, tv4;
  {
    const int t = wave * 64 + lane;
    const int lx = idx * 128 + (t & 127);
    const char* px = (const char*)(x + (size_t)(xcd >> 1) * 512 * D_) + (size_t)lx * 128;
    const int lw_ = idx * 192 + (t < 192 ? t : 0);
    const char* pw = (const char*)wqkv + (size_t)(lw_ / 12) * 3072 + ((lw_ % 12) >> 2) * 1024 + (xcd & 1) * 512 + (lw_ & 3) * 128;
    if (t >= 240) px = (const char*)wo + (size_t)(blockIdx.x * 16 + t - 240) * 128;
    const size_t o = (size_t)(blockIdx.x * 43 + ((wave * 11 + (lane < 11 ? lane : 0)) < 43 ? (wave * 11 + (lane < 11 ? lane : 0)) : 0)) * 128;
    tv0 = *(const unsigned*)pw;
    tv1 = *(const unsigned*)px;
    tv2 = *(const unsigned*)((const char*)wg + o);
    tv3 = *(const unsigned*)((const char*)wu + o);
    tv4 = *(const unsigned*)((const char*)wd + o);
  }
  const int aoff = (wr * 64 + r) * 128 + ((g ^ ((r >> 1) & 7)) << 4);
  const int aoff0 = aoff + wc * 4096, aoff1 = aoff + (wc ^ 1) * 4096;
  const int boff = 16384 + (2 * g) * 768 + q4 * 64 + ((wc ^ (g & 1)) << 5) + p * 8;
  const u32x4 ones = ONES_FRAG;
  f32x4 accq[4] = {}, acck[4] = {}, accv[4] = {};
  f32x4 rs[2] = {}, sqm[2] = {}, cq = {}, ck = {}, cv = {};
  u32x4 fa[2][4], fb[2][3];
  auto rdfrags = [&](int t) {
    const char* slot = smem + ((t >> 1) % K1_NSLOT) * K1_SLOT;
    const int kk = t & 1;
#pragma unroll
    for (int i = 0; i < 4; ++i)
      fa[kk][i] = *(const u32x4*)(slot + (((i < 2 ? aoff0 : aoff1) + (i & 1) * 2048) ^ (kk << 6)));
#pragma unroll
    for (int j = 0; j < 3; ++j)
      fb[kk][j] = tr_frag(slot + boff + kk * 6144 + j * 256, slot + boff + kk * 6144 + 768 + j * 256);
  };
  auto compute = [&](int t) {
    const int kk = t & 1;
    const u32x4* a = fa[kk];
    const u32x4 bq = fb[kk][0], bk = fb[kk][1], bv = fb[kk][2];
#pragma unroll
    for (int i = 0; i < 4; ++i) {
      accq[i] = mfma16(bq, a[i], accq[i]);
      acck[i] = mfma16(a[i], bk, acck[i]);
      accv[i] = mfma16(a[i], bv, accv[i]);
    }
    rs[0] = mfma16(ones, a[0], rs[0]);
    rs[1] = mfma16(ones, a[1], rs[1]);
    sqm[0] = mfma16(a[0], a[0], sqm[0]);
    sqm[1] = mfma16(a[1], a[1], sqm[1]);
    if (kk == wr) {
      cq = mfma16(bq, ones, cq);
      ck = mfma16(ones, bk, ck);
      cv = mfma16(ones, bv, cv);
    }
  };
  BARE_BAR();
  STAMP(0,2);
  rdfrags(0);
#pragma unroll
  for (int s = 0; s < 8; ++s) {
    rdfrags(2 * s + 1);
    compute(2 * s);
    __builtin_amdgcn_sched_barrier(0);
    if (s + 1 < 8) {
      RAW_BAR();
      STAMP(0,3+s);
      rdfrags(2 * s + 2);
    }
    compute(2 * s + 1);
    __builtin_amdgcn_sched_barrier(0);
  }
  STAMP(0,11);
  float* st = (float*)(smem);
  float* csl = (float*)(smem + 2048);
  float* st2 = (float*)(smem + 4096);
  float* red = (float*)(smem + 8192);
  {
#pragma unroll
    for (int i = 0; i < 2; ++i) {
      const float q01 = (r & 1) ? sqm[i][1] : sqm[i][0], q23 = (r & 1) ? sqm[i][3] : sqm[i][2];
      const float q = (r & 2) ? q23 : q01;
      if (g == (r >> 2)) {
        f32x2 v = {rs[i][0], q};
        *(f32x2*)(st + (wr * 64 + wc * 32 + i * 16 + r) * 2) = v;
      }
    }
    float* c = csl + (wr * 2 + wc) * 48;
    if (r == 0) *(f32x4*)(c + 4 * g) = cq;
    if (g == 0) { c[16 + r] = ck[0]; c[32 + r] = cv[0]; }
  }
  __syncthreads();
  STAMP(0,12);
  {
    const f32x2 a0 = *(const f32x2*)(st + (wr * 64 + lane) * 2);
    const float mean = a0[0] * (1.f / 512.f);
    const float var = fmaxf(a0[1] * (1.f / 512.f) - mean * mean, 0.f);
    f32x2 v = {mean, __builtin_amdgcn_rsqf(var + 1e-5f)};
    *(f32x2*)(st2 + (wave * 64 + lane) * 2) = v;
  }
  const f32x4 csq4 = *(const f32x4*)(csl + wc * 48 + 4 * g) + *(const f32x4*)(csl + (2 + wc) * 48 + 4 * g);
  const float csk = csl[wc * 48 + 16 + r] + csl[(2 + wc) * 48 + 16 + r];
  const float csv = csl[wc * 48 + 32 + r] + csl[(2 + wc) * 48 + 32 + r];
  f32x4 kv = {}, zz = {};
#pragma unroll
  for (int ih = 0; ih < 2; ++ih) {
    u32x4 pk, vv;
#pragma unroll
    for (int i2 = 0; i2 < 2; ++i2) {
      const int i = ih * 2 + i2;
      const int mbl = ((ih ^ wc) << 1) + i2;
      {
        const f32x2 mi = *(const f32x2*)(st2 + (wave * 64 + mbl * 16 + r) * 2);
        float qv[4];
#pragma unroll
        for (int j = 0; j < 4; ++j) qv[j] = phi_f((accq[i][j] - mi[0] * csq4[j]) * mi[1]);
        u32x2 o = {pack_bf(qv[0], qv[1]), pack_bf(qv[2], qv[3])};
        const int mblk = rt * 8 + wr * 4 + mbl;
        *(u32x2*)(pq + ((size_t)(mblk * 16 + cg) * 64 + r + 16 * (2 * wc + (g >> 1))) * 8 + (g & 1) * 4) = o;
      }
      {
        const f32x4 m0 = *(const f32x4*)(st2 + (wave * 64 + mbl * 16 + 4 * g) * 2);
        const f32x4 m1 = *(const f32x4*)(st2 + (wave * 64 + mbl * 16 + 4 * g) * 2 + 4);
        const float mean[4] = {m0[0], m0[2], m1[0], m1[2]}, inv[4] = {m0[1], m0[3], m1[1], m1[3]};
        float kk[4], vj[4];
#pragma unroll
        for (int j = 0; j < 4; ++j) {
          kk[j] = phi_f((acck[i][j] - mean[j] * csk) * inv[j]);
          vj[j] = (accv[i][j] - mean[j] * csv) * inv[j];
        }
        pk[2 * i2] = pack_bf(kk[0], kk[1]);
        pk[2 * i2 + 1] = pack_bf(kk[2], kk[3]);
        vv[2 * i2] = pack_bf(vj[0], vj[1]);
        vv[2 * i2 + 1] = pack_bf(vj[2], vj[3]);
      }
    }
    kv = mfma16(pk, vv, kv);
    zz = mfma16(pk, ones, zz);
  }
  {
    float* rd = red + (wc * 2 + wr) * 272;
#pragma unroll
    for (int j = 0; j < 4; ++j) rd[(4 * g + j) * 16 + r] = kv[j];
    if (r == 0) *(f32x4*)(rd + 256 + 4 * g) = zz;
  }
  __syncthreads();
  for (int i = tid; i < 544; i += 256) {
    const int hh = i >= 272 ? 1 : 0, e = i - hh * 272;
    const float* rr = red + hh * 2 * 272 + e;
    part[(size_t)((cg * 2 + hh) * 16 + rt) * 272 + e] = rr[0] + rr[272];
  }
  asm volatile("" :: "v"(tv0), "v"(tv1), "v"(tv2), "v"(tv3), "v"(tv4));
  STAMP(0,13);
}

typedef __attribute__((address_space(1))) unsigned gu32;
DI void poll_ge(unsigned* c, unsigned target) {
  for (unsigned spins = 0; spins < (1u << 17); ++spins) {
    const unsigned v = __builtin_amdgcn_readfirstlane(
        __hip_atomic_load((gu32*)c, __ATOMIC_RELAXED, __HIP_MEMORY_SCOPE_AGENT));
    if (v >= target) break;
    __builtin_amdgcn_s_sleep(1);
  }
  asm volatile("" ::: "memory");
}
DI void glds16_sc1(const void* sbase, unsigned voff, unsigned dst) {
  asm volatile("s_mov_b32 m0, %2\n\ts_nop 0\n\tglobal_load_lds_dwordx4 %0, %1 sc1"
               :: "v"(voff), "s"(sbase), "s"(dst) : "memory");
}
DI void glds16v(const char* gsrc, unsigned dst) {
  asm volatile("s_mov_b32 m0, %1\n\ts_nop 0\n\tglobal_load_lds_dwordx4 %0, off" :: "v"(gsrc), "s"(dst) : "memory");
}
constexpr int K3_SLOT = 16384 + 24576, K3_NSLOT = 4;

__global__ __launch_bounds__(512) void kb_rest(const u16* __restrict__ x, const u16* __restrict__ pq,
                                               const float* __restrict__ part, const u16* __restrict__ wo,
                                               u16* wp, unsigned* cnt, u16* hf, float* hst,
                                               const float* __restrict__ csp,
                                               const u16* __restrict__ wg, const u16* __restrict__ wu,
                                               const u16* __restrict__ wd, u16* hhf, u16* __restrict__ out) {
  __shared__ __attribute__((aligned(16))) char smem[K3_SLOT * K3_NSLOT];
  const int tid = threadIdx.x, lane = tid & 63;
  const int wave = __builtin_amdgcn_readfirstlane(tid >> 6);
  const int xcd = blockIdx.x & 7, idx = blockIdx.x >> 3;
  const int r = lane & 15, g = lane >> 4;
  const int q4 = (lane & 15) >> 2, p = lane & 3;
  const __amdgpu_buffer_rsrc_t hrs = __builtin_amdgcn_make_buffer_rsrc(hf, 0, S_ * D_ * 2, 0x00020000);
  const __amdgpu_buffer_rsrc_t srs = __builtin_amdgcn_make_buffer_rsrc(hst, 0, S_ * 32 * 4 + 16 * 2752 * 4, 0x00020000);
  unsigned* cnt_w = cnt;
  unsigned* cnt_h = cnt + 8 * 64;
  unsigned* cnt_hh = cnt + 24 * 64;
  STAMP(2,0);
  {
    const int rt = xcd * 4 + (idx >> 3);
    const int ct = idx & 7;
    const int kg = wave >> 2, rb = wave & 3;
    const int mblk = rt * 4 + rb;
    const __amdgpu_buffer_rsrc_t wrs = __builtin_amdgcn_make_buffer_rsrc(wp, 0, 524288 + 8192, 0x00020000);
    const int ph = blockIdx.x >> 3, pc = blockIdx.x & 7;
    float* kv = (float*)(smem + 65536 + 16384);
    float pv[16];
    if (tid < 272) {
#pragma unroll
      for (int pp = 0; pp < 16; ++pp) pv[pp] = part[(size_t)(ph * 16 + pp) * 272 + tid];
    }
    float w[16];
    if (tid < 256) {
#pragma unroll
      for (int e = 0; e < 16; ++e) w[e] = bf_lo(wo[(size_t)(ph * 16 + e) * D_ + pc * 64 + (tid & 63)]);
    }
    if (tid < 272) {
      float s = 0.f;
#pragma unroll
      for (int pp = 0; pp < 16; ++pp) s += pv[pp];
      kv[tid] = s;
    }
    __syncthreads();
    STAMP(2,1);
    if (tid < 256) {
      const int c = tid & 63, dq = tid >> 6;
      float o[4];
#pragma unroll
      for (int j = 0; j < 4; ++j) {
        float s = 0.f;
#pragma unroll
        for (int e4 = 0; e4 < 4; ++e4) {
          const f32x4 k4 = *(const f32x4*)(kv + (dq * 4 + j) * 16 + e4 * 4);
          s = fmaf(k4[0], w[e4 * 4], s); s = fmaf(k4[1], w[e4 * 4 + 1], s);
          s = fmaf(k4[2], w[e4 * 4 + 2], s); s = fmaf(k4[3], w[e4 * 4 + 3], s);
        }
        o[j] = s;
      }
      const int n = pc * 64 + c, nb = n >> 4, cc = n & 15;
      const int ks = ph >> 1, gg = (ph & 1) * 2 + (dq >> 1);
      u32x2 v = {pack_bf(o[0], o[1]), pack_bf(o[2], o[3])};
      __builtin_amdgcn_raw_buffer_store_b64(v, wrs, (unsigned)((nb * 16 + ks) * 64 + gg * 16 + cc) * 16u + (dq & 1) * 8u, 0, 16);
    } else if (tid < 258) {
      const float* zs = kv + 256 + (tid - 256) * 8;
      u32x4 v = {pack_bf(zs[0], zs[1]), pack_bf(zs[2], zs[3]), pack_bf(zs[4], zs[5]), pack_bf(zs[6], zs[7])};
      __builtin_amdgcn_raw_buffer_store_b128(v, wrs, 524288u + (unsigned)(pc * 512 + ph * 16 + (tid - 256) * 8) * 2u, 0, 16);
    }
    WAIT_VM(0);
    __syncthreads();
    if (tid == 0) __hip_atomic_fetch_add((gu32*)(cnt_w + pc * 64), 1u, __ATOMIC_RELAXED, __HIP_MEMORY_SCOPE_AGENT);
    STAMP(2,2);
    float cpart[8];
    const int ccol = ((rt & 1) * 8 + ct) * 172 + (tid - 272);
    const bool do_cs = tid >= 272 && tid < 444;
    if (do_cs) {
#pragma unroll
      for (int kp = 0; kp < 8; ++kp) cpart[kp] = csp[kp * 2752 + ccol];
    }
    u32x4 a[8];
#pragma unroll
    for (int i = 0; i < 8; ++i) a[i] = *(const u32x4*)(pq + ((size_t)(mblk * 16 + kg * 8 + i) * 64 + lane) * 8);
    u32x2 xr[2];
#pragma unroll
    for (int j = 0; j < 2; ++j)
      xr[j] = *(const u32x2*)(x + (size_t)(mblk * 16 + r) * D_ + ct * 64 + (kg * 2 + j) * 16 + g * 4);
    if (do_cs) {
      const float c = ((cpart[0] + cpart[1]) + (cpart[2] + cpart[3])) + ((cpart[4] + cpart[5]) + (cpart[6] + cpart[7]));
      __builtin_amdgcn_raw_buffer_store_b32(__float_as_uint(c), srs, (unsigned)(S_ * 32 * 4 + ((rt >> 1) * 2752 + ccol) * 4), 0, 16);
    }
    if (wave == 0) poll_ge(cnt_w + ct * 64, 32u);
    __syncthreads();
    STAMP(2,3);
    u32x4 zf[8], wv[8];
#pragma unroll
    for (int i = 0; i < 8; ++i)
      zf[i] = __builtin_amdgcn_raw_buffer_load_b128(wrs, 524288u + (unsigned)(ct * 512 + (kg * 8 + i) * 32 + g * 8) * 2u, 0, 16);
#pragma unroll
    for (int i = 0; i < 8; ++i)
      wv[i] = __builtin_amdgcn_raw_buffer_load_b128(wrs, (unsigned)(ct * 65536 + (wave * 8 + i) * 1024 + lane * 16), 0, 16);
#pragma unroll
    for (int i = 0; i < 8; ++i) {
      float s = dot2(a[i][0], zf[i][0], 0.f);
      s = dot2(a[i][1], zf[i][1], s);
      s = dot2(a[i][2], zf[i][2], s);
      s = dot2(a[i][3], zf[i][3], s);
      const float inv = __builtin_amdgcn_rcpf(red16(s) + 1e-6f);
#pragma unroll
      for (int j = 0; j < 4; ++j) a[i][j] = pack_bf(bf_lo(a[i][j]) * inv, bf_hi(a[i][j]) * inv);
    }
#pragma unroll
    for (int i = 0; i < 8; ++i) *(u32x4*)(smem + (wave * 8 + i) * 1024 + lane * 16) = wv[i];
    __syncthreads();
    STAMP(2,4);
    f32x4 acc[4] = {};
#pragma unroll
    for (int i = 0; i < 8; ++i) {
#pragma unroll
      for (int nb = 0; nb < 4; ++nb) {
        const u32x4 b = *(const u32x4*)(smem + (nb * 16 + kg * 8 + i) * 1024 + lane * 16);
        acc[nb] = mfma16(b, a[i], acc[nb]);
      }
    }
    f32x4* xch = (f32x4*)(smem + 65536);
#pragma unroll
    for (int j = 0; j < 2; ++j) xch[(wave * 2 + j) * 64 + lane] = kg ? acc[j] : acc[2 + j];
    __syncthreads();
    float hs = 0.f, hq = 0.f;
    u32x2 oo[2];
#pragma unroll
    for (int j = 0; j < 2; ++j) {
      const f32x4 o4 = (kg ? acc[2 + j] : acc[j]) + xch[((wave ^ 4) * 2 + j) * 64 + lane];
      u32x2 o;
      o[0] = pack_bf(bf_lo(xr[j][0]) + o4[0], bf_hi(xr[j][0]) + o4[1]);
      o[1] = pack_bf(bf_lo(xr[j][1]) + o4[2], bf_hi(xr[j][1]) + o4[3]);
      oo[j] = o;
      hs = dot2(o[0], 0x3f803f80u, hs); hs = dot2(o[1], 0x3f803f80u, hs);
      hq = dot2(o[0], o[0], hq); hq = dot2(o[1], o[1], hq);
    }
    __builtin_amdgcn_raw_buffer_store_b128(
        merge16(oo[0], oo[1]), hrs, (unsigned)(((mblk * 16 + ct * 2 + kg) * 64 + r + 16 * (2 * (g & 1) + (g >> 1))) * 16), 0, 16);
    hs = red32(red16(hs)); hq = red32(red16(hq));
    if (g == 0) {
      u32x2 v = {__float_as_uint(hs), __float_as_uint(hq)};
      __builtin_amdgcn_raw_buffer_store_b64(v, srs, (unsigned)(((ct * 2 + kg) * S_ + mblk * 16 + r) * 8), 0, 16);
    }
    WAIT_VM(0);
    __syncthreads();
    if (tid == 0) __hip_atomic_fetch_add((gu32*)(cnt_h + (rt >> 1) * 64), 1u, __ATOMIC_RELAXED, __HIP_MEMORY_SCOPE_AGENT);
    STAMP(2,5);
  }
  const int rt3 = (xcd >> 1) * 4 + (idx >> 3);
  const int cg = (xcd & 1) * 8 + (idx & 7);
  const int f0 = cg < 11 ? cg * 96 : 1056 + (cg - 11) * 64;
  const int nvalid = cg < 11 ? 6 : 4;
  const int rt4 = xcd * 4 + (idx >> 3);
  const int ct4 = idx & 7;
  if (wave >= 4) {
    const int lw = wave - 4;
    const unsigned lbase = lds_addr(smem);
    {
      const char* abase = (const char*)(hf + (size_t)((rt3 * 8 + lw * 2) * 16) * 512);
      const long wdelta = (const char*)wu - (const char*)wg;
      const char* bsrc[6];
#pragma unroll
      for (int i = 0; i < 6; ++i) {
        const int line = (lw * 6 + i) * 4 + g;
        const int kq = line / 6, cgrp = line % 6;
        const int k = kq * 4 + ((lane >> 2) & 3);
        const int ch = (lane & 3) ^ (((k >> 3) & 1) << 1);
        int c3 = cgrp < 3 ? cgrp : cgrp - 3;
        c3 = (c3 * 2 < nvalid) ? c3 : 0;
        bsrc[i] = (const char*)(wg + f0) + (unsigned)(k * F_ + c3 * 32 + ch * 8) * 2u + (cgrp >= 3 ? wdelta : 0);
      }
      auto stA = [&](int s) {
        const unsigned slot = lbase + (s % K3_NSLOT) * K3_SLOT;
#pragma unroll
        for (int i = 0; i < 4; ++i)
          glds16_sc1(abase + (i >> 1) * 16384 + (s * 2 + (i & 1)) * 1024, lane * 16, slot + (lw * 4 + i) * 1024);
      };
      auto stB = [&](int s) {
        const unsigned slot = lbase + (s % K3_NSLOT) * K3_SLOT;
#pragma unroll
        for (int i = 0; i < 6; ++i) glds16v(bsrc[i] + (size_t)s * 64 * F_ * 2, slot + 16384 + (lw * 6 + i) * 1024);
      };
      stB(0); stB(1);
      poll_ge(cnt_h + rt3 * 64, 16u);
      stA(0); stA(1); stA(2); stB(2);
      wait_vm<14>(); BARE_BAR(); stA(3); stB(3);
      wait_vm<20>(); BARE_BAR(); stA(4); stB(4);
      wait_vm<20>(); BARE_BAR(); stA(5); stB(5);
      wait_vm<20>(); BARE_BAR(); stA(6); stB(6);
      wait_vm<20>(); BARE_BAR(); stA(7); stB(7);
      wait_vm<20>(); BARE_BAR();
      wait_vm<10>(); BARE_BAR();
      wait_vm<0>();  BARE_BAR();
    }
    {
      const char* abase = (const char*)(hhf + (size_t)((rt4 * 4 + lw) * KS4) * 512);
      const char* bbase = (const char*)(wd + (size_t)lw * 32 * D_ + ct4 * 64);
      const unsigned boffs = (unsigned)(((g >> 1) * 4 + ((lane >> 2) & 3)) * D_ + (g & 1) * 32 + (lane & 3) * 8) * 2u;
      auto stA = [&](int s) {
        const unsigned slot = lbase + (s & 3) * 32768;
#pragma unroll
        for (int i = 0; i < 4; ++i)
          glds16_sc1(abase + (s * 4 + ((s == 10 && i == 3) ? 2 : i)) * 1024, lane * 16, slot + lw * 4096 + i * 1024);
      };
      auto stB = [&](int s) {
        const unsigned slot = lbase + (s & 3) * 32768;
        const int so = (s == 10 && lw == 3) ? s - 1 : s;
#pragma unroll
        for (int i = 0; i < 4; ++i)
          glds16(bbase + (size_t)(so * 128 + i * 8) * D_ * 2, (i & 1) ? (boffs ^ 32u) : boffs, slot + 16384 + lw * 4096 + i * 1024);
      };
      stB(0); stB(1); stB(2);
      BARE_BAR();
      poll_ge(cnt_hh + (rt4 >> 1) * 64, 16u);
      stA(0); stA(1); stA(2);
      wait_vm<8>();  BARE_BAR(); stA(3); stB(3);
      wait_vm<12>(); BARE_BAR(); stA(4); stB(4);
      wait_vm<16>(); BARE_BAR(); stA(5); stB(5);
#pragma unroll
      for (int s = 2; s < 10; ++s) {
        if (s <= 7) wait_vm<16>(); else if (s == 8) wait_vm<8>(); else wait_vm<0>();
        BARE_BAR();
        if (s + 4 < 11) { stA(s + 4); stB(s + 4); }
      }
    }
    return;
  }
  const int wr = wave >> 1, wc = wave & 1;
  {
    const int aoff = wr * 8192 + lane * 16;
    const int bbase2 = 16384 + (2 * g) * 1536 + q4 * 64 + p * 8;
    int boff[3];
#pragma unroll
    for (int j = 0; j < 3; ++j) {
      const int fbl = 3 * wc + j;
      boff[j] = bbase2 + (fbl >> 1) * 256 + (((fbl & 1) ^ (g & 1)) << 5);
    }
    u32x2 hp[16];
    f32x4 cg4[3], cu4[3];
    float mean_l = 0.f, inv2_l = 0.f;
    f32x4 accg[4][3] = {}, accu[4][3] = {};
    u32x4 fa[2][4], fb[2][6];
    auto rdfrags = [&](int t) {
      const char* slot = smem + ((t >> 1) % K3_NSLOT) * K3_SLOT;
      const int kk = t & 1;
#pragma unroll
      for (int i = 0; i < 4; ++i) fa[kk][i] = *(const u32x4*)(slot + aoff + i * 2048 + kk * 1024);
#pragma unroll
      for (int j = 0; j < 3; ++j) {
        fb[kk][j] = tr_frag(slot + boff[j] + kk * 12288, slot + boff[j] + kk * 12288 + 1536);
        fb[kk][3 + j] = tr_frag(slot + boff[j] + 768 + kk * 12288, slot + boff[j] + 768 + kk * 12288 + 1536);
      }
    };
    auto compute = [&](int t) {
      const int kk = t & 1;
      const u32x4* a = fa[kk];
#pragma unroll
      for (int j = 0; j < 3; ++j) {
        const u32x4 bg = fb[kk][j], bu = fb[kk][3 + j];
#pragma unroll
        for (int i = 0; i < 4; ++i) {
          accg[i][j] = mfma16(bg, a[i], accg[i][j]);
          accu[i][j] = mfma16(bu, a[i], accu[i][j]);
        }
      }
    };
    BARE_BAR();
    STAMP(3,0);
    rdfrags(0);
#pragma unroll
    for (int s = 0; s < 8; ++s) {
      rdfrags(2 * s + 1);
      compute(2 * s);
      __builtin_amdgcn_sched_barrier(0);
      if (s + 1 < 8) {
        RAW_BAR();
        STAMP(3,1+s);
        rdfrags(2 * s + 2);
      }
      if (s == 4) {
#pragma unroll
        for (int i = 0; i < 16; ++i)
          hp[i] = __builtin_amdgcn_raw_buffer_load_b64(srs, (unsigned)((i * S_ + rt3 * 128 + wr * 64 + lane) * 8), 0, 16);
      }
      if (s == 6) {
        f32x2 t2 = {};
#pragma unroll
        for (int i = 0; i < 16; ++i) t2 += __builtin_bit_cast(f32x2, hp[i]);
        mean_l = t2[0] * (1.f / 512.f);
        const float var = fmaxf(t2[1] * (1.f / 512.f) - mean_l * mean_l, 0.f);
        inv2_l = __builtin_amdgcn_rcpf(var + 1e-5f);
        asm volatile("" : "+v"(mean_l), "+v"(inv2_l));
#pragma unroll
        for (int j = 0; j < 3; ++j) {
          const int fbl = (3 * wc + j) < nvalid ? (3 * wc + j) : 0;
          const unsigned co = (unsigned)(S_ * 32 * 4 + (rt3 * 2752 + f0 + fbl * 16 + 4 * g) * 4);
          cg4[j] = __builtin_bit_cast(f32x4, __builtin_amdgcn_raw_buffer_load_b128(srs, co, 0, 16));
          cu4[j] = __builtin_bit_cast(f32x4, __builtin_amdgcn_raw_buffer_load_b128(srs, co + F_ * 4, 0, 16));
        }
      }
      compute(2 * s + 1);
      __builtin_amdgcn_sched_barrier(0);
    }
    STAMP(3,9);
    const __amdgpu_buffer_rsrc_t hhrs = __builtin_amdgcn_make_buffer_rsrc(hhf, 0, 128 * KS4 * 1024, 0x00020000);
#pragma unroll
    for (int ip = 0; ip < 2; ++ip) {
      float mean[2], inv2[2];
#pragma unroll
      for (int i2 = 0; i2 < 2; ++i2) {
        mean[i2] = __shfl(mean_l, (ip * 2 + i2) * 16 + r);
        inv2[i2] = __shfl(inv2_l, (ip * 2 + i2) * 16 + r);
      }
      const int mblk = rt3 * 8 + wr * 4 + ip * 2 + (g & 1);
#pragma unroll
      for (int j = 0; j < 3; ++j) {
        const int fbl = 3 * wc + j;
        u32x2 o[2];
#pragma unroll
        for (int i2 = 0; i2 < 2; ++i2) {
          const int i = ip * 2 + i2;
          float hv[4];
#pragma unroll
          for (int e = 0; e < 4; ++e)
            hv[e] = ((accg[i][j][e] - mean[i2] * cg4[j][e]) * inv2[i2]) * (accu[i][j][e] - mean[i2] * cu4[j][e]);
          o[i2][0] = pack_bf(hv[0], hv[1]); o[i2][1] = pack_bf(hv[2], hv[3]);
        }
        const u32x4 v = merge16(o[0], o[1]);
        if (fbl < nvalid) {
          const int fb_ = (f0 >> 4) + fbl;
          __builtin_amdgcn_raw_buffer_store_b128(
              v, hhrs, (unsigned)(((mblk * KS4 + (fb_ >> 1)) * 64 + r + 16 * (2 * (fb_ & 1) + (g >> 1))) * 16), 0, 16);
        }
      }
    }
    WAIT_VM(0);
    RAW_BAR();
    if (tid == 0) __hip_atomic_fetch_add((gu32*)(cnt_hh + rt3 * 64), 1u, __ATOMIC_RELAXED, __HIP_MEMORY_SCOPE_AGENT);
    STAMP(3,10);
  }
  {
    const int boff = 16384 + (2 * g) * 512 + wc * 256 + q4 * 64 + p * 8;
    const int aoff = wr * 8192 + lane * 16;
    u32x2 hr[2][2];
    f32x4 acc[2][2] = {};
    u32x4 fa[2][2][2], fb[2][2][2];
    auto rdfrags = [&](int t) {
      const char* slot = smem + ((t >> 1) & 3) * 32768;
#pragma unroll
      for (int kk = 0; kk < 2; ++kk) {
        const int ksl = (t & 1) * 2 + kk;
#pragma unroll
        for (int mb = 0; mb < 2; ++mb) fa[t & 1][kk][mb] = *(const u32x4*)(slot + aoff + mb * 4096 + ksl * 1024);
#pragma unroll
        for (int nb = 0; nb < 2; ++nb) {
          const int ho = (nb ^ (g & 1)) << 5;
          fb[t & 1][kk][nb] = tr_frag(slot + boff + ksl * 4096 + ho, slot + boff + ksl * 4096 + 512 + ho);
        }
      }
    };
    auto compute = [&](int t) {
#pragma unroll
      for (int kk = 0; kk < 2; ++kk) {
        if (2 * t + kk < KS4) {
#pragma unroll
          for (int mb = 0; mb < 2; ++mb)
#pragma unroll
            for (int nb = 0; nb < 2; ++nb) acc[mb][nb] = mfma16(fb[t & 1][kk][nb], fa[t & 1][kk][mb], acc[mb][nb]);
        }
      }
    };
    BARE_BAR();
    STAMP(4,0);
    rdfrags(0);
#pragma unroll
    for (int mb = 0; mb < 2; ++mb)
#pragma unroll
      for (int nb = 0; nb < 2; ++nb)
        hr[mb][nb] = __builtin_amdgcn_raw_buffer_load_b64(
            hrs, (unsigned)((((rt4 * 4 + wr * 2 + mb) * 16 + ct4 * 2 + wc) * 64 + r + 16 * (2 * nb + (g >> 1))) * 16 + (g & 1) * 8), 0, 16);
#pragma unroll
    for (int s = 0; s < 11; ++s) {
      rdfrags(2 * s + 1);
      compute(2 * s);
      if (s + 1 < 11) {
        RAW_BAR();
        STAMP(4,1+s);
        rdfrags(2 * s + 2);
      }
      compute(2 * s + 1);
    }
#pragma unroll
    for (int mb = 0; mb < 2; ++mb)
#pragma unroll
      for (int nb = 0; nb < 2; ++nb) {
        const f32x4 o4 = acc[mb][nb];
        u32x2 o;
        o[0] = pack_bf(bf_lo(hr[mb][nb][0]) + o4[0], bf_hi(hr[mb][nb][0]) + o4[1]);
        o[1] = pack_bf(bf_lo(hr[mb][nb][1]) + o4[2], bf_hi(hr[mb][nb][1]) + o4[3]);
        *(u32x2*)(out + (size_t)(rt4 * 64 + wr * 32 + mb * 16 + r) * D_ + ct4 * 64 + wc * 32 + nb * 16 + g * 4) = o;
      }
    STAMP(4,14);
  }
}

extern "C" void kernel_launch(void* const* d_in, const int* in_sizes, int n_in, void* d_out, int out_size,
                              void* d_ws, size_t ws_size, hipStream_t stream) {
  const u16* x = (const u16*)d_in[0];
  const u16* wqkv = (const u16*)d_in[1];
  const u16* wo = (const u16*)d_in[2];
  const u16* wg = (const u16*)d_in[4];
  const u16* wu = (const u16*)d_in[5];
  const u16* wd = (const u16*)d_in[6];
  u16* out = (u16*)d_out;
  char* ws = (char*)d_ws;
  u16* pq = (u16*)(ws);
  u16* hf = (u16*)(ws + (2u << 20));
  u16* wp = (u16*)(ws + (4u << 20));
  float* part = (float*)(ws + (5u << 20));
  unsigned* cnt = (unsigned*)(ws + (6u << 20));
  float* csp = (float*)(ws + (6u << 20) + 65536);
  float* hst = (float*)(ws + (7u << 20));
  u16* hhf = (u16*)(ws + (8u << 20));
  k1_qkv<<<256, 512, 0, stream>>>(x, wqkv, pq, part, wg, wu, csp, cnt, wo, wd);
  kb_rest<<<256, 512, 0, stream>>>(x, pq, part, wo, wp, cnt, hf, hst, csp, wg, wu, wd, hhf, out);
}
```
